```python
import torch

def _softmax(x, axis=-1):
    return torch.softmax(x, dim=axis)
def _gelu(x):
    return torch.nn.functional.gelu(x, approximate="tanh")
def _silu(x):
    return torch.nn.functional.silu(x)
def _sigmoid(x):
    return torch.sigmoid(x)
def _layernorm(x, axis=-1, eps=1e-5):
    m = x.mean(dim=axis, keepdim=True)
    v = x.var(dim=axis, keepdim=True, unbiased=False)
    return (x - m) / torch.sqrt(v + eps)
def _scatter_add(tgt, idx, src, axis=0):
    return tgt.clone().index_add_(axis, idx, src)
def _segment_sum(src, seg, n):
    out = torch.zeros((n,) + src.shape[1:], dtype=src.dtype)
    return out.index_add_(0, seg, src)
def _pad(x, pw):
    flat = tuple(v for pair in reversed(pw) for v in pair)
    return torch.nn.functional.pad(x, flat)

kernel_name = 'recurrent_scan__sliding_block__dense_activation_squared_relu__prompt'

def setup_inputs():

    _w = (lambda a: (lambda t: t.to(torch.bfloat16) if t.is_floating_point() else t)(torch.as_tensor(a)))
    def _A_setup():
        B, S, D = 1, 2048, 640
        return {
            "x": _w((lambda *s: torch.randn(*s, dtype=torch.bfloat16) * 0.05)(B, S, D)),
            "wqkv": _w((lambda *s: torch.randn(*s, dtype=torch.bfloat16) * 0.05)(D, 3 * D)),
            "wo": _w((lambda *s: torch.randn(*s, dtype=torch.bfloat16) * 0.05)(D, D)),
        }
    _A_in = _A_setup()

    def _B_setup():
        B, S, D, F = 1, 2048, 640, 1536
        return {
            "x": _w((lambda *s: torch.randn(*s, dtype=torch.bfloat16) * 0.05)(B, S, D)),
            "w1": _w((lambda *s: torch.randn(*s, dtype=torch.bfloat16) * 0.05)(D, F)),
            "b1": _w((lambda *s: torch.randn(*s, dtype=torch.bfloat16) * 0.4)(F)),
            "w2": _w((lambda *s: torch.randn(*s, dtype=torch.bfloat16) * 0.05)(F, D)),
            "b2": _w((lambda *s: torch.randn(*s, dtype=torch.bfloat16) * 0.8)(D)),
        }
    _B_in = _B_setup()

    return {
        "A_x": _A_in["x"],
        "A_wqkv": _A_in["wqkv"],
        "A_wo": _A_in["wo"],
        "B_x": _B_in["x"],
        "B_w1": _B_in["w1"],
        "B_b1": _B_in["b1"],
        "B_w2": _B_in["w2"],
        "B_b2": _B_in["b2"],
    }

def reference(A_x, A_wqkv, A_wo, B_x, B_w1, B_b1, B_w2, B_b2):
    def _A_ref(x, wqkv, wo):
        B, S, D = x.shape
        H = 32; Dh = D // H; W = 256
        qkv = (lambda x, *d: x.permute(*d))((x @ wqkv).reshape(B, S, 3, H, Dh), 2, 0, 3, 1, 4)
        q, k, v = qkv[0], qkv[1], qkv[2]
        scores = torch.einsum("bhqd,bhkd->bhqk", q, k) * (Dh ** -0.5)
        idx = torch.arange(S)
        mask = (idx[:, None] - idx[None, :] >= 0) & (idx[:, None] - idx[None, :] < W)
        scores = torch.where(mask[None, None], scores, -1e9)
        p = _softmax(scores, axis=-1)
        o = torch.einsum("bhqk,bhkd->bhqd", p, v)
        return (lambda x, *d: x.permute(*d))(o, 0, 2, 1, 3).reshape(B, S, D) @ wo

    def _B_ref(x, w1, b1, w2, b2):
        h = torch.einsum("bsd,df->bsf", x, w1) + b1
        zero = torch.zeros_like(h)
        r = torch.where(h > zero, h, zero)
        h = r * r
        out = torch.einsum("bsf,fd->bsd", h, w2) + b2
        return out

    x = A_x
    xn = _layernorm(x, axis=-1)
    s = _A_ref(xn, A_wqkv, A_wo)
    g = _silu(_B_ref(xn, B_w1, B_b1, B_w2, B_b2))
    return x + s * g
```

```cpp
#include <hip/hip_runtime.h>
#include <hip/hip_bf16.h>
typedef __hip_bfloat16 bf16;
static __device__ __forceinline__ float b2f(bf16 v) { return __bfloat162float(v); }
static __device__ __forceinline__ bf16 f2b(float v) { return __float2bfloat16(v); }

__global__ void k_ln(const bf16* x, bf16* xn) {
  int row = blockIdx.x; int t = threadIdx.x;
  __shared__ float red[64];
  float s = 0;
  for (int i = t; i < 640; i += 64) s += b2f(x[row * 640 + i]);
  for (int m = 32; m >= 1; m >>= 1) s += __shfl_xor(s, m);
  float mean = s / 640.f;
  float v = 0;
  for (int i = t; i < 640; i += 64) { float d = b2f(x[row * 640 + i]) - mean; v += d * d; }
  for (int m = 32; m >= 1; m >>= 1) v += __shfl_xor(v, m);
  float r = rsqrtf(v / 640.f + 1e-5f);
  for (int i = t; i < 640; i += 64) xn[row * 640 + i] = f2b((b2f(x[row * 640 + i]) - mean) * r);
}
__global__ void k_gemm(const bf16* A, const bf16* B, const bf16* bias, void* C, int M, int N, int K, int mode) {
  int n = blockIdx.x * 64 + threadIdx.x; int m = blockIdx.y;
  if (n >= N) return;
  float acc = 0;
  for (int k = 0; k < K; ++k) acc += b2f(A[m * K + k]) * b2f(B[k * N + n]);
  if (mode == 0) ((bf16*)C)[m * N + n] = f2b(acc);
  else if (mode == 1) { float h = acc + b2f(bias[n]); h = h > 0 ? h : 0; ((bf16*)C)[m * N + n] = f2b(h * h); }
  else ((float*)C)[m * N + n] = acc;
}
__global__ void k_attn(const bf16* qkv, bf16* o) {
  int q = blockIdx.x, h = blockIdx.y, t = threadIdx.x;
  __shared__ float p[256]; __shared__ float red[4];
  int key = q - 255 + t;
  float s = -INFINITY;
  if (key >= 0) { s = 0; for (int d = 0; d < 20; ++d) s += b2f(qkv[q * 1920 + h * 20 + d]) * b2f(qkv[key * 1920 + 640 + h * 20 + d]); s *= 0.22360679775f; }
  float m = s; for (int k = 32; k >= 1; k >>= 1) m = fmaxf(m, __shfl_xor(m, k));
  if ((t & 63) == 0) red[t >> 6] = m; __syncthreads();
  m = fmaxf(fmaxf(red[0], red[1]), fmaxf(red[2], red[3])); __syncthreads();
  float e = key >= 0 ? __expf(s - m) : 0.f;
  float l = e; for (int k = 32; k >= 1; k >>= 1) l += __shfl_xor(l, k);
  if ((t & 63) == 0) red[t >> 6] = l; p[t] = e; __syncthreads();
  l = red[0] + red[1] + red[2] + red[3];
  if (t < 20) { float acc = 0; for (int j = 0; j < 256; ++j) { int kk = q - 255 + j; if (kk >= 0) acc += p[j] * b2f(qkv[kk * 1920 + 1280 + h * 20 + t]); } o[q * 640 + h * 20 + t] = f2b(acc / l); }
}
__global__ void k_fin(const bf16* x, const float* s, const float* g, const bf16* b2, bf16* out) {
  int i = blockIdx.x * 256 + threadIdx.x;
  float gg = g[i] + b2f(b2[i % 640]);
  gg = gg / (1.f + __expf(-gg));
  out[i] = f2b(b2f(x[i]) + s[i] * gg);
}
extern "C" void kernel_launch(void* const* d_in, const int* in_sizes, int n_in, void* d_out, int out_size, void* d_ws, size_t ws_size, hipStream_t stream) {
  const bf16* x = (const bf16*)d_in[0]; const bf16* wqkv = (const bf16*)d_in[1]; const bf16* wo = (const bf16*)d_in[2];
  const bf16* w1 = (const bf16*)d_in[4]; const bf16* b1 = (const bf16*)d_in[5]; const bf16* w2 = (const bf16*)d_in[6]; const bf16* b2 = (const bf16*)d_in[7];
  char* ws = (char*)d_ws;
  bf16* xn = (bf16*)ws; bf16* qkv = (bf16*)(ws + (4 << 20)); bf16* h = (bf16*)(ws + (16 << 20)); bf16* o = (bf16*)(ws + (24 << 20));
  float* s = (float*)(ws + (32 << 20)); float* g = (float*)(ws + (40 << 20));
  k_ln<<<2048, 64, 0, stream>>>(x, xn);
  k_gemm<<<dim3(30, 2048), 64, 0, stream>>>(xn, wqkv, nullptr, qkv, 2048, 1920, 640, 0);
  k_gemm<<<dim3(24, 2048), 64, 0, stream>>>(xn, w1, b1, h, 2048, 1536, 640, 1);
  k_attn<<<dim3(2048, 32), 256, 0, stream>>>(qkv, o);
  k_gemm<<<dim3(10, 2048), 64, 0, stream>>>(o, wo, nullptr, s, 2048, 640, 640, 2);
  k_gemm<<<dim3(10, 2048), 64, 0, stream>>>(h, w2, nullptr, g, 2048, 640, 1536, 2);
  k_fin<<<2048 * 640 / 256, 256, 0, stream>>>(x, s, g, b2, (bf16*)d_out);
}
```

```cpp
#include <hip/hip_runtime.h>
#include <stdint.h>

#define LAS __attribute__((address_space(3)))
typedef short bf16x8 __attribute__((ext_vector_type(8)));
typedef short s16x4 __attribute__((ext_vector_type(4)));
typedef float f32x4 __attribute__((ext_vector_type(4)));
typedef unsigned u32x4 __attribute__((ext_vector_type(4)));
typedef unsigned u32x2 __attribute__((ext_vector_type(2)));
typedef __bf16 bf16x2_t __attribute__((ext_vector_type(2)));
typedef float f32x2 __attribute__((ext_vector_type(2)));

constexpr int S_ = 2048, D_ = 640, F_ = 1536;
constexpr size_t PLANE = 2048 * 16;
constexpr size_t OFF_QKV = 0;
constexpr size_t OFF_H = OFF_QKV + 240 * PLANE;
constexpr size_t OFF_O = OFF_H + 192 * PLANE;
constexpr size_t OFF_W2F = OFF_O + 80 * PLANE;
constexpr size_t OFF_WOF = OFF_W2F + (size_t)8 * 48 * 5120;
constexpr size_t OFF_G = OFF_WOF + (size_t)8 * 20 * 5120;

__device__ __forceinline__ f32x4 mfma16(bf16x8 a, bf16x8 b, f32x4 c) {
  return __builtin_amdgcn_mfma_f32_16x16x32_bf16(a, b, c, 0, 0, 0);
}
__device__ __forceinline__ unsigned pk2(float a, float b) {
  f32x2 v = {a, b};
  bf16x2_t r = __builtin_convertvector(v, bf16x2_t);
  return __builtin_bit_cast(unsigned, r);
}
__device__ __forceinline__ float dot2(unsigned a, unsigned b, float c) {
  return __builtin_amdgcn_fdot2_f32_bf16(__builtin_bit_cast(bf16x2_t, a), __builtin_bit_cast(bf16x2_t, b), c, false);
}
__device__ __forceinline__ float bflo(unsigned u) { return __builtin_bit_cast(float, u << 16); }
__device__ __forceinline__ float bfhi(unsigned u) { return __builtin_bit_cast(float, u & 0xffff0000u); }
__device__ __forceinline__ void dma1(const char* sbase, unsigned voff, unsigned lds) {
  unsigned keep;
  asm volatile("s_mov_b32 %0, m0\n\ts_mov_b32 m0, %3\n\ts_nop 0\n\tglobal_load_lds_dwordx4 %1, %2\n\ts_mov_b32 m0, %0"
               : "=&s"(keep) : "v"(voff), "s"(sbase), "s"(lds) : "memory");
}
__device__ __forceinline__ void dma_stage(const char* sa, unsigned va0, unsigned va1, unsigned la, const char* sb, unsigned vb, unsigned lb) {
  unsigned keep;
  asm volatile("s_mov_b32 %0, m0\n\ts_mov_b32 m0, %4\n\ts_nop 0\n\tglobal_load_lds_dwordx4 %2, %1\n\t"
               "s_add_u32 m0, m0, 1024\n\ts_nop 0\n\tglobal_load_lds_dwordx4 %3, %1\n\t"
               "s_mov_b32 m0, %7\n\ts_nop 0\n\tglobal_load_lds_dwordx4 %6, %5\n\t"
               "s_add_u32 m0, m0, 8064\n\ts_nop 0\n\tglobal_load_lds_dwordx4 %6, %5 offset:128\n\t"
               "s_add_u32 m0, m0, 8064\n\ts_nop 0\n\tglobal_load_lds_dwordx4 %6, %5 offset:256\n\t"
               "s_add_u32 m0, m0, 8064\n\ts_nop 0\n\tglobal_load_lds_dwordx4 %6, %5 offset:384\n\t"
               "s_mov_b32 m0, %0"
               : "=&s"(keep) : "s"(sa), "v"(va0), "v"(va1), "s"(la), "s"(sb), "v"(vb), "s"(lb) : "memory", "scc");
}
__device__ __forceinline__ s16x4 trread(LAS char* p) {
  return __builtin_amdgcn_ds_read_tr16_b64_v4i16((LAS s16x4*)p);
}
__device__ __forceinline__ bf16x8 cat44(s16x4 a, s16x4 b) {
  return __builtin_shufflevector(a, b, 0, 1, 2, 3, 4, 5, 6, 7);
}
#define WAIT_VM(n) asm volatile("s_waitcnt vmcnt(" #n ")" ::: "memory")
#define WAIT_LGKM0() asm volatile("s_waitcnt lgkmcnt(0)" ::: "memory")
#define BAR() __builtin_amdgcn_s_barrier()

constexpr int STG = 49152;
constexpr int RT_OFF = 3 * STG;
constexpr int LDS_IN = RT_OFF + 16384;

__global__ __launch_bounds__(512, 2) void k_in(const char* __restrict__ x, const char* __restrict__ wqkv, const char* __restrict__ w1,
                                               const char* __restrict__ b1, const char* __restrict__ w2, const char* __restrict__ wo,
                                               char* __restrict__ ws) {
  extern __shared__ __attribute__((aligned(16))) char smem_[];
  LAS char* smem = (LAS char*)smem_;
  const int tid = threadIdx.x, lane = tid & 63, w = __builtin_amdgcn_readfirstlane(tid >> 6);
  const int b = blockIdx.x, xcd = b & 7, idx = b >> 3;
  const int rt = (xcd & 1) * 8 + (idx & 7), cg = (xcd >> 1) * 4 + (idx >> 3);
  const int rot = ((idx & 7) + 3 * (idx >> 3)) % 10;
  int u0, nu, ldw, typ;
  const char* W;
  if (cg < 9) {
    typ = cg / 3;
    const int r = cg - 3 * typ;
    u0 = 40 * typ + (r == 0 ? 0 : (r == 1 ? 14 : 27));
    nu = r == 0 ? 14 : 13;
    W = wqkv; ldw = 3840;
  } else {
    const int c = cg - 9;
    typ = 3;
    u0 = c < 6 ? 14 * c : 83;
    nu = c < 5 ? 14 : 13;
    W = w1; ldw = 3072;
  }
  const int line0 = u0 >> 2, lu0 = u0 & 3;
  const int g = lane >> 4, l15 = lane & 15, lr = lane >> 3, ls = lane & 7;

  const int bk = 8 * w + lr;
  const int bsig = ((bk >> 1) & 1) | (((bk >> 3) & 1) << 1);
  const unsigned vB = bk * ldw + line0 * 128 + ((ls ^ (bsig << 1)) << 4);
  const int ar = 16 * w + lr;
  const unsigned vA0 = (rt * 128 + ar) * 1280 + ((ls ^ ((ar >> 1) & 7)) << 4);
  const unsigned vA1 = (rt * 128 + ar + 8) * 1280 + ((ls ^ (((ar + 8) >> 1) & 7)) << 4);
  const unsigned lds0 = (unsigned)(size_t)smem;
  auto stage = [&](int t) {
    int kt = t + rot; kt = kt >= 10 ? kt - 10 : kt;
    const unsigned dst = lds0 + (t % 3) * STG;
    dma_stage(x + kt * 128, vA0, vA1, dst + w * 2048, W + (size_t)kt * 64 * ldw, vB, dst + 16384 + w * 1024);
  };

  const bool do_rt = b < 170;
  const int rkb = b / 5, rnb = b - 5 * rkb;
  if (do_rt) {
    const char* Wr = rkb < 24 ? w2 + (size_t)rkb * 64 * 1280 : wo + (size_t)(rkb - 24) * 64 * 1280;
    const unsigned vr = bk * 1280 + rnb * 256 + ((ls ^ (bsig << 1)) << 4);
    dma1(Wr, vr, lds0 + RT_OFF + w * 1024);
    dma1(Wr + 128, vr, lds0 + RT_OFF + 8192 + w * 1024);
  }
  stage(0);
  stage(1);

  const int mw = w & 3, nh = w >> 2;
  const int q4 = l15 >> 2, p4 = lane & 3;
  const int sigl = ((q4 >> 1) & 1) | ((g & 1) << 1);
  const int xsw = g ^ (l15 >> 1);
  int xa[2][2];
#pragma unroll
  for (int i = 0; i < 2; ++i) {
    const int row = 32 * mw + 16 * (i ^ nh) + l15;
    xa[i][0] = row * 128 + (xsw << 4);
    xa[i][1] = row * 128 + ((xsw ^ 4) << 4);
  }
  int wa[7];
#pragma unroll
  for (int j = 0; j < 7; ++j) {
    int jt = j + 2 * mw; jt = jt >= 7 ? jt - 7 : jt;
    int lu = lu0 + 7 * nh + jt; lu = lu > 15 ? 15 : lu;
    wa[j] = 16384 + (lu >> 2) * 8192 + (8 * g + q4) * 128 + (((lu & 3) ^ sigl) << 5) + p4 * 8;
  }
  f32x4 acc[2][7];
#pragma unroll
  for (int i = 0; i < 2; ++i)
#pragma unroll
    for (int j = 0; j < 7; ++j) acc[i][j] = f32x4{0.f, 0.f, 0.f, 0.f};
  float sx = 0.f, sxx = 0.f, cs0 = 0.f, cs1 = 0.f;
  const unsigned ONES = 0x3f803f80u;

  for (int t = 0; t < 10; ++t) {
    if (t < 9) WAIT_VM(6); else WAIT_VM(0);
    BAR();
    if (t == 0 && do_rt) {
      char* dstb = ws + (rkb < 24 ? OFF_W2F : OFF_WOF);
      const int nks = rkb < 24 ? 48 : 20, kb = rkb < 24 ? rkb : rkb - 24;
      const int nt = 8 * rnb + w, ct = nt / 5, jj = nt - 5 * ct;
      LAS char* rp = smem + RT_OFF + (w >> 2) * 8192 + (8 * g + q4) * 128 + (((w & 3) ^ sigl) << 5) + p4 * 8;
#pragma unroll
      for (int ks = 0; ks < 2; ++ks) {
        bf16x8 f = cat44(trread(rp + ks * 4096), trread(rp + ks * 4096 + 512));
        *(bf16x8*)(dstb + ((size_t)(ct * nks + 2 * kb + ks) * 5 + jj) * 1024 + lane * 16) = f;
      }
    }
    if (t < 8) stage(t + 2);
    LAS char* sb = smem + (t % 3) * STG;
#pragma unroll
    for (int ks = 0; ks < 2; ++ks) {
      bf16x8 xf[2], wf[7];
#pragma unroll
      for (int i = 0; i < 2; ++i) xf[i] = *(LAS bf16x8*)(sb + xa[i][ks]);
#pragma unroll
      for (int j = 0; j < 7; ++j) wf[j] = cat44(trread(sb + wa[j] + ks * 4096), trread(sb + wa[j] + ks * 4096 + 512));
#pragma unroll
      for (int j = 0; j < 7; ++j)
#pragma unroll
        for (int i = 0; i < 2; ++i) acc[i][j] = mfma16(wf[j], xf[i], acc[i][j]);
      const u32x4 xu = __builtin_bit_cast(u32x4, xf[0]);
      const u32x4 w0 = __builtin_bit_cast(u32x4, wf[0]), w1u = __builtin_bit_cast(u32x4, wf[1]);
#pragma unroll
      for (int e = 0; e < 4; ++e) {
        sx = dot2(xu[e], ONES, sx);
        sxx = dot2(xu[e], xu[e], sxx);
        cs0 = dot2(w0[e], ONES, cs0);
        cs1 = dot2(w1u[e], ONES, cs1);
      }
    }
    WAIT_LGKM0();
  }
  BAR();
  sx += __shfl_xor(sx, 16); sx += __shfl_xor(sx, 32);
  sxx += __shfl_xor(sxx, 16); sxx += __shfl_xor(sxx, 32);
  cs0 += __shfl_xor(cs0, 16); cs0 += __shfl_xor(cs0, 32);
  cs1 += __shfl_xor(cs1, 16); cs1 += __shfl_xor(cs1, 32);
  LAS float* st = (LAS float*)smem;
  LAS float* csl = (LAS float*)(smem + 1024);
  if (lane < 16) {
    const float mean = sx * (1.f / 640.f);
    float var = sxx * (1.f / 640.f) - mean * mean;
    var = var < 0.f ? 0.f : var;
    const int row = 32 * mw + 16 * nh + l15;
    st[row * 2] = mean;
    st[row * 2 + 1] = rsqrtf(var + 1e-5f);
    int j0 = 2 * mw, j1 = 2 * mw + 1; j1 = j1 >= 7 ? j1 - 7 : j1;
    csl[(nh * 7 + j0) * 16 + l15] = cs0;
    csl[(nh * 7 + j1) * 16 + l15] = cs1;
  }
  __syncthreads();
  float mean[2], rstd[2];
#pragma unroll
  for (int i = 0; i < 2; ++i) {
    const int row = 32 * mw + 16 * (i ^ nh) + l15;
    mean[i] = st[row * 2];
    rstd[i] = st[row * 2 + 1];
  }
  const float qs = typ == 0 ? 0.22360679774997896f * 1.4426950408889634f : 1.f;
  char* outb = ws + (typ == 3 ? OFF_H : OFF_QKV);
  const int tok = rt * 128 + 32 * mw + 16 * ((g & 1) ^ nh) + l15;
#pragma unroll
  for (int j = 0; j < 7; ++j) {
    int jt = j + 2 * mw; jt = jt >= 7 ? jt - 7 : jt;
    const int ul = 7 * nh + jt;
    if (ul < nu) {
      const int u = u0 + ul;
      const f32x4 c4 = *(LAS f32x4*)(csl + (nh * 7 + jt) * 16 + 4 * g);
      float v[2][4];
#pragma unroll
      for (int i = 0; i < 2; ++i)
#pragma unroll
        for (int r = 0; r < 4; ++r) v[i][r] = rstd[i] * (acc[i][j][r] - mean[i] * c4[r]);
      if (typ == 3) {
        const u32x2 bb = *(const u32x2*)(b1 + (u * 16 + 4 * g) * 2);
        const float bv[4] = {bflo(bb[0]), bfhi(bb[0]), bflo(bb[1]), bfhi(bb[1])};
#pragma unroll
        for (int i = 0; i < 2; ++i)
#pragma unroll
          for (int r = 0; r < 4; ++r) { float h = v[i][r] + bv[r]; h = h > 0.f ? h : 0.f; v[i][r] = h * h; }
      } else {
#pragma unroll
        for (int i = 0; i < 2; ++i)
#pragma unroll
          for (int r = 0; r < 4; ++r) v[i][r] *= qs;
      }
      const unsigned x0 = pk2(v[0][0], v[0][1]), x1 = pk2(v[0][2], v[0][3]);
      const unsigned y0 = pk2(v[1][0], v[1][1]), y1 = pk2(v[1][2], v[1][3]);
      const auto s0 = __builtin_amdgcn_permlane16_swap(x0, y0, false, false);
      const auto s1 = __builtin_amdgcn_permlane16_swap(x1, y1, false, false);
      const u32x4 o = {s0[0], s1[0], s0[1], s1[1]};
      *(u32x4*)(outb + ((size_t)(2 * u + (g >> 1)) * 2048 + tok) * 16) = o;
    }
  }
}

template <int NKS, int DEPTH>
__device__ __forceinline__ void frag_gemm(const char* __restrict__ ap, const char* __restrict__ wp, f32x4 (&acc)[4][5]) {
  bf16x8 af[DEPTH][4], wf[DEPTH][5];
#pragma unroll
  for (int s = 0; s < DEPTH && s < NKS; ++s) {
#pragma unroll
    for (int j = 0; j < 5; ++j) wf[s][j] = *(const bf16x8*)(wp + s * 5120 + j * 1024);
#pragma unroll
    for (int i = 0; i < 4; ++i) af[s][i] = *(const bf16x8*)(ap + (size_t)s * 4 * PLANE + i * 256);
  }
  __builtin_amdgcn_sched_barrier(0);
#pragma unroll
  for (int s = 0; s < NKS; ++s) {
    const int c = s % DEPTH;
#pragma unroll
    for (int i = 0; i < 4; ++i)
#pragma unroll
      for (int j = 0; j < 5; ++j) acc[i][j] = mfma16(wf[c][j], af[c][i], acc[i][j]);
    __builtin_amdgcn_sched_barrier(0);
    if (s + DEPTH < NKS) {
#pragma unroll
      for (int j = 0; j < 5; ++j) wf[c][j] = *(const bf16x8*)(wp + (s + DEPTH) * 5120 + j * 1024);
#pragma unroll
      for (int i = 0; i < 4; ++i) af[c][i] = *(const bf16x8*)(ap + (size_t)(s + DEPTH) * 4 * PLANE + i * 256);
      __builtin_amdgcn_sched_barrier(0);
    }
  }
}
__device__ __forceinline__ void red_write(LAS char* red, int kw, int lane, const f32x4 (&acc)[4][5]) {
#pragma unroll
  for (int i = 0; i < 4; ++i) {
    if (i != kw) {
      const int slot = kw - (kw > i ? 1 : 0);
#pragma unroll
      for (int j = 0; j < 5; ++j) *(LAS f32x4*)(red + ((i * 3 + slot) * 5 + j) * 1024 + lane * 16) = acc[i][j];
    }
  }
}
__device__ __forceinline__ void red_sum(LAS char* red, int kw, int lane, const f32x4 (&acc)[4][5], f32x4 (&o)[5]) {
#pragma unroll
  for (int j = 0; j < 5; ++j) {
    f32x4 a = kw == 0 ? acc[0][j] : (kw == 1 ? acc[1][j] : (kw == 2 ? acc[2][j] : acc[3][j]));
#pragma unroll
    for (int s = 0; s < 3; ++s) a += *(LAS f32x4*)(red + ((kw * 3 + s) * 5 + j) * 1024 + lane * 16);
    o[j] = a;
  }
}

constexpr int KP = 8192, VP = 8192 + 128;
constexpr int K_OFF = 0, V_OFF = 3 * KP, RED_OFF = V_OFF + 4 * VP;
constexpr int LDS_MID = RED_OFF + 61440;

template <int FIRST>
__device__ __forceinline__ f32x4 mask_tile(f32x4 s, int g, int l15) {
  f32x4 r;
#pragma unroll
  for (int e = 0; e < 4; ++e) {
    const bool ok = FIRST ? (4 * g + e > l15) : (4 * g + e <= l15);
    r[e] = ok ? s[e] : -INFINITY;
  }
  return r;
}

__global__ __launch_bounds__(512, 2) void k_mid(const char* __restrict__ b2, char* __restrict__ ws) {
  extern __shared__ __attribute__((aligned(16))) char smem_[];
  LAS char* smem = (LAS char*)smem_;
  const int tid = threadIdx.x, lane = tid & 63, w = __builtin_amdgcn_readfirstlane(tid >> 6);
  const int b = blockIdx.x, xcd = b & 7, idx = b >> 3;
  const int g = lane >> 4, l15 = lane & 15;
  const int head = idx, q0 = xcd * 256;
  const int ch = (5 * head) >> 1;
  {
#pragma unroll
    for (int i = 0; i < 6; ++i) {
      const int n = w * 6 + i, pl = n >> 3, seg = n & 7;
      int tk = q0 - 256 + seg * 64 + lane; tk = tk < 0 ? 0 : tk;
      const int gp = pl < 3 ? 80 + ch + pl : 160 + ch + (pl - 3);
      const int lo = pl < 3 ? K_OFF + pl * KP : V_OFF + (pl - 3) * VP;
      dma1(ws + OFF_QKV + (size_t)gp * PLANE, tk * 16, (unsigned)(size_t)smem + lo + seg * 1024);
    }
    *(LAS u32x4*)(smem + V_OFF + 3 * VP + tid * 16) = u32x4{0x3f803f80u, 0x3f803f80u, 0x3f803f80u, 0x3f803f80u};
  }
  WAIT_VM(0);
  __syncthreads();
  if (w < 4) {
    const int mt = (xcd >> 1) * 8 + (idx & 7), ct = (xcd & 1) * 4 + (idx >> 3);
    f32x4 acc[4][5];
#pragma unroll
    for (int i = 0; i < 4; ++i)
#pragma unroll
      for (int j = 0; j < 5; ++j) acc[i][j] = f32x4{0.f, 0.f, 0.f, 0.f};
    const char* ap = ws + OFF_H + ((size_t)(48 * w + g) * 2048 + 64 * mt + l15) * 16;
    const char* wp = ws + OFF_W2F + (size_t)(ct * 48 + 12 * w) * 5120 + lane * 16;
    frag_gemm<12, 3>(ap, wp, acc);
    red_write(smem + RED_OFF, w, lane, acc);
    __syncthreads();
    f32x4 o[5];
    red_sum(smem + RED_OFF, w, lane, acc, o);
#pragma unroll
    for (int j = 0; j < 5; ++j) {
      const u32x2 bb = *(const u32x2*)(b2 + (80 * ct + 16 * j + 4 * g) * 2);
      const float bv[4] = {bflo(bb[0]), bfhi(bb[0]), bflo(bb[1]), bfhi(bb[1])};
      f32x4 r;
#pragma unroll
      for (int e = 0; e < 4; ++e) { const float t = o[j][e] + bv[e]; r[e] = t / (1.f + __expf(-t)); }
      *(f32x4*)(ws + OFF_G + ((size_t)((mt * 8 + ct) * 4 + w) * 5 + j) * 1024 + lane * 16) = r;
    }
  } else {
    const int aw = w - 4;
    const int odd = head & 1;
    const int gq = g < 2 ? g : 2;
    const int q4 = l15 >> 2, p4 = lane & 3;
    for (int ps = 0; ps < 2; ++ps) {
      const int ql = 64 * aw + 32 * ps;
      bf16x8 qf[2];
#pragma unroll
      for (int a = 0; a < 2; ++a) {
        u32x4 v = *(const u32x4*)(ws + OFF_QKV + ((size_t)(ch + gq) * 2048 + q0 + ql + 16 * a + l15) * 16);
        const bool zlo = g == 3 || (odd && g == 0), zhi = g == 3 || (!odd && g == 2);
        v[0] = zlo ? 0u : v[0]; v[1] = zlo ? 0u : v[1];
        v[2] = zhi ? 0u : v[2]; v[3] = zhi ? 0u : v[3];
        qf[a] = __builtin_bit_cast(bf16x8, v);
      }
      const int tmin = q0 == 0 ? (256 - ql) >> 4 : 0;
      LAS char* kp = smem + K_OFF + gq * KP + (ql + l15) * 16;
      f32x4 sa[17], sb[17];
      const f32x4 z4 = {0.f, 0.f, 0.f, 0.f}, ninf = {-INFINITY, -INFINITY, -INFINITY, -INFINITY};
#pragma unroll
      for (int T = 0; T < 18; ++T) {
        const bf16x8 kf = *(LAS bf16x8*)(kp + T * 256);
        const f32x4 c = T >= tmin ? z4 : ninf;
        if (T < 17) sa[T] = mfma16(kf, qf[0], c);
        if (T > 0) sb[T - 1] = mfma16(kf, qf[1], c);
      }
      sa[0] = mask_tile<1>(sa[0], g, l15); sa[16] = mask_tile<0>(sa[16], g, l15);
      sb[0] = mask_tile<1>(sb[0], g, l15); sb[16] = mask_tile<0>(sb[16], g, l15);
      float ma = sa[16][0], mb = sb[16][0];
#pragma unroll
      for (int T = 0; T < 17; ++T) {
        ma = fmaxf(fmaxf(ma, sa[T][0]), sa[T][1]); ma = fmaxf(fmaxf(ma, sa[T][2]), sa[T][3]);
        mb = fmaxf(fmaxf(mb, sb[T][0]), sb[T][1]); mb = fmaxf(fmaxf(mb, sb[T][2]), sb[T][3]);
      }
      ma = fmaxf(ma, __shfl_xor(ma, 16)); ma = fmaxf(ma, __shfl_xor(ma, 32));
      mb = fmaxf(mb, __shfl_xor(mb, 16)); mb = fmaxf(mb, __shfl_xor(mb, 32));
      f32x4 oa[2] = {z4, z4}, ob[2] = {z4, z4};
      LAS char* vp = smem + V_OFF + (p4 >> 1) * VP + (ql + 4 * g + q4) * 16 + (p4 & 1) * 8;
#pragma unroll
      for (int u = 0; u < 9; ++u) {
        bf16x8 vf[2];
#pragma unroll
        for (int bl = 0; bl < 2; ++bl)
          vf[bl] = cat44(trread(vp + bl * 2 * VP + u * 512), trread(vp + bl * 2 * VP + u * 512 + 256));
        u32x4 pa, pb;
        {
          const f32x4 s0 = sa[2 * u];
          pa[0] = pk2(__builtin_amdgcn_exp2f(s0[0] - ma), __builtin_amdgcn_exp2f(s0[1] - ma));
          pa[1] = pk2(__builtin_amdgcn_exp2f(s0[2] - ma), __builtin_amdgcn_exp2f(s0[3] - ma));
          if (u < 8) {
            const f32x4 s1 = sa[2 * u + 1];
            pa[2] = pk2(__builtin_amdgcn_exp2f(s1[0] - ma), __builtin_amdgcn_exp2f(s1[1] - ma));
            pa[3] = pk2(__builtin_amdgcn_exp2f(s1[2] - ma), __builtin_amdgcn_exp2f(s1[3] - ma));
          } else { pa[2] = 0u; pa[3] = 0u; }
          if (u > 0) {
            const f32x4 t0 = sb[2 * u - 1];
            pb[0] = pk2(__builtin_amdgcn_exp2f(t0[0] - mb), __builtin_amdgcn_exp2f(t0[1] - mb));
            pb[1] = pk2(__builtin_amdgcn_exp2f(t0[2] - mb), __builtin_amdgcn_exp2f(t0[3] - mb));
          } else { pb[0] = 0u; pb[1] = 0u; }
          const f32x4 t1 = sb[2 * u];
          pb[2] = pk2(__builtin_amdgcn_exp2f(t1[0] - mb), __builtin_amdgcn_exp2f(t1[1] - mb));
          pb[3] = pk2(__builtin_amdgcn_exp2f(t1[2] - mb), __builtin_amdgcn_exp2f(t1[3] - mb));
        }
#pragma unroll
        for (int bl = 0; bl < 2; ++bl) {
          oa[bl] = mfma16(vf[bl], __builtin_bit_cast(bf16x8, pa), oa[bl]);
          ob[bl] = mfma16(vf[bl], __builtin_bit_cast(bf16x8, pb), ob[bl]);
        }
      }
      const float ia = 1.f / __shfl(oa[1][0], l15 + 32), ib = 1.f / __shfl(ob[1][0], l15 + 32);
#pragma unroll
      for (int a = 0; a < 2; ++a) {
        const float il = a ? ib : ia;
        const int tok = q0 + ql + 16 * a + l15;
#pragma unroll
        for (int bl = 0; bl < 2; ++bl) {
          const f32x4 o = a ? ob[bl] : oa[bl];
          const int slot = 16 * bl + 4 * g;
          const bool ok = odd ? (slot >= 4 && slot < 24) : (slot < 20);
          if (ok) {
            const u32x2 pkd = {pk2(o[0] * il, o[1] * il), pk2(o[2] * il, o[3] * il)};
            *(u32x2*)(ws + OFF_O + ((size_t)(ch + (slot >> 3)) * 2048 + tok) * 16 + ((slot >> 2) & 1) * 8) = pkd;
          }
        }
      }
    }
    __syncthreads();
  }
}

__global__ __launch_bounds__(256, 2) void k_out(const char* __restrict__ x, const char* __restrict__ ws, char* __restrict__ out) {
  extern __shared__ __attribute__((aligned(16))) char smem_[];
  LAS char* smem = (LAS char*)smem_;
  const int tid = threadIdx.x, lane = tid & 63, w = __builtin_amdgcn_readfirstlane(tid >> 6);
  const int b = blockIdx.x, xcd = b & 7, idx = b >> 3;
  const int g = lane >> 4, l15 = lane & 15;
  const int mt = (xcd >> 1) * 8 + (idx & 7), ct = (xcd & 1) * 4 + (idx >> 3);
  f32x4 acc[4][5];
#pragma unroll
  for (int i = 0; i < 4; ++i)
#pragma unroll
    for (int j = 0; j < 5; ++j) acc[i][j] = f32x4{0.f, 0.f, 0.f, 0.f};
  f32x4 gv[5];
  u32x2 xv[5];
  const size_t orow = (size_t)(64 * mt + 16 * w + l15) * 1280 + (80 * ct + 4 * g) * 2;
#pragma unroll
  for (int j = 0; j < 5; ++j) {
    gv[j] = *(const f32x4*)(ws + OFF_G + ((size_t)((mt * 8 + ct) * 4 + w) * 5 + j) * 1024 + lane * 16);
    xv[j] = *(const u32x2*)(x + orow + j * 32);
  }
  const char* ap = ws + OFF_O + ((size_t)(20 * w + g) * 2048 + 64 * mt + l15) * 16;
  const char* wp = ws + OFF_WOF + (size_t)(ct * 20 + 5 * w) * 5120 + lane * 16;
  frag_gemm<5, 3>(ap, wp, acc);
  red_write(smem, w, lane, acc);
  __syncthreads();
  f32x4 o[5];
  red_sum(smem, w, lane, acc, o);
#pragma unroll
  for (int j = 0; j < 5; ++j) {
    const float r0 = bflo(xv[j][0]) + o[j][0] * gv[j][0], r1 = bfhi(xv[j][0]) + o[j][1] * gv[j][1];
    const float r2 = bflo(xv[j][1]) + o[j][2] * gv[j][2], r3 = bfhi(xv[j][1]) + o[j][3] * gv[j][3];
    *(u32x2*)(out + orow + j * 32) = u32x2{pk2(r0, r1), pk2(r2, r3)};
  }
}

extern "C" void kernel_launch(void* const* d_in, const int* in_sizes, int n_in, void* d_out, int out_size, void* d_ws,
                              size_t ws_size, hipStream_t stream) {
  const char* x = (const char*)d_in[0];
  const char* wqkv = (const char*)d_in[1];
  const char* wo = (const char*)d_in[2];
  const char* w1 = (const char*)d_in[4];
  const char* b1 = (const char*)d_in[5];
  const char* w2 = (const char*)d_in[6];
  const char* b2 = (const char*)d_in[7];
  char* ws = (char*)d_ws;
  (void)hipFuncSetAttribute((const void*)k_in, hipFuncAttributeMaxDynamicSharedMemorySize, LDS_IN);
  (void)hipFuncSetAttribute((const void*)k_mid, hipFuncAttributeMaxDynamicSharedMemorySize, LDS_MID);
  (void)hipFuncSetAttribute((const void*)k_out, hipFuncAttributeMaxDynamicSharedMemorySize, 61440);
  k_in<<<256, 512, LDS_IN, stream>>>(x, wqkv, w1, b1, w2, wo, ws);
  k_mid<<<256, 512, LDS_MID, stream>>>(b2, ws);
  k_out<<<256, 256, 61440, stream>>>(x, ws, (char*)d_out);
}
```

```cpp
#include <hip/hip_runtime.h>
#include <stdint.h>

#define LAS __attribute__((address_space(3)))
#define GAS __attribute__((address_space(1)))
#define CAS __attribute__((address_space(4)))
typedef short bf16x8 __attribute__((ext_vector_type(8)));
typedef short s16x4 __attribute__((ext_vector_type(4)));
typedef float f32x4 __attribute__((ext_vector_type(4)));
typedef unsigned u32x4 __attribute__((ext_vector_type(4)));
typedef unsigned u32x2 __attribute__((ext_vector_type(2)));
typedef __bf16 bf16x2_t __attribute__((ext_vector_type(2)));
typedef float f32x2 __attribute__((ext_vector_type(2)));
typedef float f32x16 __attribute__((ext_vector_type(16)));
typedef unsigned long long u64;

constexpr unsigned OFF_FL = 0;
constexpr unsigned FL_W = 1024, FL_Q = FL_W + 32768, FL_H = FL_Q + 32768, FL_O = FL_H + 32768;
constexpr unsigned OFF_RS = 163840;
constexpr unsigned OFF_CS = OFF_RS + 16 * 10 * 2048;
constexpr unsigned OFF_QKV = OFF_CS + 2 * 16 * 10 * 1024;
constexpr unsigned OFF_H = OFF_QKV + 32 * 240 * 1024;
constexpr unsigned OFF_O = OFF_H + 32 * 192 * 1024;
constexpr unsigned OFF_W2F = OFF_O + 32 * 80 * 1024;
constexpr unsigned OFF_WOF = OFF_W2F + 8 * 48 * 5120;
constexpr unsigned WS_USED = OFF_WOF + 8 * 20 * 5120;

__device__ __forceinline__ f32x4 mfma16(bf16x8 a, bf16x8 b, f32x4 c) {
  return __builtin_amdgcn_mfma_f32_16x16x32_bf16(a, b, c, 0, 0, 0);
}
__device__ __forceinline__ unsigned pk2(float a, float b) {
  f32x2 v = {a, b};
  bf16x2_t r = __builtin_convertvector(v, bf16x2_t);
  return __builtin_bit_cast(unsigned, r);
}
__device__ __forceinline__ float bflo(unsigned u) { return __builtin_bit_cast(float, u << 16); }
__device__ __forceinline__ float bfhi(unsigned u) { return __builtin_bit_cast(float, u & 0xffff0000u); }
__device__ __forceinline__ s16x4 trread(unsigned a) {
  return __builtin_amdgcn_ds_read_tr16_b64_v4i16((LAS s16x4*)(size_t)a);
}
__device__ __forceinline__ bf16x8 cat44(s16x4 a, s16x4 b) {
  return __builtin_shufflevector(a, b, 0, 1, 2, 3, 4, 5, 6, 7);
}
#define WAIT_VM(n) asm volatile("s_waitcnt vmcnt(" #n ")" ::: "memory")
#define WAIT_LGKM0() __builtin_amdgcn_s_waitcnt(0xC07F)
#define BAR() __builtin_amdgcn_s_barrier()
#define SB() __builtin_amdgcn_sched_barrier(0)
#define SGB(m, n) __builtin_amdgcn_sched_group_barrier(m, n, 0)
#define MAX3(a, b, c) __builtin_fmaxf(__builtin_fmaxf(a, b), c)
#define RLX_AGENT __ATOMIC_RELAXED, __HIP_MEMORY_SCOPE_AGENT
constexpr int SC1 = 16;
template <int N> struct IC { static constexpr int value = N; };

#define STAMP(i)
#define CLK(v)

__device__ __forceinline__ void dma1(const char* sbase, unsigned voff, unsigned lds) {
  unsigned keep;
  asm volatile("s_mov_b32 %0, m0\n\ts_mov_b32 m0, %3\n\ts_nop 0\n\tglobal_load_lds_dwordx4 %1, %2\n\ts_mov_b32 m0, %0"
               : "=&s"(keep) : "v"(voff), "s"(sbase), "s"(lds) : "memory");
}
__device__ __forceinline__ void dma1_sc1(const char* sbase, unsigned voff, unsigned lds) {
  unsigned keep;
  asm volatile("s_mov_b32 %0, m0\n\ts_mov_b32 m0, %3\n\ts_nop 0\n\tglobal_load_lds_dwordx4 %1, %2 sc1\n\ts_mov_b32 m0, %0"
               : "=&s"(keep) : "v"(voff), "s"(sbase), "s"(lds) : "memory");
}
__device__ __forceinline__ void dma_stage(const char* sa, unsigned va0, unsigned va1, unsigned va2, unsigned va3, unsigned la,
                                          const char* sb, unsigned vb0, unsigned vb1, unsigned lb) {
  unsigned keep;
  asm volatile("s_mov_b32 %0, m0\n\ts_mov_b32 m0, %10\n\ts_nop 0\n\tglobal_load_lds_dwordx4 %8, %7\n\t"
               "s_add_u32 m0, m0, 1024\n\ts_nop 0\n\tglobal_load_lds_dwordx4 %9, %7\n\t"
               "s_add_u32 m0, m0, 7040\n\ts_nop 0\n\tglobal_load_lds_dwordx4 %8, %7 offset:128\n\t"
               "s_add_u32 m0, m0, 1024\n\ts_nop 0\n\tglobal_load_lds_dwordx4 %9, %7 offset:128\n\t"
               "s_mov_b32 m0, %6\n\ts_nop 0\n\tglobal_load_lds_dwordx4 %2, %1\n\t"
               "s_add_u32 m0, m0, 1024\n\ts_nop 0\n\tglobal_load_lds_dwordx4 %3, %1\n\t"
               "s_add_u32 m0, m0, 1024\n\ts_nop 0\n\tglobal_load_lds_dwordx4 %4, %1\n\t"
               "s_add_u32 m0, m0, 1024\n\ts_nop 0\n\tglobal_load_lds_dwordx4 %5, %1\n\t"
               "s_mov_b32 m0, %0"
               : "=&s"(keep) : "s"(sa), "v"(va0), "v"(va1), "v"(va2), "v"(va3), "s"(la), "s"(sb), "v"(vb0), "v"(vb1), "s"(lb)
               : "memory", "scc");
}
__device__ __forceinline__ void dma_kv(const char* sb, unsigned vk, unsigned vv, unsigned l) {
  unsigned keep;
  asm volatile("s_mov_b32 %0, m0\n\ts_mov_b32 m0, %4\n\ts_nop 0\n\tglobal_load_lds_dwordx4 %2, %1 sc1\n\t"
               "s_add_u32 m0, m0, 7168\n\ts_nop 0\n\tglobal_load_lds_dwordx4 %2, %1 offset:1024 sc1\n\t"
               "s_add_u32 m0, m0, 7168\n\ts_nop 0\n\tglobal_load_lds_dwordx4 %2, %1 offset:2048 sc1\n\t"
               "s_add_u32 m0, m0, 18432\n\ts_nop 0\n\tglobal_load_lds_dwordx4 %3, %1 sc1\n\t"
               "s_add_u32 m0, m0, 7232\n\ts_nop 0\n\tglobal_load_lds_dwordx4 %3, %1 offset:1024 sc1\n\t"
               "s_add_u32 m0, m0, 7232\n\ts_nop 0\n\tglobal_load_lds_dwordx4 %3, %1 offset:2048 sc1\n\t"
               "s_mov_b32 m0, %0"
               : "=&s"(keep) : "s"(sb), "v"(vk), "v"(vv), "s"(l) : "memory", "scc");
}
__device__ __forceinline__ u32x2 aload8(const char* sbase, unsigned voff) {
  u32x2 r;
  asm volatile("global_load_dwordx2 %0, %1, %2 sc1" : "=v"(r) : "v"(voff), "s"(sbase) : "memory");
  return r;
}
__device__ __forceinline__ u32x4 aload16(const char* sbase, unsigned voff) {
  u32x4 r;
  asm volatile("global_load_dwordx4 %0, %1, %2 sc1" : "=v"(r) : "v"(voff), "s"(sbase) : "memory");
  return r;
}
#define PIN(x) asm volatile("" : "+v"(x))
template <int N> __device__ __forceinline__ void wait_vm() { asm volatile("s_waitcnt vmcnt(%0)" :: "i"(N) : "memory"); }
__device__ __forceinline__ void poll_flags(const char* flag, bool need, u64 tag) {
#pragma unroll 1
  for (int spins = 0; spins < (1 << 20); ++spins) {
    const u64 v = need ? __hip_atomic_load((const GAS u64*)flag, RLX_AGENT) : tag;
    if (__all(v == tag)) break;
    __builtin_amdgcn_s_sleep(1);
  }
  __builtin_amdgcn_fence(__ATOMIC_ACQUIRE, "wavefront");
}
__device__ __forceinline__ unsigned lds_arrive(LAS unsigned* c) {
  unsigned r = 0;
  if ((threadIdx.x & 63) == 0) r = __hip_atomic_fetch_add(c, 1u, __ATOMIC_RELAXED, __HIP_MEMORY_SCOPE_WORKGROUP);
  return __builtin_amdgcn_readfirstlane(r);
}
__device__ __forceinline__ void lds_wait(LAS unsigned* c, unsigned n) {
#pragma unroll 1
  for (int spins = 0; spins < (1 << 22); ++spins) {
    if (__builtin_amdgcn_readfirstlane(__hip_atomic_load(c, __ATOMIC_RELAXED, __HIP_MEMORY_SCOPE_WORKGROUP)) >= n) break;
  }
  __builtin_amdgcn_fence(__ATOMIC_ACQUIRE, "wavefront");
}

#define WPRE 5

constexpr int STG = 32768;
constexpr int RT_OFF = 4 * STG;
constexpr int LDS_ALL = 163840;
constexpr int ST_OFF = 161792;
constexpr int CS_OFF = ST_OFF + 1024, SYNC_OFF = ST_OFF + 2032;
constexpr int KP = 8192, VP = 8192 + 64;
constexpr int K_OFF = 0, V_OFF = 4 * KP;
constexpr int REDS_OFF = 0, REDG_OFF = 66560, SX_OFF = 128000;

__device__ __forceinline__ bf16x8 ld16(__amdgpu_buffer_rsrc_t rs, unsigned voff, unsigned soff) {
  return __builtin_bit_cast(bf16x8, __builtin_amdgcn_raw_buffer_load_b128(rs, voff, soff, SC1));
}
__device__ __forceinline__ void red_write(LAS char* red, int kw, int lane, const f32x4 (&acc)[4][5]) {
#pragma unroll
  for (int i = 0; i < 4; ++i) {
    if (i != kw) {
      const int slot = kw - (kw > i ? 1 : 0);
#pragma unroll
      for (int j = 0; j < 5; ++j) *(LAS f32x4*)(red + ((i * 3 + slot) * 5 + j) * 1024 + lane * 16) = acc[i][j];
    }
  }
}
__device__ __forceinline__ void red_sum(LAS char* red, int kw, int lane, const f32x4 (&acc)[4][5], f32x4 (&o)[5]) {
#pragma unroll
  for (int j = 0; j < 5; ++j) {
    f32x4 a = kw == 0 ? acc[0][j] : (kw == 1 ? acc[1][j] : (kw == 2 ? acc[2][j] : acc[3][j]));
#pragma unroll
    for (int s = 0; s < 3; ++s) a += *(LAS f32x4*)(red + ((kw * 3 + s) * 5 + j) * 1024 + lane * 16);
    o[j] = a;
  }
}
__device__ __forceinline__ int wg_of(int rt, int cg) { return (((cg & 3) * 8 + (rt & 7)) << 3) | ((cg >> 2) << 1) | (rt >> 3); }
template <int N>
__device__ __forceinline__ bool sum_pairs(const u32x4 (&v)[N], unsigned last_on, unsigned tg, float& a, float& b) {
  bool ok = true;
  a = 0.f; b = 0.f;
#pragma unroll
  for (int k = 0; k < N; ++k) {
    ok = ok && v[k][1] == tg && v[k][3] == tg;
    const unsigned m = k == N - 1 ? last_on : 0xffffffffu;
    a += __uint_as_float(v[k][0] & m);
    b += __uint_as_float(v[k][2] & m);
  }
  return __all(ok);
}
template <int N>
__device__ __forceinline__ void regather(__amdgpu_buffer_rsrc_t rs, const unsigned (&vo)[N], unsigned last_on, unsigned tg, float& a, float& b) {
#pragma unroll 1
  for (int spins = 0; spins < (1 << 20); ++spins) {
    u32x4 v[N];
#pragma unroll
    for (int k = 0; k < N; ++k) v[k] = __builtin_amdgcn_raw_buffer_load_b128(rs, vo[k], 0, SC1);
    if (sum_pairs<N>(v, last_on, tg, a, b)) break;
    __builtin_amdgcn_s_sleep(2);
  }
}

template <int NT, int S0>
__device__ __forceinline__ void mfma_part(unsigned (&xa)[2], const unsigned (&wa_)[NT], f32x4 (&acc)[4][NT], char* ws) {
  unsigned wa[NT];
#pragma unroll
  for (int j = 0; j < NT; ++j) wa[j] = wa_[j] + (S0 & 3) * STG;
  bf16x8 xf[2][4], wf[2][NT];
#define LDX(set, i, ks) xf[set][i] = *(LAS bf16x8*)(size_t)(xa[ks] + (i) * 2048)
#define LDW(set, j, ks) wf[set][j] = cat44(trread(wa[j] + (ks) * 4096), trread(wa[j] + (ks) * 4096 + 512))
#define LDSET(set, ks) do { _Pragma("unroll") for (int i = 0; i < 4; ++i) LDX(set, i, ks); \
                            _Pragma("unroll") for (int j = 0; j < NT; ++j) LDW(set, j, ks); } while (0)
#define MMSET(set) do { _Pragma("unroll") for (int j = 0; j < NT; ++j) _Pragma("unroll") for (int i = 0; i < 4; ++i) \
                          acc[i][j] = mfma16(wf[set][j], xf[set][i], acc[i][j]); } while (0)
#define ILV() do { _Pragma("unroll") for (int k = 0; k < 4 + 2 * NT; ++k) { SGB(0x8, 1); SGB(0x100, 1); } SGB(0x8, 2 * NT - 4); } while (0)
  LDSET(0, 0);
  WAIT_LGKM0();
  int buf = S0 & 3;
#pragma unroll 1
  for (int t = 0; t < 10; ++t) {
    LDSET(1, 1);
    MMSET(0);
    ILV();
    SB();
    {
      const int d = buf == 3 ? -3 * STG : STG;
      xa[0] += d; xa[1] += d;
#pragma unroll
      for (int j = 0; j < NT; ++j) wa[j] += d;
      buf = (buf + 1) & 3;
    }
    WAIT_LGKM0();
    if (S0 == 10) asm volatile("s_cmp_lg_u32 %0, 1\n\ts_cbranch_scc1 .Lk%=\n\ts_waitcnt vmcnt(0)\n.Lk%=:" :: "s"(t) : "memory", "scc");
    BAR();
    SB();
    LDSET(0, 0);
    MMSET(1);
    ILV();
    SB();
    WAIT_LGKM0();
  }
#undef LDX
#undef LDW
}
template <int NT, bool ISH>
__device__ __forceinline__ void store_part(const f32x4 (&acc)[4][NT], const float (&mean)[4], const float (&rstd)[4], LAS float* cs,
                                           const u32x2 (&b1v)[3], float qs, __amdgpu_buffer_rsrc_t rs, unsigned vo, unsigned so, int g) {
#pragma unroll
  for (int j = 0; j < NT; ++j) {
    const f32x4 c4 = *(LAS f32x4*)(cs + j * 16 + 4 * g);
    float bv[4] = {0.f, 0.f, 0.f, 0.f};
    if (ISH) { bv[0] = bflo(b1v[j][0]); bv[1] = bfhi(b1v[j][0]); bv[2] = bflo(b1v[j][1]); bv[3] = bfhi(b1v[j][1]); }
#pragma unroll
    for (int pp = 0; pp < 2; ++pp) {
      float v[2][4];
#pragma unroll
      for (int i = 0; i < 2; ++i)
#pragma unroll
        for (int r = 0; r < 4; ++r) {
          float t = rstd[2 * pp + i] * (acc[2 * pp + i][j][r] - mean[2 * pp + i] * c4[r]);
          if (ISH) { t += bv[r]; t = t > 0.f ? t : 0.f; t = t * t; } else t *= qs;
          v[i][r] = t;
        }
      const unsigned x0 = pk2(v[0][0], v[0][1]), x1 = pk2(v[0][2], v[0][3]);
      const unsigned y0 = pk2(v[1][0], v[1][1]), y1 = pk2(v[1][2], v[1][3]);
      const auto s0 = __builtin_amdgcn_permlane16_swap(x0, y0, false, false);
      const auto s1 = __builtin_amdgcn_permlane16_swap(x1, y1, false, false);
      const u32x4 o = {s0[0], s1[0], s0[1], s1[1]};
      __builtin_amdgcn_raw_buffer_store_b128(o, rs, vo + pp * 512, so + 2 * j * 1024, SC1);
    }
  }
}

__global__ __launch_bounds__(512, 2) void k_all(const char* __restrict__ x, const char* __restrict__ wqkv, const char* __restrict__ w1,
                                                const char* __restrict__ b1, const char* __restrict__ w2, const char* __restrict__ wo,
                                                const char* __restrict__ b2, char* ws, char* __restrict__ out) {
  extern __shared__ __attribute__((aligned(16))) char smem_[];
  LAS char* smem = (LAS char*)smem_;
  const int tid = threadIdx.x, lane = tid & 63, w = __builtin_amdgcn_readfirstlane(tid >> 6);
  const int b = blockIdx.x, xcd = b & 7, idx = b >> 3;
  const int g = lane >> 4, l15 = lane & 15, q4 = l15 >> 2, p4 = lane & 3;
  const unsigned lds0 = (unsigned)(size_t)smem;
  const u64 tag = *(const CAS u64*)(ws + OFF_FL) + 1;
  const unsigned tg = (unsigned)tag;
  const __amdgpu_buffer_rsrc_t rs = __builtin_amdgcn_make_buffer_rsrc(ws, 0, WS_USED, 0x00020000);
  LAS unsigned* sync = (LAS unsigned*)(smem + SYNC_OFF);
  STAMP(0);
  const int rt = (xcd & 1) * 8 + (idx & 7), cg = (xcd >> 1) * 4 + (idx >> 3);
  const int cq = cg < 15 ? cg : 14;
  const int rot = (rt + 3 * cg) % 10;
  const int lu1 = (6 * cg) & 3;
  const int sigl = ((q4 >> 1) & 1) | ((g & 1) << 1);
  const int xsw = g ^ (l15 >> 1);
  const int mt = (xcd >> 1) * 8 + (idx & 7), ct = (xcd & 1) * 4 + (idx >> 3);
  const int head = 4 * xcd + (idx & 3), qb = idx >> 2;
  const int ch = (5 * head) >> 1, odd = head & 1;
  f32x4 acc[4][5];
  const unsigned va = g * 1024 + l15 * 16;
  bf16x8 wpre[5][5];
  const unsigned wb = w < 4 ? OFF_W2F + (ct * 48 + 12 * w) * 5120 : OFF_WOF + (ct * 20 + 5 * (w - 4)) * 5120;

  if (w >= 4) {
    __builtin_amdgcn_s_waitcnt(0);
    const int lw = w - 4, lr = lane >> 3, ls = lane & 7, lt = tid - 256;
    const int bk = 16 * lw + lr;
    const int bs0 = ((bk >> 1) & 1) | (((bk >> 3) & 1) << 1), bs1 = bs0 ^ 2;
    const unsigned vq0 = bk * 3840 + cq * 256 + ((ls ^ (bs0 << 1)) << 4), vq1 = (bk + 8) * 3840 + cq * 256 + ((ls ^ (bs1 << 1)) << 4);
    const unsigned vh0 = bk * 3072 + ((6 * cg) >> 2) * 128 + ((ls ^ (bs0 << 1)) << 4);
    const unsigned vh1 = (bk + 8) * 3072 + ((6 * cg) >> 2) * 128 + ((ls ^ (bs1 << 1)) << 4);
    const unsigned vA = (rt * 128 + 32 * lw + lr) * 1280 + ((ls ^ (lr >> 1)) << 4);
    const bool do_rt = b < 170;
    const int rkb = b / 5, rnb = b - 5 * rkb;
    int ktn = rot;
    auto issue = [&](auto S) {
      constexpr int s = decltype(S)::value;
      const unsigned dst = lds0 + (s & 3) * STG;
      const char* sw = s >= 10 ? w1 + (size_t)ktn * (64 * 3072) : wqkv + (size_t)ktn * (64 * 3840);
      dma_stage(x + ktn * 128, vA, (vA + 10240) ^ 64u, vA + 20480, (vA + 30720) ^ 64u, dst + lw * 4096, sw,
                s >= 10 ? vh0 : vq0, s >= 10 ? vh1 : vq1, dst + 16384 + lw * 2048);
      ktn = ktn == 9 ? 0 : ktn + 1;
    };
    issue(IC<0>{});
    if (do_rt) {
      const char* Wr = rkb < 24 ? w2 + (size_t)rkb * 64 * 1280 : wo + (size_t)(rkb - 24) * 64 * 1280;
      const unsigned r0 = bk * 1280 + rnb * 256 + ((ls ^ (bs0 << 1)) << 4), r1 = (bk + 8) * 1280 + rnb * 256 + ((ls ^ (bs1 << 1)) << 4);
      dma1(Wr, r0, lds0 + RT_OFF + lw * 2048);
      dma1(Wr, r1, lds0 + RT_OFF + lw * 2048 + 1024);
      dma1(Wr + 128, r0, lds0 + RT_OFF + 8192 + lw * 2048);
      dma1(Wr + 128, r1, lds0 + RT_OFF + 8192 + lw * 2048 + 1024);
    }
    issue(IC<1>{}); issue(IC<2>{}); issue(IC<3>{});
    if (lt < 4) sync[lt] = 0u;
    WAIT_VM(24);
    BAR();
    STAMP(1);
    const bf16x8 ONE8 = {0x3f80, 0x3f80, 0x3f80, 0x3f80, 0x3f80, 0x3f80, 0x3f80, 0x3f80};
    const f32x4 z4 = {0.f, 0.f, 0.f, 0.f};
    bf16x8 sw_[2][2], sx_[2][2];
    u32x4 gr[5], gc[3];
    u32x2 fl;
    const int fkb = lane < 12 ? 6 * lw + (lane >> 1) : 24 + ((5 * lw) >> 1) + (lane < 18 ? (lane - 12) >> 1 : 0);
    const unsigned fW = FL_W + (fkb * 5 + ((5 * ct + (lane & 1) * 4) >> 3)) * 128;
    const int ty = lane < 24 ? lane >> 3 : 0, rr0 = 2 * qb - 2 + ((lane >> 1) & 3);
    const int rr = rr0 < 0 ? 0 : (ty == 0 && rr0 < 2 * qb ? 2 * qb : rr0);
    const unsigned fQ = FL_Q + wg_of(rr, 5 * ty + ((ch + (lane & 1) * 2) >> 4)) * 128;
    unsigned ro[5], co[3];
#pragma unroll
    for (int k = 0; k < 5; ++k) ro[k] = OFF_RS + (rt * 10 + 2 * k + (lane >> 5)) * 2048 + (32 * lw + (lane & 31)) * 16;
#pragma unroll
    for (int k = 0; k < 3; ++k) co[k] = OFF_CS + (cg * 10 + (k == 2 && g >= 2 ? g : g + 4 * k)) * 1024 + (16 * lw + l15) * 16;
    const unsigned c_on = g < 2 ? 0xffffffffu : 0u;
    auto stage = [&](auto S) {
      constexpr int s = decltype(S)::value;
      if constexpr (s == 0 || s == 10) {
        const unsigned sb = lds0 + (s & 3) * STG;
#pragma unroll
        for (int i = 0; i < 2; ++i) {
          const int lux = ((s == 0 ? 0 : lu1) + 2 * lw + i) & 7;
          const unsigned wq = sb + 16384 + (lux >> 2) * 8192 + (8 * g + q4) * 128 + (((lux & 3) ^ sigl) << 5) + p4 * 8;
          sw_[i][0] = cat44(trread(wq), trread(wq + 512));
          sw_[i][1] = cat44(trread(wq + 4096), trread(wq + 4608));
          if (s == 0) {
            const unsigned xq = sb + (32 * lw + 16 * i + l15) * 128;
            sx_[i][0] = *(LAS bf16x8*)(size_t)(xq + (xsw << 4));
            sx_[i][1] = *(LAS bf16x8*)(size_t)(xq + ((xsw ^ 4) << 4));
          }
        }
      }
      if constexpr (s <= 16) wait_vm<(s == 7 || s == 8) ? 25 : ((s >= 10 && s <= 12) ? 41 : ((s == 15 || s == 16) ? 19 : 16))>();
      if constexpr (s == 17) wait_vm<0>();
      if constexpr (s == 9) {
#pragma unroll
        for (int k = 0; k < 5; ++k) PIN(gr[k]);
        float sx, sxx;
        if (!sum_pairs<5>(gr, 0xffffffffu, tg, sx, sxx)) regather<5>(rs, ro, 0xffffffffu, tg, sx, sxx);
        sx += __shfl_xor(sx, 32); sxx += __shfl_xor(sxx, 32);
        const float mean = sx * (1.f / 640.f);
        float var = sxx * (1.f / 640.f) - mean * mean;
        var = var < 0.f ? 0.f : var;
        if (lane < 32) *(LAS f32x2*)(smem + ST_OFF + (32 * lw + lane) * 8) = f32x2{mean, rsqrtf(var + 1e-5f)};
      }
      if constexpr (s == 9 || s == 17) {
#pragma unroll
        for (int k = 0; k < 3; ++k) PIN(gc[k]);
        float c0, c1;
        if (!sum_pairs<3>(gc, c_on, tg, c0, c1)) regather<3>(rs, co, c_on, tg, c0, c1);
        c0 += __shfl_xor(c0, 16); c1 += __shfl_xor(c1, 16);
        c0 += __shfl_xor(c0, 32); c1 += __shfl_xor(c1, 32);
        if (lane < 16 && (s == 9 || lw < 3)) *(LAS f32x2*)(smem + CS_OFF + (s == 9 ? 0 : 512) + (16 * lw + lane) * 8) = f32x2{c0, c1};
      }
      WAIT_LGKM0();
      BAR();
      if constexpr (s < 16) issue(IC<s + 4>{});
      if constexpr (s == 0 || s == 10) {
#pragma unroll
        for (int i = 0; i < 2; ++i) {
          const int nt = 2 * lw + i;
          const u32x4 cu = __builtin_bit_cast(u32x4, mfma16(sw_[i][1], ONE8, mfma16(sw_[i][0], ONE8, z4)));
          if (l15 == 0) {
            const unsigned o = OFF_CS + (((s ? 16 : 0) + cg) * 10 + rot) * 1024 + nt * 128;
            __builtin_amdgcn_raw_buffer_store_b128(u32x4{cu[0], tg, cu[1], tg}, rs, g * 32, o, SC1);
            __builtin_amdgcn_raw_buffer_store_b128(u32x4{cu[2], tg, cu[3], tg}, rs, g * 32 + 16, o, SC1);
          }
          if (s == 0) {
            const f32x4 sx = mfma16(ONE8, sx_[i][1], mfma16(ONE8, sx_[i][0], z4));
            const f32x4 sq = mfma16(sx_[i][1], sx_[i][1], mfma16(sx_[i][0], sx_[i][0], z4));
            const int r = l15 & 3;
            const float sxx = r == 0 ? sq[0] : (r == 1 ? sq[1] : (r == 2 ? sq[2] : sq[3]));
            if (q4 == g)
              __builtin_amdgcn_raw_buffer_store_b128(u32x4{__float_as_uint(sx[0]), tg, __float_as_uint(sxx), tg}, rs,
                                                     (32 * lw + 16 * i + l15) * 16, OFF_RS + (rt * 10 + rot) * 2048, SC1);
          }
        }
      }
      if constexpr (s == 1) {
        if (do_rt) {
          const unsigned dstb = rkb < 24 ? OFF_W2F : OFF_WOF;
          const int nks = rkb < 24 ? 48 : 20, kb = rkb < 24 ? rkb : rkb - 24;
#pragma unroll
          for (int i = 0; i < 2; ++i) {
            const int nl = 2 * lw + i, nt = 8 * rnb + nl, c5 = nt / 5, jj = nt - 5 * c5;
            const unsigned rp = lds0 + RT_OFF + (nl >> 2) * 8192 + (8 * g + q4) * 128 + (((nl & 3) ^ sigl) << 5) + p4 * 8;
#pragma unroll
            for (int ks = 0; ks < 2; ++ks) {
              const bf16x8 f = cat44(trread(rp + ks * 4096), trread(rp + ks * 4096 + 512));
              __builtin_amdgcn_raw_buffer_store_b128(__builtin_bit_cast(u32x4, f), rs, lane * 16,
                                                     dstb + ((c5 * nks + 2 * kb + ks) * 5 + jj) * 1024, SC1);
            }
          }
        }
      }
      if constexpr (s == 4 || s == 11) {
        if (lt == 0 && (s == 4 || cg < 15)) __hip_atomic_store((GAS u64*)(ws + (s == 4 ? FL_W : FL_Q) + b * 128), tag, RLX_AGENT);
      }
      if constexpr (s == 6) {
        fl = aload8(ws, fW);
#pragma unroll
        for (int k = 0; k < 5; ++k) gr[k] = aload16(ws, ro[k]);
      }
      if constexpr (s == 6 || s == 14) {
#pragma unroll
        for (int k = 0; k < 3; ++k) { if (s == 14) co[k] += 16 * 10240; gc[k] = aload16(ws, co[k]); }
      }
      if constexpr (s == 9) {
        PIN(fl);
        if (!__all(fl[0] == tg && fl[1] == (unsigned)(tag >> 32))) poll_flags(ws + fW, true, tag);
#pragma unroll
        for (int k = 0; k < 5; ++k)
#pragma unroll
          for (int j = 0; j < 5; ++j) wpre[k][j] = ld16(rs, lane * 16, wb + k * 5120 + j * 1024);
      }
      if constexpr (s == 16) fl = aload8(ws, fQ);
      if constexpr (s == 17) {
        PIN(fl);
        if (!__all(fl[0] == tg && fl[1] == (unsigned)(tag >> 32))) poll_flags(ws + fQ, true, tag);
#pragma unroll
        for (int i = 0; i < 2; ++i) {
          const int seg = 2 * lw + i;
          int tb = 4 * qb - 4 + seg; tb = tb < 0 ? 0 : tb;
          dma_kv(ws + OFF_QKV + (size_t)(tb * 240 + 80 + ch) * 1024, lane * 16, lane * 16 + 81920, lds0 + seg * 1024);
        }
      }
    };
    stage(IC<0>{}); stage(IC<1>{}); stage(IC<2>{}); stage(IC<3>{}); stage(IC<4>{});
    stage(IC<5>{}); stage(IC<6>{}); stage(IC<7>{}); stage(IC<8>{}); stage(IC<9>{});
    stage(IC<10>{}); stage(IC<11>{}); stage(IC<12>{}); stage(IC<13>{}); stage(IC<14>{});
    stage(IC<15>{}); stage(IC<16>{}); stage(IC<17>{}); stage(IC<18>{}); stage(IC<19>{});
    STAMP(2);
    const int l31 = lane & 31, hh = lane >> 5;
    u32x4 qraw[2][2];
#pragma unroll
    for (int a = 0; a < 2; ++a)
#pragma unroll
      for (int ks = 0; ks < 2; ++ks)
        qraw[a][ks] = __builtin_amdgcn_raw_buffer_load_b128(rs, (ks ? 2 : hh) * 1024 + (32 * a + l31) * 16,
                                                            OFF_QKV + ((4 * qb + lw) * 240 + ch) * 1024, SC1);
    {
      const u32x4 one4 = {0x3f803f80u, 0x3f803f80u, 0x3f803f80u, 0x3f803f80u};
      *(LAS u32x4*)(smem + V_OFF + 3 * VP + lt * 16) = one4;
      *(LAS u32x4*)(smem + V_OFF + 3 * VP + 4096 + lt * 16) = one4;
      *(LAS u32x4*)(smem + K_OFF + 3 * KP + lt * 16) = u32x4{qb == 0 ? 0xff80u : 0u, 0u, 0u, 0u};
      *(LAS u32x4*)(smem + K_OFF + 3 * KP + 4096 + lt * 16) = u32x4{0u, 0u, 0u, 0u};
    }
    bf16x8 qf[2][2];
#pragma unroll
    for (int a = 0; a < 2; ++a) {
      u32x4 v0 = qraw[a][0], v1 = qraw[a][1];
      if (odd && hh == 0) { v0[0] = 0u; v0[1] = 0u; }
      if (hh) v1 = u32x4{0x3f80u, 0u, 0u, 0u};
      else if (!odd) { v1[2] = 0u; v1[3] = 0u; }
      qf[a][0] = __builtin_bit_cast(bf16x8, v0);
      qf[a][1] = __builtin_bit_cast(bf16x8, v1);
    }
    f32x16 mF, z16;
#pragma unroll
    for (int e = 0; e < 16; ++e) {
      mF[e] = (e & 3) + 8 * (e >> 2) + 4 * hh > l31 ? 0.f : -INFINITY;
      z16[e] = 0.f;
    }
    WAIT_VM(0);
    WAIT_LGKM0();
    lds_arrive(sync);
    lds_wait(sync, 4);
    __builtin_amdgcn_s_setprio(2);
    STAMP(5);
    unsigned kp = lds0 + K_OFF + hh * KP + (64 * lw + l31) * 16;
    unsigned vp = lds0 + V_OFF + (2 * (g & 1) + (p4 >> 1)) * VP + (64 * lw + 4 * hh + q4) * 16 + (p4 & 1) * 8;
    unsigned ov = l31 * 16 + hh * 8;
    bf16x8 q0 = qf[0][0], q1 = qf[0][1];
#define LDK(T, ks) (*(LAS bf16x8*)(size_t)(kp + (T) * 512 + (ks) * 2 * KP))
#define MF32(a, b, c) __builtin_amdgcn_mfma_f32_32x32x16_bf16(a, b, c, 0, 0, 0)
#define EX2(s_, e) pk2(__builtin_amdgcn_exp2f((s_)[e]), __builtin_amdgcn_exp2f((s_)[(e) + 1]))
#pragma unroll 1
    for (int un = 0; un < 2; ++un) {
      float m0 = -INFINITY, m1 = -INFINITY;
      {
        bf16x8 kf[9][2];
#pragma unroll
        for (int T = 0; T < 3; ++T) { kf[T][0] = LDK(T, 0); kf[T][1] = LDK(T, 1); }
        f32x16 sp = z16;
#pragma unroll
        for (int T = 0; T < 10; ++T) {
          f32x16 sn = z16;
          if (T + 3 < 9) { kf[T + 3][0] = LDK(T + 3, 0); kf[T + 3][1] = LDK(T + 3, 1); }
          if (T < 9) sn = MF32(kf[T][1], q1, MF32(kf[T][0], q0, T == 0 ? mF : z16));
          if (T == 9) {
#pragma unroll
            for (int e = 0; e < 16; ++e) sp[e] = mF[e] == 0.f ? -INFINITY : sp[e];
          }
          if (T > 0) {
#pragma unroll
            for (int e = 0; e < 16; e += 4) { m0 = MAX3(m0, sp[e], sp[e + 1]); m1 = MAX3(m1, sp[e + 2], sp[e + 3]); }
          }
          SGB(0x100, 2); SGB(0x8, 1); SGB(0x2, 4); SGB(0x8, 1); SGB(0x2, 4);
          asm volatile("" :: "v"(sn), "v"(m0), "v"(m1));
          SB();
          sp = sn;
        }
      }
      float mx = fmaxf(m0, m1);
      mx = fmaxf(mx, __shfl_xor(mx, 32));
      f32x16 ca;
#pragma unroll
      for (int e = 0; e < 16; ++e) ca[e] = -mx;
      f32x16 oa = z16;
      {
        bf16x8 kc[2][2], vf[2][2];
        f32x16 sc[2];
        u32x4 pa[2] = {{0u, 0u, 0u, 0u}, {0u, 0u, 0u, 0u}};
        kc[0][0] = LDK(0, 0); kc[0][1] = LDK(0, 1); kc[1][0] = LDK(1, 0); kc[1][1] = LDK(1, 1);
        sc[0] = MF32(kc[0][1], q1, MF32(kc[0][0], q0, ca + mF));
        sc[1] = z16;
        asm volatile("" :: "v"(sc[0]));
        SB();
#pragma unroll
        for (int T = 0; T < 10; ++T) {
          const int c = T & 1, n = c ^ 1;
          if (T < 9) {
#pragma unroll
            for (int ks = 0; ks < 2; ++ks) vf[c][ks] = cat44(trread(vp + T * 512 + ks * 256), trread(vp + T * 512 + ks * 256 + 128));
          }
          if (T < 8) sc[n] = MF32(kc[n][1], q1, MF32(kc[n][0], q0, ca));
          if (T < 7) { kc[c][0] = LDK(T + 2, 0); kc[c][1] = LDK(T + 2, 1); }
          if (T > 0) oa = MF32(vf[n][1], __builtin_bit_cast(bf16x8, pa[1]), MF32(vf[n][0], __builtin_bit_cast(bf16x8, pa[0]), oa));
          if (T == 8) {
#pragma unroll
            for (int e = 0; e < 16; ++e) sc[c][e] = mF[e] == 0.f ? -INFINITY : sc[c][e];
          }
          if (T < 9) {
#pragma unroll
            for (int e = 0; e < 4; ++e) { pa[0][e] = EX2(sc[c], 2 * e); pa[1][e] = EX2(sc[c], 8 + 2 * e); }
          }
          SGB(0x100, 6);
          if (T == 8) SGB(0x2, 16);
          SGB(0x8, 1); SGB(0x400, 4); SGB(0x2, 1);
#pragma unroll
          for (int k = 0; k < 3; ++k) { SGB(0x8, 1); SGB(0x400, 2); SGB(0x2, 1); SGB(0x400, 2); SGB(0x2, 1); }
          SGB(0x2, 1);
          asm volatile("" :: "v"(sc[n]), "v"(oa), "v"(pa[0]), "v"(pa[1]));
          SB();
        }
      }
      const float il = __builtin_amdgcn_rcpf(oa[12]);
#pragma unroll
      for (int pl = 0; pl < 3; ++pl) {
        const bool ok = pl == 1 || (pl == 0 ? !(odd && hh == 0) : (odd || hh == 0));
        if (ok) {
          const u32x2 pkd = {pk2(oa[4 * pl] * il, oa[4 * pl + 1] * il), pk2(oa[4 * pl + 2] * il, oa[4 * pl + 3] * il)};
          __builtin_amdgcn_raw_buffer_store_b64(pkd, rs, pl * 1024 + ov, OFF_O + ((4 * qb + lw) * 80 + ch) * 1024, SC1);
        }
      }
      kp += 512; vp += 512; ov += 512;
      q0 = qf[1][0]; q1 = qf[1][1];
    }
    __builtin_amdgcn_s_setprio(0);
    STAMP(6);
    WAIT_VM(0);
    if (lds_arrive(sync + 1) == 3 && lane == 0) __hip_atomic_store((GAS u64*)(ws + FL_O + b * 128), tag, RLX_AGENT);
    const unsigned ab = OFF_O + (mt * 80 + 20 * lw) * 1024;
    {
      const int hd = 8 * lw + (lane & 7);
      poll_flags(ws + FL_O + (((((mt >> 2) << 2) | (hd & 3)) << 3) | (hd >> 2)) * 128, lane < 8, tag);
    }
    STAMP(7);
    bf16x8 af[3][4];
#pragma unroll
    for (int k = 0; k < 3; ++k)
#pragma unroll
      for (int i = 0; i < 4; ++i) af[k][i] = ld16(rs, va + i * 256, ab + k * 4096);
    SB();
#pragma unroll
    for (int i = 0; i < 4; ++i)
#pragma unroll
      for (int j = 0; j < 5; ++j) acc[i][j] = z4;
#pragma unroll
    for (int k = 0; k < 5; ++k) {
#pragma unroll
      for (int i = 0; i < 4; ++i)
#pragma unroll
        for (int j = 0; j < 5; ++j) acc[i][j] = mfma16(wpre[k][j], af[k % 3][i], acc[i][j]);
      SB();
      if (k < 2) {
#pragma unroll
        for (int i = 0; i < 4; ++i) af[k][i] = ld16(rs, va + i * 256, ab + (k + 3) * 4096);
        SB();
      }
    }
    STAMP(8);
    lds_wait(sync + 1, 4);
    red_write(smem + REDS_OFF, lw, lane, acc);
  } else {
    const int mw = w & 1, nw = w >> 1;
    u32x2 b1v[3];
#pragma unroll
    for (int j = 0; j < 3; ++j) b1v[j] = *(const u32x2*)(b1 + ((6 * cg + 3 * nw + j) * 16 + 4 * g) * 2);
    unsigned xa[2];
    xa[0] = lds0 + (64 * mw + l15) * 128 + (xsw << 4);
    xa[1] = lds0 + (64 * mw + l15) * 128 + ((xsw ^ 4) << 4);
    const unsigned wl = lds0 + 16384 + (8 * g + q4) * 128 + p4 * 8;
    const unsigned vo = (g >> 1) * 1024 + (16 * (g & 1) + l15) * 16;
    float mean[4], rstd[4];
    BAR();
    STAMP(1);
    {
      unsigned wa[4];
      f32x4 acc[4][4];
#pragma unroll
      for (int j = 0; j < 4; ++j) {
        wa[j] = wl + nw * 8192 + ((j ^ sigl) << 5);
#pragma unroll
        for (int i = 0; i < 4; ++i) acc[i][j] = f32x4{0.f, 0.f, 0.f, 0.f};
      }
      mfma_part<4, 0>(xa, wa, acc, ws);
      STAMP(12);
#pragma unroll
      for (int i = 0; i < 4; ++i) {
        const f32x2 st = *(LAS f32x2*)(smem + ST_OFF + (64 * mw + 16 * i + l15) * 8);
        mean[i] = st[0]; rstd[i] = st[1];
      }
      if (cg < 15)
        store_part<4, false>(acc, mean, rstd, (LAS float*)(smem + CS_OFF) + 64 * nw, b1v,
                             cg < 5 ? 0.22360679774997896f * 1.4426950408889634f : 1.f, rs, vo,
                             OFF_QKV + ((2 * rt + mw) * 240 + 16 * cg + 8 * nw) * 1024, g);
      STAMP(13);
    }
    {
      unsigned wa[3];
      f32x4 acc[4][3];
#pragma unroll
      for (int j = 0; j < 3; ++j) {
        const int lu = lu1 + 3 * nw + j;
        wa[j] = wl + (lu >> 2) * 8192 + (((lu & 3) ^ sigl) << 5);
#pragma unroll
        for (int i = 0; i < 4; ++i) acc[i][j] = f32x4{0.f, 0.f, 0.f, 0.f};
      }
      mfma_part<3, 10>(xa, wa, acc, ws);
      STAMP(2);
      store_part<3, true>(acc, mean, rstd, (LAS float*)(smem + CS_OFF + 512) + 48 * nw, b1v, 1.f, rs, vo,
                          OFF_H + ((2 * rt + mw) * 192 + 12 * cg + 6 * nw) * 1024, g);
    }
    STAMP(3);
#pragma unroll
    for (int k = 0; k < WPRE; ++k)
#pragma unroll
      for (int j = 0; j < 5; ++j) wpre[k][j] = ld16(rs, lane * 16, wb + k * 5120 + j * 1024);
    wait_vm<5 * WPRE>();
    if (lds_arrive(sync + 2) == 3 && lane == 0) __hip_atomic_store((GAS u64*)(ws + FL_H + b * 128), tag, RLX_AGENT);
    STAMP(4);
    const unsigned ab = OFF_H + (mt * 192 + 48 * w) * 1024;
    poll_flags(ws + FL_H + wg_of(mt >> 1, 4 * w + (lane & 3)) * 128, lane < 4, tag);
    STAMP(7);
    bf16x8 af[3][4];
#pragma unroll
    for (int k = 0; k < 3; ++k)
#pragma unroll
      for (int i = 0; i < 4; ++i) af[k][i] = ld16(rs, va + i * 256, ab + k * 4096);
    SB();
#pragma unroll
    for (int i = 0; i < 4; ++i)
#pragma unroll
      for (int j = 0; j < 5; ++j) acc[i][j] = f32x4{0.f, 0.f, 0.f, 0.f};
#pragma unroll
    for (int k = 0; k < 12; ++k) {
#pragma unroll
      for (int i = 0; i < 4; ++i)
#pragma unroll
        for (int j = 0; j < 5; ++j) acc[i][j] = mfma16(wpre[k % WPRE][j], af[k % 3][i], acc[i][j]);
      SB();
      if (k + 3 < 12) {
#pragma unroll
        for (int i = 0; i < 4; ++i) af[k % 3][i] = ld16(rs, va + i * 256, ab + (k + 3) * 4096);
      }
      if (k + WPRE < 12) {
#pragma unroll
        for (int j = 0; j < 5; ++j) wpre[k % WPRE][j] = ld16(rs, lane * 16, wb + (k + WPRE) * 5120 + j * 1024);
      }
      SB();
    }
    STAMP(8);
    red_write(smem + REDG_OFF, w, lane, acc);
  }
  const int ow = w & 3;
  const size_t orow = (size_t)(64 * mt + 16 * ow + l15) * 1280 + (80 * ct + 4 * g) * 2;
  u32x2 xv[5], bb[5];
  if (w < 4) {
#pragma unroll
    for (int j = 0; j < 5; ++j) {
      xv[j] = *(const u32x2*)(x + orow + j * 32);
      bb[j] = *(const u32x2*)(b2 + (80 * ct + 16 * j + 4 * g) * 2);
    }
  }
  __syncthreads();
  STAMP(9);
  f32x4 o[5];
  red_sum(smem + (w < 4 ? REDG_OFF : REDS_OFF), ow, lane, acc, o);
  if (w >= 4) {
#pragma unroll
    for (int j = 0; j < 5; ++j) *(LAS f32x4*)(smem + SX_OFF + (ow * 5 + j) * 1024 + lane * 16) = o[j];
  }
  __syncthreads();
  if (w < 4) {
#pragma unroll
    for (int j = 0; j < 5; ++j) {
      const f32x4 sv = *(LAS f32x4*)(smem + SX_OFF + (ow * 5 + j) * 1024 + lane * 16);
      const float bv[4] = {bflo(bb[j][0]), bfhi(bb[j][0]), bflo(bb[j][1]), bfhi(bb[j][1])};
      const float xr[4] = {bflo(xv[j][0]), bfhi(xv[j][0]), bflo(xv[j][1]), bfhi(xv[j][1])};
      float r[4];
#pragma unroll
      for (int e = 0; e < 4; ++e) {
        const float t = o[j][e] + bv[e];
        r[e] = xr[e] + sv[e] * (t * __builtin_amdgcn_rcpf(1.f + __expf(-t)));
      }
      *(u32x2*)(out + orow + j * 32) = u32x2{pk2(r[0], r[1]), pk2(r[2], r[3])};
    }
  } else if (b == 0 && w == 4) {
#pragma unroll
    for (int i = 0; i < 4; ++i) poll_flags(ws + FL_W + (lane + 64 * i) * 128, true, tag);
    if (lane == 0) __hip_atomic_store((GAS u64*)(ws + OFF_FL), tag, RLX_AGENT);
  }
  STAMP(10);
}

extern "C" void kernel_launch(void* const* d_in, const int* in_sizes, int n_in, void* d_out, int out_size, void* d_ws,
                              size_t ws_size, hipStream_t stream) {
  (void)hipFuncSetAttribute((const void*)k_all, hipFuncAttributeMaxDynamicSharedMemorySize, LDS_ALL);
  k_all<<<256, 512, LDS_ALL, stream>>>((const char*)d_in[0], (const char*)d_in[1], (const char*)d_in[4], (const char*)d_in[5],
                                       (const char*)d_in[6], (const char*)d_in[2], (const char*)d_in[7], (char*)d_ws, (char*)d_out);
}
```

```cpp
#include <hip/hip_runtime.h>
#include <stdint.h>

#define LAS __attribute__((address_space(3)))
#define GAS __attribute__((address_space(1)))
#define CAS __attribute__((address_space(4)))
typedef short bf16x8 __attribute__((ext_vector_type(8)));
typedef short s16x4 __attribute__((ext_vector_type(4)));
typedef float f32x4 __attribute__((ext_vector_type(4)));
typedef unsigned u32x4 __attribute__((ext_vector_type(4)));
typedef unsigned u32x2 __attribute__((ext_vector_type(2)));
typedef __bf16 bf16x2_t __attribute__((ext_vector_type(2)));
typedef float f32x2 __attribute__((ext_vector_type(2)));
typedef float f32x16 __attribute__((ext_vector_type(16)));
typedef unsigned long long u64;

constexpr unsigned OFF_FL = 0;
constexpr unsigned FL_W = 1024, FL_Q = FL_W + 32768, FL_H = FL_Q + 32768, FL_O = FL_H + 32768;
constexpr unsigned OFF_RS = 163840;
constexpr unsigned OFF_CS = OFF_RS + 16 * 10 * 2048;
constexpr unsigned OFF_QKV = OFF_CS + 2 * 16 * 10 * 1024;
constexpr unsigned OFF_H = OFF_QKV + 32 * 240 * 1024;
constexpr unsigned OFF_O = OFF_H + 32 * 192 * 1024;
constexpr unsigned OFF_W2F = OFF_O + 32 * 80 * 1024;
constexpr unsigned OFF_WOF = OFF_W2F + 8 * 48 * 5120;
constexpr unsigned WS_USED = OFF_WOF + 8 * 20 * 5120;

__device__ __forceinline__ f32x4 mfma16(bf16x8 a, bf16x8 b, f32x4 c) {
  return __builtin_amdgcn_mfma_f32_16x16x32_bf16(a, b, c, 0, 0, 0);
}
__device__ __forceinline__ unsigned pk2(float a, float b) {
  f32x2 v = {a, b};
  bf16x2_t r = __builtin_convertvector(v, bf16x2_t);
  return __builtin_bit_cast(unsigned, r);
}
__device__ __forceinline__ float bflo(unsigned u) { return __builtin_bit_cast(float, u << 16); }
__device__ __forceinline__ float bfhi(unsigned u) { return __builtin_bit_cast(float, u & 0xffff0000u); }
__device__ __forceinline__ s16x4 trread(unsigned a) {
  return __builtin_amdgcn_ds_read_tr16_b64_v4i16((LAS s16x4*)(size_t)a);
}
__device__ __forceinline__ bf16x8 cat44(s16x4 a, s16x4 b) {
  return __builtin_shufflevector(a, b, 0, 1, 2, 3, 4, 5, 6, 7);
}
#define WAIT_VM(n) asm volatile("s_waitcnt vmcnt(" #n ")" ::: "memory")
#define WAIT_LGKM0() __builtin_amdgcn_s_waitcnt(0xC07F)
#define BAR() __builtin_amdgcn_s_barrier()
#define SB() __builtin_amdgcn_sched_barrier(0)
#define SGB(m, n) __builtin_amdgcn_sched_group_barrier(m, n, 0)
#define MAX3(a, b, c) __builtin_fmaxf(__builtin_fmaxf(a, b), c)
#define RLX_AGENT __ATOMIC_RELAXED, __HIP_MEMORY_SCOPE_AGENT
constexpr int SC1 = 16;
constexpr int CODE_KB = 32;
template <int N> struct IC { static constexpr int value = N; };

#define STAMP(i)
#define CLK(v)

__device__ __forceinline__ void dma1(const char* sbase, unsigned voff, unsigned lds) {
  unsigned keep;
  asm volatile("s_mov_b32 %0, m0\n\ts_mov_b32 m0, %3\n\ts_nop 0\n\tglobal_load_lds_dwordx4 %1, %2\n\ts_mov_b32 m0, %0"
               : "=&s"(keep) : "v"(voff), "s"(sbase), "s"(lds) : "memory");
}
__device__ __forceinline__ void dma1_sc1(const char* sbase, unsigned voff, unsigned lds) {
  unsigned keep;
  asm volatile("s_mov_b32 %0, m0\n\ts_mov_b32 m0, %3\n\ts_nop 0\n\tglobal_load_lds_dwordx4 %1, %2 sc1\n\ts_mov_b32 m0, %0"
               : "=&s"(keep) : "v"(voff), "s"(sbase), "s"(lds) : "memory");
}
__device__ __forceinline__ void dma_stage(const char* sa, unsigned va0, unsigned va1, unsigned va2, unsigned va3, unsigned la,
                                          const char* sb, unsigned vb0, unsigned vb1, unsigned lb) {
  unsigned keep;
  asm volatile("s_mov_b32 %0, m0\n\ts_mov_b32 m0, %10\n\ts_nop 0\n\tglobal_load_lds_dwordx4 %8, %7\n\t"
               "s_add_u32 m0, m0, 1024\n\ts_nop 0\n\tglobal_load_lds_dwordx4 %9, %7\n\t"
               "s_add_u32 m0, m0, 7040\n\ts_nop 0\n\tglobal_load_lds_dwordx4 %8, %7 offset:128\n\t"
               "s_add_u32 m0, m0, 1024\n\ts_nop 0\n\tglobal_load_lds_dwordx4 %9, %7 offset:128\n\t"
               "s_mov_b32 m0, %6\n\ts_nop 0\n\tglobal_load_lds_dwordx4 %2, %1\n\t"
               "s_add_u32 m0, m0, 1024\n\ts_nop 0\n\tglobal_load_lds_dwordx4 %3, %1\n\t"
               "s_add_u32 m0, m0, 1024\n\ts_nop 0\n\tglobal_load_lds_dwordx4 %4, %1\n\t"
               "s_add_u32 m0, m0, 1024\n\ts_nop 0\n\tglobal_load_lds_dwordx4 %5, %1\n\t"
               "s_mov_b32 m0, %0"
               : "=&s"(keep) : "s"(sa), "v"(va0), "v"(va1), "v"(va2), "v"(va3), "s"(la), "s"(sb), "v"(vb0), "v"(vb1), "s"(lb)
               : "memory", "scc");
}
__device__ __forceinline__ void dma_kv(const char* sb, unsigned vk, unsigned vv, unsigned l) {
  unsigned keep;
  asm volatile("s_mov_b32 %0, m0\n\ts_mov_b32 m0, %4\n\ts_nop 0\n\tglobal_load_lds_dwordx4 %2, %1 sc1\n\t"
               "s_add_u32 m0, m0, 7168\n\ts_nop 0\n\tglobal_load_lds_dwordx4 %2, %1 offset:1024 sc1\n\t"
               "s_add_u32 m0, m0, 7168\n\ts_nop 0\n\tglobal_load_lds_dwordx4 %2, %1 offset:2048 sc1\n\t"
               "s_add_u32 m0, m0, 18432\n\ts_nop 0\n\tglobal_load_lds_dwordx4 %3, %1 sc1\n\t"
               "s_add_u32 m0, m0, 7232\n\ts_nop 0\n\tglobal_load_lds_dwordx4 %3, %1 offset:1024 sc1\n\t"
               "s_add_u32 m0, m0, 7232\n\ts_nop 0\n\tglobal_load_lds_dwordx4 %3, %1 offset:2048 sc1\n\t"
               "s_mov_b32 m0, %0"
               : "=&s"(keep) : "s"(sb), "v"(vk), "v"(vv), "s"(l) : "memory", "scc");
}
__device__ __forceinline__ u32x2 aload8(const char* sbase, unsigned voff) {
  u32x2 r;
  asm volatile("global_load_dwordx2 %0, %1, %2 sc1" : "=v"(r) : "v"(voff), "s"(sbase) : "memory");
  return r;
}
__device__ __forceinline__ u32x4 aload16(const char* sbase, unsigned voff) {
  u32x4 r;
  asm volatile("global_load_dwordx4 %0, %1, %2 sc1" : "=v"(r) : "v"(voff), "s"(sbase) : "memory");
  return r;
}
#define PIN(x) asm volatile("" : "+v"(x))
template <int N> __device__ __forceinline__ void wait_vm() { asm volatile("s_waitcnt vmcnt(%0)" :: "i"(N) : "memory"); }
__device__ __forceinline__ void poll_flags(const char* flag, bool need, u64 tag) {
#pragma unroll 1
  for (int spins = 0; spins < (1 << 20); ++spins) {
    const u64 v = need ? __hip_atomic_load((const GAS u64*)flag, RLX_AGENT) : tag;
    if (__all(v == tag)) break;
    __builtin_amdgcn_s_sleep(1);
  }
  __builtin_amdgcn_fence(__ATOMIC_ACQUIRE, "wavefront");
}
__device__ __forceinline__ unsigned lds_arrive(LAS unsigned* c) {
  unsigned r = 0;
  if ((threadIdx.x & 63) == 0) r = __hip_atomic_fetch_add(c, 1u, __ATOMIC_RELAXED, __HIP_MEMORY_SCOPE_WORKGROUP);
  return __builtin_amdgcn_readfirstlane(r);
}
__device__ __forceinline__ void lds_wait(LAS unsigned* c, unsigned n) {
#pragma unroll 1
  for (int spins = 0; spins < (1 << 22); ++spins) {
    if (__builtin_amdgcn_readfirstlane(__hip_atomic_load(c, __ATOMIC_RELAXED, __HIP_MEMORY_SCOPE_WORKGROUP)) >= n) break;
  }
  __builtin_amdgcn_fence(__ATOMIC_ACQUIRE, "wavefront");
}

#define WPRE 5

constexpr int STG = 32768;
constexpr int RT_OFF = 4 * STG;
constexpr int LDS_ALL = 163840;
constexpr int ST_OFF = 161792;
constexpr int CS_OFF = ST_OFF + 1024, SYNC_OFF = ST_OFF + 2032;
constexpr int KP = 8192, VP = 8192 + 64;
constexpr int K_OFF = 0, V_OFF = 4 * KP;
constexpr int REDS_OFF = 0, REDG_OFF = 66560, SX_OFF = 128000;

__device__ __forceinline__ bf16x8 ld16(__amdgpu_buffer_rsrc_t rs, unsigned voff, unsigned soff) {
  return __builtin_bit_cast(bf16x8, __builtin_amdgcn_raw_buffer_load_b128(rs, voff, soff, SC1));
}
__device__ __forceinline__ void red_write(LAS char* red, int kw, int lane, const f32x4 (&acc)[4][5]) {
#pragma unroll
  for (int i = 0; i < 4; ++i) {
    if (i != kw) {
      const int slot = kw - (kw > i ? 1 : 0);
#pragma unroll
      for (int j = 0; j < 5; ++j) *(LAS f32x4*)(red + ((i * 3 + slot) * 5 + j) * 1024 + lane * 16) = acc[i][j];
    }
  }
}
__device__ __forceinline__ void red_sum(LAS char* red, int kw, int lane, const f32x4 (&acc)[4][5], f32x4 (&o)[5]) {
#pragma unroll
  for (int j = 0; j < 5; ++j) {
    f32x4 a = kw == 0 ? acc[0][j] : (kw == 1 ? acc[1][j] : (kw == 2 ? acc[2][j] : acc[3][j]));
#pragma unroll
    for (int s = 0; s < 3; ++s) a += *(LAS f32x4*)(red + ((kw * 3 + s) * 5 + j) * 1024 + lane * 16);
    o[j] = a;
  }
}
__device__ __forceinline__ int wg_of(int rt, int cg) { return (((cg & 3) * 8 + (rt & 7)) << 3) | ((cg >> 2) << 1) | (rt >> 3); }
template <int N>
__device__ __forceinline__ bool sum_pairs(const u32x4 (&v)[N], unsigned last_on, unsigned tg, float& a, float& b) {
  bool ok = true;
  a = 0.f; b = 0.f;
#pragma unroll
  for (int k = 0; k < N; ++k) {
    ok = ok && v[k][1] == tg && v[k][3] == tg;
    const unsigned m = k == N - 1 ? last_on : 0xffffffffu;
    a += __uint_as_float(v[k][0] & m);
    b += __uint_as_float(v[k][2] & m);
  }
  return __all(ok);
}
template <int N>
__device__ __forceinline__ void regather(__amdgpu_buffer_rsrc_t rs, const unsigned (&vo)[N], unsigned last_on, unsigned tg, float& a, float& b) {
#pragma unroll 1
  for (int spins = 0; spins < (1 << 20); ++spins) {
    u32x4 v[N];
#pragma unroll
    for (int k = 0; k < N; ++k) v[k] = __builtin_amdgcn_raw_buffer_load_b128(rs, vo[k], 0, SC1);
    if (sum_pairs<N>(v, last_on, tg, a, b)) break;
    __builtin_amdgcn_s_sleep(2);
  }
}

template <int NT, int S0>
__device__ __forceinline__ void mfma_part(unsigned (&xa)[2], const unsigned (&wa_)[NT], f32x4 (&acc)[4][NT], char* ws) {
  unsigned wa[NT];
#pragma unroll
  for (int j = 0; j < NT; ++j) wa[j] = wa_[j] + (S0 & 3) * STG;
  bf16x8 xf[2][4], wf[2][NT];
#define LDX(set, i, ks) xf[set][i] = *(LAS bf16x8*)(size_t)(xa[ks] + (i) * 2048)
#define LDW(set, j, ks) wf[set][j] = cat44(trread(wa[j] + (ks) * 4096), trread(wa[j] + (ks) * 4096 + 512))
#define LDSET(set, ks) do { _Pragma("unroll") for (int i = 0; i < 4; ++i) LDX(set, i, ks); \
                            _Pragma("unroll") for (int j = 0; j < NT; ++j) LDW(set, j, ks); } while (0)
#define MMSET(set) do { _Pragma("unroll") for (int j = 0; j < NT; ++j) _Pragma("unroll") for (int i = 0; i < 4; ++i) \
                          acc[i][j] = mfma16(wf[set][j], xf[set][i], acc[i][j]); } while (0)
#define ILV() do { _Pragma("unroll") for (int k = 0; k < 4 + 2 * NT; ++k) { SGB(0x8, 1); SGB(0x100, 1); } SGB(0x8, 2 * NT - 4); } while (0)
  LDSET(0, 0);
  WAIT_LGKM0();
  int buf = S0 & 3;
#pragma unroll 1
  for (int t = 0; t < 10; ++t) {
    LDSET(1, 1);
    MMSET(0);
    ILV();
    SB();
    {
      const int d = buf == 3 ? -3 * STG : STG;
      xa[0] += d; xa[1] += d;
#pragma unroll
      for (int j = 0; j < NT; ++j) wa[j] += d;
      buf = (buf + 1) & 3;
    }
    WAIT_LGKM0();
    if (S0 == 10) asm volatile("s_cmp_lg_u32 %0, 1\n\ts_cbranch_scc1 .Lk%=\n\ts_waitcnt vmcnt(0)\n.Lk%=:" :: "s"(t) : "memory", "scc");
    BAR();
    SB();
    LDSET(0, 0);
    MMSET(1);
    ILV();
    SB();
    WAIT_LGKM0();
  }
#undef LDX
#undef LDW
}
template <int NT, bool ISH>
__device__ __forceinline__ void store_part(const f32x4 (&acc)[4][NT], const float (&mean)[4], const float (&rstd)[4], LAS float* cs,
                                           const u32x2 (&b1v)[3], float qs, __amdgpu_buffer_rsrc_t rs, unsigned vo, unsigned so, int g) {
#pragma unroll
  for (int j = 0; j < NT; ++j) {
    const f32x4 c4 = *(LAS f32x4*)(cs + j * 16 + 4 * g);
    float bv[4] = {0.f, 0.f, 0.f, 0.f};
    if (ISH) { bv[0] = bflo(b1v[j][0]); bv[1] = bfhi(b1v[j][0]); bv[2] = bflo(b1v[j][1]); bv[3] = bfhi(b1v[j][1]); }
#pragma unroll
    for (int pp = 0; pp < 2; ++pp) {
      float v[2][4];
#pragma unroll
      for (int i = 0; i < 2; ++i)
#pragma unroll
        for (int r = 0; r < 4; ++r) {
          float t = rstd[2 * pp + i] * (acc[2 * pp + i][j][r] - mean[2 * pp + i] * c4[r]);
          if (ISH) { t += bv[r]; t = t > 0.f ? t : 0.f; t = t * t; } else t *= qs;
          v[i][r] = t;
        }
      const unsigned x0 = pk2(v[0][0], v[0][1]), x1 = pk2(v[0][2], v[0][3]);
      const unsigned y0 = pk2(v[1][0], v[1][1]), y1 = pk2(v[1][2], v[1][3]);
      const auto s0 = __builtin_amdgcn_permlane16_swap(x0, y0, false, false);
      const auto s1 = __builtin_amdgcn_permlane16_swap(x1, y1, false, false);
      const u32x4 o = {s0[0], s1[0], s0[1], s1[1]};
      __builtin_amdgcn_raw_buffer_store_b128(o, rs, vo + pp * 512, so + 2 * j * 1024, SC1);
    }
  }
}

__global__ __launch_bounds__(512, 2) void k_all(const char* __restrict__ x, const char* __restrict__ wqkv, const char* __restrict__ w1,
                                                const char* __restrict__ b1, const char* __restrict__ w2, const char* __restrict__ wo,
                                                const char* __restrict__ b2, char* ws, char* __restrict__ out) {
  extern __shared__ __attribute__((aligned(16))) char smem_[];
  LAS char* smem = (LAS char*)smem_;
  const int tid = threadIdx.x, lane = tid & 63, w = __builtin_amdgcn_readfirstlane(tid >> 6);
  const int b = blockIdx.x, xcd = b & 7, idx = b >> 3;
  const int g = lane >> 4, l15 = lane & 15, q4 = l15 >> 2, p4 = lane & 3;
  const unsigned lds0 = (unsigned)(size_t)smem;
  asm volatile("" :: "s"(x), "s"(wqkv), "s"(w1), "s"(b1), "s"(w2), "s"(wo), "s"(ws));
  u64 pc_;
  asm volatile("s_getpc_b64 %0" : "=s"(pc_));
  const char* pc0 = (const char*)(pc_ & ~(u64)63);
  u64 tag;
  auto load_tag = [&]() {
    const char* p = ws;
    asm volatile("" : "+s"(p));
    return *(const CAS u64*)(p + OFF_FL) + 1;
  };
  const __amdgpu_buffer_rsrc_t rs = __builtin_amdgcn_make_buffer_rsrc(ws, 0, WS_USED, 0x00020000);
  LAS unsigned* sync = (LAS unsigned*)(smem + SYNC_OFF);
  STAMP(0);
  const int rt = (xcd & 1) * 8 + (idx & 7), cg = (xcd >> 1) * 4 + (idx >> 3);
  const int cq = cg < 15 ? cg : 14;
  const int rot = (rt + 3 * cg) % 10;
  const int lu1 = (6 * cg) & 3;
  const int sigl = ((q4 >> 1) & 1) | ((g & 1) << 1);
  const int xsw = g ^ (l15 >> 1);
  const int mt = (xcd >> 1) * 8 + (idx & 7), ct = (xcd & 1) * 4 + (idx >> 3);
  const int head = 4 * xcd + (idx & 3), qb = idx >> 2;
  const int ch = (5 * head) >> 1, odd = head & 1;
  f32x4 acc[4][5];
  const unsigned va = g * 1024 + l15 * 16;
  bf16x8 wpre[5][5];
  const unsigned wb = w < 4 ? OFF_W2F + (ct * 48 + 12 * w) * 5120 : OFF_WOF + (ct * 20 + 5 * (w - 4)) * 5120;

  if (__builtin_expect(w >= 4, 1)) {
    __builtin_amdgcn_s_waitcnt(0);
    const int lw = w - 4, lr = lane >> 3, ls = lane & 7, lt = tid - 256;
    const int bk = 16 * lw + lr;
    const int bs0 = ((bk >> 1) & 1) | (((bk >> 3) & 1) << 1), bs1 = bs0 ^ 2;
    const unsigned vq0 = bk * 3840 + cq * 256 + ((ls ^ (bs0 << 1)) << 4), vq1 = (bk + 8) * 3840 + cq * 256 + ((ls ^ (bs1 << 1)) << 4);
    const unsigned vh0 = bk * 3072 + ((6 * cg) >> 2) * 128 + ((ls ^ (bs0 << 1)) << 4);
    const unsigned vh1 = (bk + 8) * 3072 + ((6 * cg) >> 2) * 128 + ((ls ^ (bs1 << 1)) << 4);
    const unsigned vA = (rt * 128 + 32 * lw + lr) * 1280 + ((ls ^ (lr >> 1)) << 4);
    const bool do_rt = b < 170;
    const int rkb = b / 5, rnb = b - 5 * rkb;
    int ktn = rot;
    auto issue = [&](auto S) {
      constexpr int s = decltype(S)::value;
      const unsigned dst = lds0 + (s & 3) * STG;
      const char* sw = s >= 10 ? w1 + (size_t)ktn * (64 * 3072) : wqkv + (size_t)ktn * (64 * 3840);
      dma_stage(x + ktn * 128, vA, (vA + 10240) ^ 64u, vA + 20480, (vA + 30720) ^ 64u, dst + lw * 4096, sw,
                s >= 10 ? vh0 : vq0, s >= 10 ? vh1 : vq1, dst + 16384 + lw * 2048);
      ktn = ktn == 9 ? 0 : ktn + 1;
    };
    issue(IC<0>{});
    if (do_rt) {
      const char* Wr = rkb < 24 ? w2 + (size_t)rkb * 64 * 1280 : wo + (size_t)(rkb - 24) * 64 * 1280;
      const unsigned r0 = bk * 1280 + rnb * 256 + ((ls ^ (bs0 << 1)) << 4), r1 = (bk + 8) * 1280 + rnb * 256 + ((ls ^ (bs1 << 1)) << 4);
      dma1(Wr, r0, lds0 + RT_OFF + lw * 2048);
      dma1(Wr, r1, lds0 + RT_OFF + lw * 2048 + 1024);
      dma1(Wr + 128, r0, lds0 + RT_OFF + 8192 + lw * 2048);
      dma1(Wr + 128, r1, lds0 + RT_OFF + 8192 + lw * 2048 + 1024);
    }
    issue(IC<1>{}); issue(IC<2>{}); issue(IC<3>{});
    tag = load_tag();
    const unsigned tg = (unsigned)tag;
    if (lt < 4) sync[lt] = 0u;
    WAIT_VM(24);
    BAR();
    STAMP(1);
    const bf16x8 ONE8 = {0x3f80, 0x3f80, 0x3f80, 0x3f80, 0x3f80, 0x3f80, 0x3f80, 0x3f80};
    const f32x4 z4 = {0.f, 0.f, 0.f, 0.f};
    bf16x8 sw_[2][2], sx_[2][2];
    u32x4 gr[5], gc[3];
    u32x2 fl;
    const int fkb = lane < 12 ? 6 * lw + (lane >> 1) : 24 + ((5 * lw) >> 1) + (lane < 18 ? (lane - 12) >> 1 : 0);
    const unsigned fW = FL_W + (fkb * 5 + ((5 * ct + (lane & 1) * 4) >> 3)) * 128;
    const int ty = lane < 24 ? lane >> 3 : 0, rr0 = 2 * qb - 2 + ((lane >> 1) & 3);
    const int rr = rr0 < 0 ? 0 : (ty == 0 && rr0 < 2 * qb ? 2 * qb : rr0);
    const unsigned fQ = FL_Q + wg_of(rr, 5 * ty + ((ch + (lane & 1) * 2) >> 4)) * 128;
    unsigned ro[5], co[3];
#pragma unroll
    for (int k = 0; k < 5; ++k) ro[k] = OFF_RS + (rt * 10 + 2 * k + (lane >> 5)) * 2048 + (32 * lw + (lane & 31)) * 16;
#pragma unroll
    for (int k = 0; k < 3; ++k) co[k] = OFF_CS + (cg * 10 + (k == 2 && g >= 2 ? g : g + 4 * k)) * 1024 + (16 * lw + l15) * 16;
    const unsigned c_on = g < 2 ? 0xffffffffu : 0u;
    auto stage = [&](auto S) {
      constexpr int s = decltype(S)::value;
      if constexpr (s == 0 || s == 10) {
        const unsigned sb = lds0 + (s & 3) * STG;
#pragma unroll
        for (int i = 0; i < 2; ++i) {
          const int lux = ((s == 0 ? 0 : lu1) + 2 * lw + i) & 7;
          const unsigned wq = sb + 16384 + (lux >> 2) * 8192 + (8 * g + q4) * 128 + (((lux & 3) ^ sigl) << 5) + p4 * 8;
          sw_[i][0] = cat44(trread(wq), trread(wq + 512));
          sw_[i][1] = cat44(trread(wq + 4096), trread(wq + 4608));
          if (s == 0) {
            const unsigned xq = sb + (32 * lw + 16 * i + l15) * 128;
            sx_[i][0] = *(LAS bf16x8*)(size_t)(xq + (xsw << 4));
            sx_[i][1] = *(LAS bf16x8*)(size_t)(xq + ((xsw ^ 4) << 4));
          }
        }
      }
      if constexpr (s <= 16) wait_vm<(s == 7 || s == 8) ? 25 : ((s >= 10 && s <= 12) ? 41 : ((s == 15 || s == 16) ? 19 : 16))>();
      if constexpr (s == 17) wait_vm<0>();
      if constexpr (s == 9) {
#pragma unroll
        for (int k = 0; k < 5; ++k) PIN(gr[k]);
        float sx, sxx;
        if (!sum_pairs<5>(gr, 0xffffffffu, tg, sx, sxx)) regather<5>(rs, ro, 0xffffffffu, tg, sx, sxx);
        sx += __shfl_xor(sx, 32); sxx += __shfl_xor(sxx, 32);
        const float mean = sx * (1.f / 640.f);
        float var = sxx * (1.f / 640.f) - mean * mean;
        var = var < 0.f ? 0.f : var;
        if (lane < 32) *(LAS f32x2*)(smem + ST_OFF + (32 * lw + lane) * 8) = f32x2{mean, rsqrtf(var + 1e-5f)};
      }
      if constexpr (s == 9 || s == 17) {
#pragma unroll
        for (int k = 0; k < 3; ++k) PIN(gc[k]);
        float c0, c1;
        if (!sum_pairs<3>(gc, c_on, tg, c0, c1)) regather<3>(rs, co, c_on, tg, c0, c1);
        c0 += __shfl_xor(c0, 16); c1 += __shfl_xor(c1, 16);
        c0 += __shfl_xor(c0, 32); c1 += __shfl_xor(c1, 32);
        if (lane < 16 && (s == 9 || lw < 3)) *(LAS f32x2*)(smem + CS_OFF + (s == 9 ? 0 : 512) + (16 * lw + lane) * 8) = f32x2{c0, c1};
      }
      WAIT_LGKM0();
      BAR();
      if constexpr (s < 16) issue(IC<s + 4>{});
      if constexpr (s == 0 || s == 10) {
#pragma unroll
        for (int i = 0; i < 2; ++i) {
          const int nt = 2 * lw + i;
          const u32x4 cu = __builtin_bit_cast(u32x4, mfma16(sw_[i][1], ONE8, mfma16(sw_[i][0], ONE8, z4)));
          if (l15 == 0) {
            const unsigned o = OFF_CS + (((s ? 16 : 0) + cg) * 10 + rot) * 1024 + nt * 128;
            __builtin_amdgcn_raw_buffer_store_b128(u32x4{cu[0], tg, cu[1], tg}, rs, g * 32, o, SC1);
            __builtin_amdgcn_raw_buffer_store_b128(u32x4{cu[2], tg, cu[3], tg}, rs, g * 32 + 16, o, SC1);
          }
          if (s == 0) {
            const f32x4 sx = mfma16(ONE8, sx_[i][1], mfma16(ONE8, sx_[i][0], z4));
            const f32x4 sq = mfma16(sx_[i][1], sx_[i][1], mfma16(sx_[i][0], sx_[i][0], z4));
            const int r = l15 & 3;
            const float sxx = r == 0 ? sq[0] : (r == 1 ? sq[1] : (r == 2 ? sq[2] : sq[3]));
            if (q4 == g)
              __builtin_amdgcn_raw_buffer_store_b128(u32x4{__float_as_uint(sx[0]), tg, __float_as_uint(sxx), tg}, rs,
                                                     (32 * lw + 16 * i + l15) * 16, OFF_RS + (rt * 10 + rot) * 2048, SC1);
          }
        }
      }
      if constexpr (s == 1) {
        if (do_rt) {
          const unsigned dstb = rkb < 24 ? OFF_W2F : OFF_WOF;
          const int nks = rkb < 24 ? 48 : 20, kb = rkb < 24 ? rkb : rkb - 24;
#pragma unroll
          for (int i = 0; i < 2; ++i) {
            const int nl = 2 * lw + i, nt = 8 * rnb + nl, c5 = nt / 5, jj = nt - 5 * c5;
            const unsigned rp = lds0 + RT_OFF + (nl >> 2) * 8192 + (8 * g + q4) * 128 + (((nl & 3) ^ sigl) << 5) + p4 * 8;
#pragma unroll
            for (int ks = 0; ks < 2; ++ks) {
              const bf16x8 f = cat44(trread(rp + ks * 4096), trread(rp + ks * 4096 + 512));
              __builtin_amdgcn_raw_buffer_store_b128(__builtin_bit_cast(u32x4, f), rs, lane * 16,
                                                     dstb + ((c5 * nks + 2 * kb + ks) * 5 + jj) * 1024, SC1);
            }
          }
        }
      }
      if constexpr (s == 4 || s == 11) {
        if (lt == 0 && (s == 4 || cg < 15)) __hip_atomic_store((GAS u64*)(ws + (s == 4 ? FL_W : FL_Q) + b * 128), tag, RLX_AGENT);
      }
      if constexpr (s == 6) {
        fl = aload8(ws, fW);
#pragma unroll
        for (int k = 0; k < 5; ++k) gr[k] = aload16(ws, ro[k]);
      }
      if constexpr (s == 6 || s == 14) {
#pragma unroll
        for (int k = 0; k < 3; ++k) { if (s == 14) co[k] += 16 * 10240; gc[k] = aload16(ws, co[k]); }
      }
      if constexpr (s == 9) {
        PIN(fl);
        if (!__all(fl[0] == tg && fl[1] == (unsigned)(tag >> 32))) poll_flags(ws + fW, true, tag);
#pragma unroll
        for (int k = 0; k < 5; ++k)
#pragma unroll
          for (int j = 0; j < 5; ++j) wpre[k][j] = ld16(rs, lane * 16, wb + k * 5120 + j * 1024);
      }
      if constexpr (s == 16) fl = aload8(ws, fQ);
      if constexpr (s == 17) {
        PIN(fl);
        if (!__all(fl[0] == tg && fl[1] == (unsigned)(tag >> 32))) poll_flags(ws + fQ, true, tag);
#pragma unroll
        for (int i = 0; i < 2; ++i) {
          const int seg = 2 * lw + i;
          int tb = 4 * qb - 4 + seg; tb = tb < 0 ? 0 : tb;
          dma_kv(ws + OFF_QKV + (size_t)(tb * 240 + 80 + ch) * 1024, lane * 16, lane * 16 + 81920, lds0 + seg * 1024);
        }
      }
    };
    stage(IC<0>{}); stage(IC<1>{}); stage(IC<2>{}); stage(IC<3>{}); stage(IC<4>{});
    stage(IC<5>{}); stage(IC<6>{}); stage(IC<7>{}); stage(IC<8>{}); stage(IC<9>{});
    stage(IC<10>{}); stage(IC<11>{}); stage(IC<12>{}); stage(IC<13>{}); stage(IC<14>{});
    stage(IC<15>{}); stage(IC<16>{}); stage(IC<17>{}); stage(IC<18>{}); stage(IC<19>{});
    STAMP(2);
    const int l31 = lane & 31, hh = lane >> 5;
    u32x4 qraw[2][2];
#pragma unroll
    for (int a = 0; a < 2; ++a)
#pragma unroll
      for (int ks = 0; ks < 2; ++ks)
        qraw[a][ks] = __builtin_amdgcn_raw_buffer_load_b128(rs, (ks ? 2 : hh) * 1024 + (32 * a + l31) * 16,
                                                            OFF_QKV + ((4 * qb + lw) * 240 + ch) * 1024, SC1);
    {
      const u32x4 one4 = {0x3f803f80u, 0x3f803f80u, 0x3f803f80u, 0x3f803f80u};
      *(LAS u32x4*)(smem + V_OFF + 3 * VP + lt * 16) = one4;
      *(LAS u32x4*)(smem + V_OFF + 3 * VP + 4096 + lt * 16) = one4;
      *(LAS u32x4*)(smem + K_OFF + 3 * KP + lt * 16) = u32x4{qb == 0 ? 0xff80u : 0u, 0u, 0u, 0u};
      *(LAS u32x4*)(smem + K_OFF + 3 * KP + 4096 + lt * 16) = u32x4{0u, 0u, 0u, 0u};
    }
    bf16x8 qf[2][2];
#pragma unroll
    for (int a = 0; a < 2; ++a) {
      u32x4 v0 = qraw[a][0], v1 = qraw[a][1];
      if (odd && hh == 0) { v0[0] = 0u; v0[1] = 0u; }
      if (hh) v1 = u32x4{0x3f80u, 0u, 0u, 0u};
      else if (!odd) { v1[2] = 0u; v1[3] = 0u; }
      qf[a][0] = __builtin_bit_cast(bf16x8, v0);
      qf[a][1] = __builtin_bit_cast(bf16x8, v1);
    }
    f32x16 mF, z16;
#pragma unroll
    for (int e = 0; e < 16; ++e) {
      mF[e] = (e & 3) + 8 * (e >> 2) + 4 * hh > l31 ? 0.f : -INFINITY;
      z16[e] = 0.f;
    }
    WAIT_VM(0);
    WAIT_LGKM0();
    lds_arrive(sync);
    lds_wait(sync, 4);
    __builtin_amdgcn_s_setprio(2);
    STAMP(5);
    unsigned kp = lds0 + K_OFF + hh * KP + (64 * lw + l31) * 16;
    unsigned vp = lds0 + V_OFF + (2 * (g & 1) + (p4 >> 1)) * VP + (64 * lw + 4 * hh + q4) * 16 + (p4 & 1) * 8;
    unsigned ov = l31 * 16 + hh * 8;
    bf16x8 q0 = qf[0][0], q1 = qf[0][1];
#define LDK(T, ks) (*(LAS bf16x8*)(size_t)(kp + (T) * 512 + (ks) * 2 * KP))
#define MF32(a, b, c) __builtin_amdgcn_mfma_f32_32x32x16_bf16(a, b, c, 0, 0, 0)
#define EX2(s_, e) pk2(__builtin_amdgcn_exp2f((s_)[e]), __builtin_amdgcn_exp2f((s_)[(e) + 1]))
#pragma unroll 1
    for (int un = 0; un < 2; ++un) {
      float m0 = -INFINITY, m1 = -INFINITY;
      {
        bf16x8 kf[9][2];
#pragma unroll
        for (int T = 0; T < 3; ++T) { kf[T][0] = LDK(T, 0); kf[T][1] = LDK(T, 1); }
        f32x16 sp = z16;
#pragma unroll
        for (int T = 0; T < 10; ++T) {
          f32x16 sn = z16;
          if (T + 3 < 9) { kf[T + 3][0] = LDK(T + 3, 0); kf[T + 3][1] = LDK(T + 3, 1); }
          if (T < 9) sn = MF32(kf[T][1], q1, MF32(kf[T][0], q0, T == 0 ? mF : z16));
          if (T == 9) {
#pragma unroll
            for (int e = 0; e < 16; ++e) sp[e] = mF[e] == 0.f ? -INFINITY : sp[e];
          }
          if (T > 0) {
#pragma unroll
            for (int e = 0; e < 16; e += 4) { m0 = MAX3(m0, sp[e], sp[e + 1]); m1 = MAX3(m1, sp[e + 2], sp[e + 3]); }
          }
          SGB(0x100, 2); SGB(0x8, 1); SGB(0x2, 4); SGB(0x8, 1); SGB(0x2, 4);
          asm volatile("" :: "v"(sn), "v"(m0), "v"(m1));
          SB();
          sp = sn;
        }
      }
      float mx = fmaxf(m0, m1);
      mx = fmaxf(mx, __shfl_xor(mx, 32));
      f32x16 ca;
#pragma unroll
      for (int e = 0; e < 16; ++e) ca[e] = -mx;
      f32x16 oa = z16;
      {
        bf16x8 kc[2][2], vf[2][2];
        f32x16 sc[2];
        u32x4 pa[2] = {{0u, 0u, 0u, 0u}, {0u, 0u, 0u, 0u}};
        kc[0][0] = LDK(0, 0); kc[0][1] = LDK(0, 1); kc[1][0] = LDK(1, 0); kc[1][1] = LDK(1, 1);
        sc[0] = MF32(kc[0][1], q1, MF32(kc[0][0], q0, ca + mF));
        sc[1] = z16;
        asm volatile("" :: "v"(sc[0]));
        SB();
#pragma unroll
        for (int T = 0; T < 10; ++T) {
          const int c = T & 1, n = c ^ 1;
          if (T < 9) {
#pragma unroll
            for (int ks = 0; ks < 2; ++ks) vf[c][ks] = cat44(trread(vp + T * 512 + ks * 256), trread(vp + T * 512 + ks * 256 + 128));
          }
          if (T < 8) sc[n] = MF32(kc[n][1], q1, MF32(kc[n][0], q0, ca));
          if (T < 7) { kc[c][0] = LDK(T + 2, 0); kc[c][1] = LDK(T + 2, 1); }
          if (T > 0) oa = MF32(vf[n][1], __builtin_bit_cast(bf16x8, pa[1]), MF32(vf[n][0], __builtin_bit_cast(bf16x8, pa[0]), oa));
          if (T == 8) {
#pragma unroll
            for (int e = 0; e < 16; ++e) sc[c][e] = mF[e] == 0.f ? -INFINITY : sc[c][e];
          }
          if (T < 9) {
#pragma unroll
            for (int e = 0; e < 4; ++e) { pa[0][e] = EX2(sc[c], 2 * e); pa[1][e] = EX2(sc[c], 8 + 2 * e); }
          }
          SGB(0x100, 6);
          if (T == 8) SGB(0x2, 16);
          SGB(0x8, 1); SGB(0x400, 4); SGB(0x2, 1);
#pragma unroll
          for (int k = 0; k < 3; ++k) { SGB(0x8, 1); SGB(0x400, 2); SGB(0x2, 1); SGB(0x400, 2); SGB(0x2, 1); }
          SGB(0x2, 1);
          asm volatile("" :: "v"(sc[n]), "v"(oa), "v"(pa[0]), "v"(pa[1]));
          SB();
        }
      }
      const float il = __builtin_amdgcn_rcpf(oa[12]);
#pragma unroll
      for (int pl = 0; pl < 3; ++pl) {
        const bool ok = pl == 1 || (pl == 0 ? !(odd && hh == 0) : (odd || hh == 0));
        if (ok) {
          const u32x2 pkd = {pk2(oa[4 * pl] * il, oa[4 * pl + 1] * il), pk2(oa[4 * pl + 2] * il, oa[4 * pl + 3] * il)};
          __builtin_amdgcn_raw_buffer_store_b64(pkd, rs, pl * 1024 + ov, OFF_O + ((4 * qb + lw) * 80 + ch) * 1024, SC1);
        }
      }
      kp += 512; vp += 512; ov += 512;
      q0 = qf[1][0]; q1 = qf[1][1];
    }
    __builtin_amdgcn_s_setprio(0);
    STAMP(6);
    WAIT_VM(0);
    if (lds_arrive(sync + 1) == 3 && lane == 0) __hip_atomic_store((GAS u64*)(ws + FL_O + b * 128), tag, RLX_AGENT);
    const unsigned ab = OFF_O + (mt * 80 + 20 * lw) * 1024;
    {
      const int hd = 8 * lw + (lane & 7);
      poll_flags(ws + FL_O + (((((mt >> 2) << 2) | (hd & 3)) << 3) | (hd >> 2)) * 128, lane < 8, tag);
    }
    STAMP(7);
    bf16x8 af[3][4];
#pragma unroll
    for (int k = 0; k < 3; ++k)
#pragma unroll
      for (int i = 0; i < 4; ++i) af[k][i] = ld16(rs, va + i * 256, ab + k * 4096);
    SB();
#pragma unroll
    for (int i = 0; i < 4; ++i)
#pragma unroll
      for (int j = 0; j < 5; ++j) acc[i][j] = z4;
#pragma unroll
    for (int k = 0; k < 5; ++k) {
#pragma unroll
      for (int i = 0; i < 4; ++i)
#pragma unroll
        for (int j = 0; j < 5; ++j) acc[i][j] = mfma16(wpre[k][j], af[k % 3][i], acc[i][j]);
      SB();
      if (k < 2) {
#pragma unroll
        for (int i = 0; i < 4; ++i) af[k][i] = ld16(rs, va + i * 256, ab + (k + 3) * 4096);
        SB();
      }
    }
    STAMP(8);
    lds_wait(sync + 1, 4);
    red_write(smem + REDS_OFF, lw, lane, acc);
  } else {
    const int mw = w & 1, nw = w >> 1;
    if (idx * 4 + w < CODE_KB) {
      const u32x4 t0 = *(const u32x4*)(pc0 + (idx * 4 + w) * 1024 + lane * 16);
      asm volatile("" :: "v"(t0));
    }
    tag = load_tag();
    u32x2 b1v[3];
#pragma unroll
    for (int j = 0; j < 3; ++j) b1v[j] = *(const u32x2*)(b1 + ((6 * cg + 3 * nw + j) * 16 + 4 * g) * 2);
    unsigned xa[2];
    xa[0] = lds0 + (64 * mw + l15) * 128 + (xsw << 4);
    xa[1] = lds0 + (64 * mw + l15) * 128 + ((xsw ^ 4) << 4);
    const unsigned wl = lds0 + 16384 + (8 * g + q4) * 128 + p4 * 8;
    const unsigned vo = (g >> 1) * 1024 + (16 * (g & 1) + l15) * 16;
    float mean[4], rstd[4];
    BAR();
    STAMP(1);
    {
      unsigned wa[4];
      f32x4 acc[4][4];
#pragma unroll
      for (int j = 0; j < 4; ++j) {
        wa[j] = wl + nw * 8192 + ((j ^ sigl) << 5);
#pragma unroll
        for (int i = 0; i < 4; ++i) acc[i][j] = f32x4{0.f, 0.f, 0.f, 0.f};
      }
      mfma_part<4, 0>(xa, wa, acc, ws);
      STAMP(12);
#pragma unroll
      for (int i = 0; i < 4; ++i) {
        const f32x2 st = *(LAS f32x2*)(smem + ST_OFF + (64 * mw + 16 * i + l15) * 8);
        mean[i] = st[0]; rstd[i] = st[1];
      }
      if (cg < 15)
        store_part<4, false>(acc, mean, rstd, (LAS float*)(smem + CS_OFF) + 64 * nw, b1v,
                             cg < 5 ? 0.22360679774997896f * 1.4426950408889634f : 1.f, rs, vo,
                             OFF_QKV + ((2 * rt + mw) * 240 + 16 * cg + 8 * nw) * 1024, g);
      STAMP(13);
    }
    {
      unsigned wa[3];
      f32x4 acc[4][3];
#pragma unroll
      for (int j = 0; j < 3; ++j) {
        const int lu = lu1 + 3 * nw + j;
        wa[j] = wl + (lu >> 2) * 8192 + (((lu & 3) ^ sigl) << 5);
#pragma unroll
        for (int i = 0; i < 4; ++i) acc[i][j] = f32x4{0.f, 0.f, 0.f, 0.f};
      }
      mfma_part<3, 10>(xa, wa, acc, ws);
      STAMP(2);
      store_part<3, true>(acc, mean, rstd, (LAS float*)(smem + CS_OFF + 512) + 48 * nw, b1v, 1.f, rs, vo,
                          OFF_H + ((2 * rt + mw) * 192 + 12 * cg + 6 * nw) * 1024, g);
    }
    STAMP(3);
#pragma unroll
    for (int k = 0; k < WPRE; ++k)
#pragma unroll
      for (int j = 0; j < 5; ++j) wpre[k][j] = ld16(rs, lane * 16, wb + k * 5120 + j * 1024);
    wait_vm<5 * WPRE>();
    if (lds_arrive(sync + 2) == 3 && lane == 0) __hip_atomic_store((GAS u64*)(ws + FL_H + b * 128), tag, RLX_AGENT);
    STAMP(4);
    const unsigned ab = OFF_H + (mt * 192 + 48 * w) * 1024;
    poll_flags(ws + FL_H + wg_of(mt >> 1, 4 * w + (lane & 3)) * 128, lane < 4, tag);
    STAMP(7);
    bf16x8 af[3][4];
#pragma unroll
    for (int k = 0; k < 3; ++k)
#pragma unroll
      for (int i = 0; i < 4; ++i) af[k][i] = ld16(rs, va + i * 256, ab + k * 4096);
    SB();
#pragma unroll
    for (int i = 0; i < 4; ++i)
#pragma unroll
      for (int j = 0; j < 5; ++j) acc[i][j] = f32x4{0.f, 0.f, 0.f, 0.f};
#pragma unroll
    for (int k = 0; k < 12; ++k) {
#pragma unroll
      for (int i = 0; i < 4; ++i)
#pragma unroll
        for (int j = 0; j < 5; ++j) acc[i][j] = mfma16(wpre[k % WPRE][j], af[k % 3][i], acc[i][j]);
      SB();
      if (k + 3 < 12) {
#pragma unroll
        for (int i = 0; i < 4; ++i) af[k % 3][i] = ld16(rs, va + i * 256, ab + (k + 3) * 4096);
      }
      if (k + WPRE < 12) {
#pragma unroll
        for (int j = 0; j < 5; ++j) wpre[k % WPRE][j] = ld16(rs, lane * 16, wb + (k + WPRE) * 5120 + j * 1024);
      }
      SB();
    }
    STAMP(8);
    red_write(smem + REDG_OFF, w, lane, acc);
  }
  const int ow = w & 3;
  const size_t orow = (size_t)(64 * mt + 16 * ow + l15) * 1280 + (80 * ct + 4 * g) * 2;
  u32x2 xv[5], bb[5];
  if (w < 4) {
#pragma unroll
    for (int j = 0; j < 5; ++j) {
      xv[j] = *(const u32x2*)(x + orow + j * 32);
      bb[j] = *(const u32x2*)(b2 + (80 * ct + 16 * j + 4 * g) * 2);
    }
  }
  __syncthreads();
  STAMP(9);
  f32x4 o[5];
  red_sum(smem + (w < 4 ? REDG_OFF : REDS_OFF), ow, lane, acc, o);
  if (w >= 4) {
#pragma unroll
    for (int j = 0; j < 5; ++j) *(LAS f32x4*)(smem + SX_OFF + (ow * 5 + j) * 1024 + lane * 16) = o[j];
  }
  __syncthreads();
  if (w < 4) {
#pragma unroll
    for (int j = 0; j < 5; ++j) {
      const f32x4 sv = *(LAS f32x4*)(smem + SX_OFF + (ow * 5 + j) * 1024 + lane * 16);
      const float bv[4] = {bflo(bb[j][0]), bfhi(bb[j][0]), bflo(bb[j][1]), bfhi(bb[j][1])};
      const float xr[4] = {bflo(xv[j][0]), bfhi(xv[j][0]), bflo(xv[j][1]), bfhi(xv[j][1])};
      float r[4];
#pragma unroll
      for (int e = 0; e < 4; ++e) {
        const float t = o[j][e] + bv[e];
        r[e] = xr[e] + sv[e] * (t * __builtin_amdgcn_rcpf(1.f + __expf(-t)));
      }
      *(u32x2*)(out + orow + j * 32) = u32x2{pk2(r[0], r[1]), pk2(r[2], r[3])};
    }
  } else if (b == 0 && w == 4) {
#pragma unroll
    for (int i = 0; i < 4; ++i) poll_flags(ws + FL_W + (lane + 64 * i) * 128, true, tag);
    if (lane == 0) __hip_atomic_store((GAS u64*)(ws + OFF_FL), tag, RLX_AGENT);
  }
  STAMP(10);
}

extern "C" void kernel_launch(void* const* d_in, const int* in_sizes, int n_in, void* d_out, int out_size, void* d_ws,
                              size_t ws_size, hipStream_t stream) {
  (void)hipFuncSetAttribute((const void*)k_all, hipFuncAttributeMaxDynamicSharedMemorySize, LDS_ALL);
  k_all<<<256, 512, LDS_ALL, stream>>>((const char*)d_in[0], (const char*)d_in[1], (const char*)d_in[4], (const char*)d_in[5],
                                       (const char*)d_in[6], (const char*)d_in[2], (const char*)d_in[7], (char*)d_ws, (char*)d_out);
}
```

```cpp
#include <hip/hip_runtime.h>
#include <stdint.h>

#define LAS __attribute__((address_space(3)))
#define GAS __attribute__((address_space(1)))
#define CAS __attribute__((address_space(4)))
typedef short bf16x8 __attribute__((ext_vector_type(8)));
typedef short s16x4 __attribute__((ext_vector_type(4)));
typedef float f32x4 __attribute__((ext_vector_type(4)));
typedef unsigned u32x4 __attribute__((ext_vector_type(4)));
typedef unsigned u32x2 __attribute__((ext_vector_type(2)));
typedef __bf16 bf16x2_t __attribute__((ext_vector_type(2)));
typedef float f32x2 __attribute__((ext_vector_type(2)));
typedef float f32x16 __attribute__((ext_vector_type(16)));
typedef unsigned long long u64;

constexpr unsigned OFF_FL = 0;
constexpr unsigned FL_W = 1024, FL_A = FL_W + 32768, FL_O = FL_A + 32768;
constexpr unsigned OFF_RS = 131072;
constexpr unsigned OFF_CS = OFF_RS + 16 * 10 * 2048;
constexpr unsigned OFF_QKV = OFF_CS + 16 * 10 * 2048;
constexpr unsigned OFF_H = OFF_QKV + 32 * 240 * 1024;
constexpr unsigned OFF_O = OFF_H + 32 * 192 * 1024;
constexpr unsigned OFF_W2F = OFF_O + 32 * 80 * 1024;
constexpr unsigned OFF_WOF = OFF_W2F + 8 * 48 * 5120;
constexpr unsigned WS_USED = OFF_WOF + 8 * 20 * 5120;

__device__ __forceinline__ f32x4 mfma16(bf16x8 a, bf16x8 b, f32x4 c) {
  return __builtin_amdgcn_mfma_f32_16x16x32_bf16(a, b, c, 0, 0, 0);
}
__device__ __forceinline__ unsigned pk2(float a, float b) {
  f32x2 v = {a, b};
  bf16x2_t r = __builtin_convertvector(v, bf16x2_t);
  return __builtin_bit_cast(unsigned, r);
}
__device__ __forceinline__ float bflo(unsigned u) { return __builtin_bit_cast(float, u << 16); }
__device__ __forceinline__ float bfhi(unsigned u) { return __builtin_bit_cast(float, u & 0xffff0000u); }
__device__ __forceinline__ s16x4 trread(unsigned a) {
  return __builtin_amdgcn_ds_read_tr16_b64_v4i16((LAS s16x4*)(size_t)a);
}
__device__ __forceinline__ bf16x8 cat44(s16x4 a, s16x4 b) {
  return __builtin_shufflevector(a, b, 0, 1, 2, 3, 4, 5, 6, 7);
}
#define WAIT_VM(n) asm volatile("s_waitcnt vmcnt(" #n ")" ::: "memory")
#define WAIT_LGKM0() __builtin_amdgcn_s_waitcnt(0xC07F)
#define BAR() __builtin_amdgcn_s_barrier()
#define SB() __builtin_amdgcn_sched_barrier(0)
#define SGB(m, n) __builtin_amdgcn_sched_group_barrier(m, n, 0)
#define MAX3(a, b, c) __builtin_fmaxf(__builtin_fmaxf(a, b), c)
#define RLX_AGENT __ATOMIC_RELAXED, __HIP_MEMORY_SCOPE_AGENT
constexpr int SC1 = 16;
constexpr int CODE_KB = 46;
template <int N> struct IC { static constexpr int value = N; };

#define STAMP(i)
#define CLK(v)

__device__ __forceinline__ void dma1(const char* sbase, unsigned voff, unsigned lds) {
  unsigned keep;
  asm volatile("s_mov_b32 %0, m0\n\ts_mov_b32 m0, %3\n\ts_nop 0\n\tglobal_load_lds_dwordx4 %1, %2\n\ts_mov_b32 m0, %0"
               : "=&s"(keep) : "v"(voff), "s"(sbase), "s"(lds) : "memory");
}
#define DMA_W2(ofs) "s_add_u32 m0, m0, 7040\n\ts_nop 0\n\tglobal_load_lds_dwordx4 %8, %7 offset:" #ofs "\n\t" \
                    "s_add_u32 m0, m0, 1024\n\ts_nop 0\n\tglobal_load_lds_dwordx4 %9, %7 offset:" #ofs "\n\t"
#define DMA_HEAD "s_mov_b32 %0, m0\n\ts_mov_b32 m0, %10\n\ts_nop 0\n\tglobal_load_lds_dwordx4 %8, %7\n\t" \
                 "s_add_u32 m0, m0, 1024\n\ts_nop 0\n\tglobal_load_lds_dwordx4 %9, %7\n\t" DMA_W2(128) DMA_W2(256)
#define DMA_X "s_mov_b32 m0, %6\n\ts_nop 0\n\tglobal_load_lds_dwordx4 %2, %1\n\t" \
              "s_add_u32 m0, m0, 1024\n\ts_nop 0\n\tglobal_load_lds_dwordx4 %3, %1\n\t" \
              "s_add_u32 m0, m0, 1024\n\ts_nop 0\n\tglobal_load_lds_dwordx4 %4, %1\n\t" \
              "s_add_u32 m0, m0, 1024\n\ts_nop 0\n\tglobal_load_lds_dwordx4 %5, %1\n\ts_mov_b32 m0, %0"
#define DMA_OPS : "=&s"(keep) : "s"(sa), "v"(va0), "v"(va1), "v"(va2), "v"(va3), "s"(la), "s"(sb), "v"(vb0), "v"(vb1), "s"(lb) : "memory", "scc"
template <int NP>
__device__ __forceinline__ void dma_stage(const char* sa, unsigned va0, unsigned va1, unsigned va2, unsigned va3, unsigned la,
                                          const char* sb, unsigned vb0, unsigned vb1, unsigned lb) {
  unsigned keep;
  if constexpr (NP == 3) asm volatile(DMA_HEAD DMA_X DMA_OPS);
  else asm volatile(DMA_HEAD DMA_W2(384) DMA_X DMA_OPS);
}
__device__ __forceinline__ void dma_kv(const char* sb, unsigned vk, unsigned vv, unsigned l) {
  unsigned keep;
  asm volatile("s_mov_b32 %0, m0\n\ts_mov_b32 m0, %4\n\ts_nop 0\n\tglobal_load_lds_dwordx4 %2, %1 sc1\n\t"
               "s_add_u32 m0, m0, 7168\n\ts_nop 0\n\tglobal_load_lds_dwordx4 %2, %1 offset:1024 sc1\n\t"
               "s_add_u32 m0, m0, 7168\n\ts_nop 0\n\tglobal_load_lds_dwordx4 %2, %1 offset:2048 sc1\n\t"
               "s_add_u32 m0, m0, 18432\n\ts_nop 0\n\tglobal_load_lds_dwordx4 %3, %1 sc1\n\t"
               "s_add_u32 m0, m0, 7232\n\ts_nop 0\n\tglobal_load_lds_dwordx4 %3, %1 offset:1024 sc1\n\t"
               "s_add_u32 m0, m0, 7232\n\ts_nop 0\n\tglobal_load_lds_dwordx4 %3, %1 offset:2048 sc1\n\t"
               "s_mov_b32 m0, %0"
               : "=&s"(keep) : "s"(sb), "v"(vk), "v"(vv), "s"(l) : "memory", "scc");
}
__device__ __forceinline__ void dma5_sc1(const char* sb, unsigned v, unsigned l) {
  unsigned keep;
  asm volatile("s_mov_b32 %0, m0\n\ts_mov_b32 m0, %3\n\ts_nop 0\n\tglobal_load_lds_dwordx4 %2, %1 sc1\n\t"
               "global_load_lds_dwordx4 %2, %1 offset:1024 sc1\n\tglobal_load_lds_dwordx4 %2, %1 offset:2048 sc1\n\t"
               "global_load_lds_dwordx4 %2, %1 offset:3072 sc1\n\ts_add_u32 m0, m0, 1024\n\ts_nop 0\n\t"
               "global_load_lds_dwordx4 %4, %1 offset:3072 sc1\n\ts_mov_b32 m0, %0"
               : "=&s"(keep) : "s"(sb), "v"(v), "s"(l), "v"(v + 1024) : "memory", "scc");
}
__device__ __forceinline__ u32x2 aload8(const char* sbase, unsigned voff) {
  u32x2 r;
  asm volatile("global_load_dwordx2 %0, %1, %2 sc1" : "=v"(r) : "v"(voff), "s"(sbase) : "memory");
  return r;
}
__device__ __forceinline__ u32x4 aload16(const char* sbase, unsigned voff) {
  u32x4 r;
  asm volatile("global_load_dwordx4 %0, %1, %2 sc1" : "=v"(r) : "v"(voff), "s"(sbase) : "memory");
  return r;
}
#define PIN(x) asm volatile("" : "+v"(x))
template <int N> __device__ __forceinline__ void wait_vm() { asm volatile("s_waitcnt vmcnt(%0)" :: "i"(N) : "memory"); }
__device__ __forceinline__ void poll_flags(const char* flag, bool need, u64 tag) {
#pragma unroll 1
  for (int spins = 0; spins < (1 << 20); ++spins) {
    const u64 v = need ? __hip_atomic_load((const GAS u64*)flag, RLX_AGENT) : tag;
    if (__all(v == tag)) break;
    __builtin_amdgcn_s_sleep(1);
  }
  __builtin_amdgcn_fence(__ATOMIC_ACQUIRE, "wavefront");
}
__device__ __forceinline__ unsigned lds_arrive(LAS unsigned* c) {
  unsigned r = 0;
  if ((threadIdx.x & 63) == 0) r = __hip_atomic_fetch_add(c, 1u, __ATOMIC_RELAXED, __HIP_MEMORY_SCOPE_WORKGROUP);
  return __builtin_amdgcn_readfirstlane(r);
}
__device__ __forceinline__ void lds_wait(LAS unsigned* c, unsigned n) {
#pragma unroll 1
  for (int spins = 0; spins < (1 << 22); ++spins) {
    if (__builtin_amdgcn_readfirstlane(__hip_atomic_load(c, __ATOMIC_RELAXED, __HIP_MEMORY_SCOPE_WORKGROUP)) >= n) break;
  }
  __builtin_amdgcn_fence(__ATOMIC_ACQUIRE, "wavefront");
}

#define WOPRE 4
constexpr int STG = 49152, RT_OFF = 3 * STG, LDS_ALL = 163840;
constexpr int ST_OFF = 160768;
constexpr int CS_OFF = ST_OFF + 1024, SYNC_OFF = ST_OFF + 2048;
constexpr int B1_OFF = 157696;
constexpr int KP = 8192, VP = 8192 + 64;
constexpr int K_OFF = 0, V_OFF = 4 * KP, SLAB_OFF = 62464;
constexpr int REDG_OFF = SLAB_OFF, GX_OFF = REDG_OFF + 61440, REDS1_OFF = 0, REDS2_OFF = GX_OFF + 8192;

__device__ __forceinline__ void st16(u32x4 v, __amdgpu_buffer_rsrc_t rs, unsigned voff, unsigned soff) {
  __builtin_amdgcn_raw_buffer_store_b128(v, rs, voff + soff, 0, SC1);
}
__device__ __forceinline__ bf16x8 ld16(__amdgpu_buffer_rsrc_t rs, unsigned voff, unsigned soff) {
  return __builtin_bit_cast(bf16x8, __builtin_amdgcn_raw_buffer_load_b128(rs, voff, soff, SC1));
}
__device__ __forceinline__ void red_write(LAS char* red, int kw, int lane, const f32x4 (&acc)[4][5]) {
#pragma unroll
  for (int i = 0; i < 4; ++i) {
    if (i != kw) {
      const int slot = kw - (kw > i ? 1 : 0);
#pragma unroll
      for (int j = 0; j < 5; ++j) *(LAS f32x4*)(red + ((i * 3 + slot) * 5 + j) * 1024 + lane * 16) = acc[i][j];
    }
  }
}
__device__ __forceinline__ void red_sum(LAS char* red, int kw, int lane, const f32x4 (&acc)[4][5], f32x4 (&o)[5]) {
#pragma unroll
  for (int j = 0; j < 5; ++j) {
    f32x4 a = kw == 0 ? acc[0][j] : (kw == 1 ? acc[1][j] : (kw == 2 ? acc[2][j] : acc[3][j]));
#pragma unroll
    for (int s = 0; s < 3; ++s) a += *(LAS f32x4*)(red + ((kw * 3 + s) * 5 + j) * 1024 + lane * 16);
    o[j] = a;
  }
}
__device__ __forceinline__ int wg_of(int rt, int cg) { return (((cg & 3) * 8 + (rt & 7)) << 3) | ((cg >> 2) << 1) | (rt >> 3); }
template <int N>
__device__ __forceinline__ bool sum_pairs(const u32x4 (&v)[N], unsigned last_on, unsigned tg, float& a, float& b) {
  bool ok = true;
  a = 0.f; b = 0.f;
#pragma unroll
  for (int k = 0; k < N; ++k) {
    ok = ok && v[k][1] == tg && v[k][3] == tg;
    const unsigned m = k == N - 1 ? last_on : 0xffffffffu;
    a += __uint_as_float(v[k][0] & m);
    b += __uint_as_float(v[k][2] & m);
  }
  return __all(ok);
}
template <int N>
__device__ __forceinline__ void regather(__amdgpu_buffer_rsrc_t rs, const unsigned (&vo)[N], unsigned last_on, unsigned tg, float& a, float& b) {
#pragma unroll 1
  for (int spins = 0; spins < (1 << 20); ++spins) {
    u32x4 v[N];
#pragma unroll
    for (int k = 0; k < N; ++k) v[k] = __builtin_amdgcn_raw_buffer_load_b128(rs, vo[k], 0, SC1);
    if (sum_pairs<N>(v, last_on, tg, a, b)) break;
    __builtin_amdgcn_s_sleep(2);
  }
}

#define STG_STAMP(s_)

struct Ctx {
  const char *x, *W, *b1, *w2, *wo;
  char* ws;
  unsigned lds0;
  int b, rt, cg, rot, ct, qb, ch, w, lane;
};

template <int NP>
__device__ __forceinline__ void mfma_phase(const Ctx& c, __amdgpu_buffer_rsrc_t rs) {
  constexpr int NT = 2 * NP;
  constexpr bool ISH = NP == 4;
  char* ws = c.ws;
  const int lane = c.lane, g = lane >> 4, l15 = lane & 15, q4 = l15 >> 2, p4 = lane & 3;
  const int mw = c.w & 1, nw = c.w >> 1;
  const int sigl = ((q4 >> 1) & 1) | ((g & 1) << 1), xsw = g ^ (l15 >> 1);
  unsigned xa[2], wa[NT];
  xa[0] = c.lds0 + (64 * mw + l15) * 128 + (xsw << 4);
  xa[1] = c.lds0 + (64 * mw + l15) * 128 + ((xsw ^ 4) << 4);
  f32x4 acc[4][NT];
#pragma unroll
  for (int j = 0; j < NT; ++j) {
    const int tl = NT * nw + j;
    wa[j] = c.lds0 + 16384 + (tl >> 2) * 8192 + (8 * g + q4) * 128 + (((tl & 3) ^ sigl) << 5) + p4 * 8;
#pragma unroll
    for (int i = 0; i < 4; ++i) acc[i][j] = f32x4{0.f, 0.f, 0.f, 0.f};
  }
  bf16x8 xf[2][4], wf[2][NT];
#define LDX(set, i, ks) xf[set][i] = *(LAS bf16x8*)(size_t)(xa[ks] + (i) * 2048)
#define LDW(set, j, ks) wf[set][j] = cat44(trread(wa[j] + (ks) * 4096), trread(wa[j] + (ks) * 4096 + 512))
#define HALF(set, ks) do { \
    _Pragma("unroll") for (int j = 0; j < NT; ++j) { \
      if (j == 0) { _Pragma("unroll") for (int i = 0; i < 4; ++i) LDX((set) ^ 1, i, ks); } \
      else if (2 * j - 2 < NT) { LDW((set) ^ 1, 2 * j - 2, ks); LDW((set) ^ 1, 2 * j - 1, ks); } \
      _Pragma("unroll") for (int i = 0; i < 4; ++i) acc[i][j] = mfma16(wf[set][j], xf[set][i], acc[i][j]); \
      SGB(0x8, 1); SGB(0x100, 1); SGB(0x8, 1); SGB(0x100, 1); SGB(0x8, 1); SGB(0x100, 1); SGB(0x8, 1); SGB(0x100, 1); \
      asm volatile("" :: "v"(acc[0][j]), "v"(acc[1][j]), "v"(acc[2][j]), "v"(acc[3][j])); \
      SB(); \
    } } while (0)
  BAR();
  STAMP(1);
#pragma unroll
  for (int i = 0; i < 4; ++i) LDX(0, i, 0);
#pragma unroll
  for (int j = 0; j < NT; ++j) LDW(0, j, 0);
  WAIT_LGKM0();
  int buf = 0;
#pragma unroll 1
  for (int t = 0; t < 10; ++t) {
    HALF(0, 1);
    {
      const int d = buf == 2 ? -2 * STG : STG;
      buf = buf == 2 ? 0 : buf + 1;
      xa[0] += d; xa[1] += d;
#pragma unroll
      for (int j = 0; j < NT; ++j) wa[j] += d;
    }
    WAIT_LGKM0();
    BAR();
    STG_STAMP(t);
    SB();
    HALF(1, 0);
    WAIT_LGKM0();
  }
  STAMP(2);
  float mean[4], rstd[4];
#pragma unroll
  for (int i = 0; i < 4; ++i) {
    const f32x2 st = *(LAS f32x2*)(size_t)(c.lds0 + ST_OFF + (64 * mw + 16 * i + l15) * 8);
    mean[i] = st[0]; rstd[i] = st[1];
  }
  const int u0 = ISH ? 16 * (c.cg - 10) + NT * nw : 12 * c.cg + NT * nw;
  const unsigned so = (ISH ? OFF_H + (2 * c.rt + mw) * 192 * 1024 : OFF_QKV + (2 * c.rt + mw) * 240 * 1024) + 2 * u0 * 1024;
  const unsigned vo = (g >> 1) * 1024 + (16 * (g & 1) + l15) * 16;
#pragma unroll
  for (int j = 0; j < NT; ++j) {
    const f32x4 c4 = *(LAS f32x4*)(size_t)(c.lds0 + CS_OFF + ((NT * nw + j) * 16 + 4 * g) * 4);
    f32x4 bv = {0.f, 0.f, 0.f, 0.f};
    if (ISH) {
      const u32x2 bb = *(LAS u32x2*)(size_t)(c.lds0 + B1_OFF + ((NT * nw + j) * 16 + 4 * g) * 2);
      bv = f32x4{bflo(bb[0]), bfhi(bb[0]), bflo(bb[1]), bfhi(bb[1])};
    }
    const float qs = u0 + j < 40 ? 0.22360679774997896f * 1.4426950408889634f : 1.f;
#pragma unroll
    for (int pp = 0; pp < 2; ++pp) {
      float v[2][4];
#pragma unroll
      for (int i = 0; i < 2; ++i)
#pragma unroll
        for (int r = 0; r < 4; ++r) {
          float t = rstd[2 * pp + i] * (acc[2 * pp + i][j][r] - mean[2 * pp + i] * c4[r]);
          if (ISH) { t += bv[r]; t = t > 0.f ? t : 0.f; t = t * t; } else t *= qs;
          v[i][r] = t;
        }
      const unsigned x0 = pk2(v[0][0], v[0][1]), x1 = pk2(v[0][2], v[0][3]);
      const unsigned y0 = pk2(v[1][0], v[1][1]), y1 = pk2(v[1][2], v[1][3]);
      const auto s0 = __builtin_amdgcn_permlane16_swap(x0, y0, false, false);
      const auto s1 = __builtin_amdgcn_permlane16_swap(x1, y1, false, false);
      const u32x4 o = {s0[0], s1[0], s0[1], s1[1]};
      st16(o, rs, vo + pp * 512, so + 2 * j * 1024);
    }
  }
}

template <int NP>
__device__ __forceinline__ void load_phase(const Ctx& c, __amdgpu_buffer_rsrc_t rs, u64& tag, bf16x8 (&wpre)[5][5], unsigned wb) {
  constexpr int D = 4 + 2 * NP, LDW_ = NP == 4 ? 3072 : 3840;
  char* ws = c.ws;
  const unsigned lds0 = c.lds0;
  const int lane = c.lane, g = lane >> 4, l15 = lane & 15, q4 = l15 >> 2, p4 = lane & 3;
  const int lw = c.w - 4, lr = lane >> 3, ls = lane & 7, lt = lw * 64 + lane;
  const int bk = 16 * lw + lr;
  const int bs0 = (bk >> 1) & 1, bs1 = bs0 ^ 2;
  const int col0 = NP == 4 ? (c.cg - 10) * 512 : c.cg * 384;
  const unsigned vb0 = bk * LDW_ + col0 + ((ls ^ (bs0 << 1)) << 4), vb1 = (bk + 8) * LDW_ + col0 + ((ls ^ (bs1 << 1)) << 4);
  const unsigned vA = (c.rt * 128 + 32 * lw + lr) * 1280 + ((ls ^ (lr >> 1)) << 4);
  int ktn = c.rot;
  auto issue = [&](auto S) {
    constexpr int s = decltype(S)::value;
    const unsigned dst = lds0 + (s % 3) * STG;
    dma_stage<NP>(c.x + ktn * 128, vA, (vA + 10240) ^ 64u, vA + 20480, (vA + 30720) ^ 64u, dst + lw * 4096,
                  c.W + (size_t)ktn * (64 * LDW_), vb0, vb1, dst + 16384 + lw * 2048);
    ktn = ktn == 9 ? 0 : ktn + 1;
  };
  issue(IC<0>{});
  const bool do_rt = c.b < 170;
  const int rkb = c.b / 5, rnb = c.b - 5 * rkb;
  if (do_rt) {
    const char* Wr = rkb < 24 ? c.w2 + (size_t)rkb * 64 * 1280 : c.wo + (size_t)(rkb - 24) * 64 * 1280;
    const unsigned r0 = bk * 1280 + rnb * 256 + ((ls ^ (bs0 << 1)) << 4), r1 = (bk + 8) * 1280 + rnb * 256 + ((ls ^ (bs1 << 1)) << 4);
    dma1(Wr, r0, lds0 + RT_OFF + lw * 2048);
    dma1(Wr, r1, lds0 + RT_OFF + lw * 2048 + 1024);
    dma1(Wr + 128, r0, lds0 + RT_OFF + 8192 + lw * 2048);
    dma1(Wr + 128, r1, lds0 + RT_OFF + 8192 + lw * 2048 + 1024);
  }
  issue(IC<1>{}); issue(IC<2>{});
  {
    const char* p = ws;
    asm volatile("" : "+s"(p));
    tag = *(const CAS u64*)(p + OFF_FL) + 1;
  }
  const unsigned tg = (unsigned)tag;
  wait_vm<2 * D>();
  BAR();
  STAMP(1);
  const int sigl = ((q4 >> 1) & 1) | ((g & 1) << 1), xsw = g ^ (l15 >> 1);
  const bf16x8 ONE8 = {0x3f80, 0x3f80, 0x3f80, 0x3f80, 0x3f80, 0x3f80, 0x3f80, 0x3f80};
  const f32x4 z4 = {0.f, 0.f, 0.f, 0.f};
  bf16x8 sw_[NP][2], sx_[2][2];
  u32x4 gr[5], gc[5];
  u32x2 fl;
  const int fkb = lane < 12 ? 6 * lw + (lane >> 1) : 24 + ((5 * lw) >> 1) + (lane < 18 ? (lane - 12) >> 1 : 0);
  const unsigned fW = FL_W + (fkb * 5 + ((5 * c.ct + (lane & 1) * 4) >> 3)) * 128;
  unsigned ro[5], co[5];
#pragma unroll
  for (int k = 0; k < 5; ++k) {
    ro[k] = OFF_RS + (c.rt * 10 + 2 * k + (lane >> 5)) * 2048 + (32 * lw + (lane & 31)) * 16;
    co[k] = OFF_CS + (c.cg * 10 + 2 * k + (lane >> 5)) * 2048 + (32 * (lw < NP ? lw : 0) + (lane & 31)) * 16;
  }
  auto stage = [&](auto S) {
    constexpr int s = decltype(S)::value;
    if constexpr (s == 0) {
#pragma unroll
      for (int i = 0; i < NP; ++i) {
        const int tl = NP * lw + i;
        const unsigned wq = lds0 + 16384 + (tl >> 2) * 8192 + (8 * g + q4) * 128 + (((tl & 3) ^ sigl) << 5) + p4 * 8;
        sw_[i][0] = cat44(trread(wq), trread(wq + 512));
        sw_[i][1] = cat44(trread(wq + 4096), trread(wq + 4608));
      }
#pragma unroll
      for (int i = 0; i < 2; ++i) {
        const unsigned xq = lds0 + (32 * lw + 16 * i + l15) * 128;
        sx_[i][0] = *(LAS bf16x8*)(size_t)(xq + (xsw << 4));
        sx_[i][1] = *(LAS bf16x8*)(size_t)(xq + ((xsw ^ 4) << 4));
      }
    }
    if constexpr (s < 9) wait_vm<s == 5 ? D + 10 : (s == 6 ? D + 11 : (s == 8 ? 5 * WOPRE : D))>();
    if constexpr (s == 7) {
#pragma unroll
      for (int k = 0; k < 5; ++k) { PIN(gr[k]); PIN(gc[k]); }
      float sx, sxx, c0, c1;
      if (!sum_pairs<5>(gr, 0xffffffffu, tg, sx, sxx)) regather<5>(rs, ro, 0xffffffffu, tg, sx, sxx);
      if (!sum_pairs<5>(gc, 0xffffffffu, tg, c0, c1)) regather<5>(rs, co, 0xffffffffu, tg, c0, c1);
      sx += __shfl_xor(sx, 32); sxx += __shfl_xor(sxx, 32);
      c0 += __shfl_xor(c0, 32); c1 += __shfl_xor(c1, 32);
      const float mean = sx * (1.f / 640.f);
      float var = sxx * (1.f / 640.f) - mean * mean;
      var = var < 0.f ? 0.f : var;
      if (lane < 32) {
        *(LAS f32x2*)(size_t)(lds0 + ST_OFF + (32 * lw + lane) * 8) = f32x2{mean, rsqrtf(var + 1e-5f)};
        *(LAS f32x2*)(size_t)(lds0 + CS_OFF + (32 * lw + lane) * 8) = f32x2{c0, c1};
      }
    }
    WAIT_LGKM0();
    BAR();
    if constexpr (s < 7) issue(IC<s + 3>{});
    if constexpr (s == 0) {
      u32x4 cu = {0u, 0u, 0u, 0u};
#pragma unroll
      for (int i = 0; i < NP; ++i) {
        const u32x4 ci = __builtin_bit_cast(u32x4, mfma16(sw_[i][1], ONE8, mfma16(sw_[i][0], ONE8, z4)));
        if (l15 == i) cu = ci;
      }
      if (l15 < NP) {
        const unsigned o = OFF_CS + (c.cg * 10 + c.rot) * 2048 + NP * lw * 128;
        st16(u32x4{cu[0], tg, cu[1], tg}, rs, l15 * 128 + g * 32, o);
        st16(u32x4{cu[2], tg, cu[3], tg}, rs, l15 * 128 + g * 32 + 16, o);
      }
#pragma unroll
      for (int i = 0; i < 2; ++i) {
        const f32x4 sx = mfma16(ONE8, sx_[i][1], mfma16(ONE8, sx_[i][0], z4));
        const f32x4 sq = mfma16(sx_[i][1], sx_[i][1], mfma16(sx_[i][0], sx_[i][0], z4));
        const int r = l15 & 3;
        const float sxx = r == 0 ? sq[0] : (r == 1 ? sq[1] : (r == 2 ? sq[2] : sq[3]));
        if (q4 == g)
          st16(u32x4{__float_as_uint(sx[0]), tg, __float_as_uint(sxx), tg}, rs,
                                                 (32 * lw + 16 * i + l15) * 16, OFF_RS + (c.rt * 10 + c.rot) * 2048);
      }
    }
    if constexpr (s == 1) {
      if (do_rt) {
        const unsigned dstb = rkb < 24 ? OFF_W2F : OFF_WOF;
        const int nks = rkb < 24 ? 48 : 20, kb = rkb < 24 ? rkb : rkb - 24;
#pragma unroll
        for (int i = 0; i < 2; ++i) {
          const int nl = 2 * lw + i, nt = 8 * rnb + nl, c5 = nt / 5, jj = nt - 5 * c5;
          const unsigned rp = lds0 + RT_OFF + (nl >> 2) * 8192 + (8 * g + q4) * 128 + (((nl & 3) ^ sigl) << 5) + p4 * 8;
#pragma unroll
          for (int ks = 0; ks < 2; ++ks) {
            const bf16x8 f = cat44(trread(rp + ks * 4096), trread(rp + ks * 4096 + 512));
            st16(__builtin_bit_cast(u32x4, f), rs, lane * 16,
                                                   dstb + ((c5 * nks + 2 * kb + ks) * 5 + jj) * 1024);
          }
        }
      }
    }
    if constexpr (s == 2) {
      if (lt < 8) *(LAS unsigned*)(size_t)(lds0 + SYNC_OFF + lt * 4) = 0u;
      if (NP == 4 && lt < 32) dma1(c.b1 + (c.cg - 10) * 512, lane * 16, lds0 + B1_OFF);
    }
    if constexpr (s == 3) {
      if (lt == 0) __hip_atomic_store((GAS u64*)(ws + FL_W + c.b * 128), tag, RLX_AGENT);
    }
    if constexpr (s == 4) {
#pragma unroll
      for (int k = 0; k < 5; ++k) { gr[k] = aload16(ws, ro[k]); gc[k] = aload16(ws, co[k]); }
    }
    if constexpr (s == 5) fl = aload8(ws, fW);
    if constexpr (s == 7) {
      PIN(fl);
      if (!__all(fl[0] == tg && fl[1] == (unsigned)(tag >> 32))) poll_flags(ws + fW, true, tag);
#pragma unroll
      for (int k = 0; k < WOPRE; ++k)
#pragma unroll
        for (int j = 0; j < 5; ++j) wpre[k][j] = ld16(rs, lane * 16, wb + k * 5120 + j * 1024);
    }
    if constexpr (s == 8) {
#pragma unroll
      for (int k = 0; k < 4; ++k)
        dma5_sc1(ws + OFF_W2F + (c.ct * 48 + 12 * lw + 4 + k) * 5120, lane * 16, lds0 + SLAB_OFF + lw * 24576 + k * 5120);
    }
  };
  stage(IC<0>{}); stage(IC<1>{}); stage(IC<2>{}); stage(IC<3>{}); stage(IC<4>{});
  stage(IC<5>{}); stage(IC<6>{}); stage(IC<7>{}); stage(IC<8>{}); stage(IC<9>{});
}

__global__ __launch_bounds__(512, 2) void k_all(const char* __restrict__ x, const char* __restrict__ wqkv, const char* __restrict__ w1,
                                                const char* __restrict__ b1, const char* __restrict__ w2, const char* __restrict__ wo,
                                                const char* __restrict__ b2, char* ws, char* __restrict__ out) {
  extern __shared__ __attribute__((aligned(16))) char smem_[];
  LAS char* smem = (LAS char*)smem_;
  const int tid = threadIdx.x, lane = tid & 63, w = __builtin_amdgcn_readfirstlane(tid >> 6);
  const int b = blockIdx.x, xcd = b & 7, idx = b >> 3;
  const int g = lane >> 4, l15 = lane & 15, q4 = l15 >> 2, p4 = lane & 3;
  const unsigned lds0 = (unsigned)(size_t)smem;
  asm volatile("" :: "s"(x), "s"(wqkv), "s"(w1), "s"(b1), "s"(w2), "s"(wo), "s"(ws));
  u64 pc_;
  asm volatile("s_getpc_b64 %0" : "=s"(pc_));
  const char* pc0 = (const char*)(pc_ & ~(u64)63);
  u64 tag;
  const __amdgpu_buffer_rsrc_t rs = __builtin_amdgcn_make_buffer_rsrc(ws, 0, WS_USED, 0x00020000);
  LAS unsigned* sync = (LAS unsigned*)(smem + SYNC_OFF);
  STAMP(0);
  const int rt = (xcd & 1) * 8 + (idx & 7), cg = (xcd >> 1) * 4 + (idx >> 3);
  const int mt = (xcd >> 1) * 8 + (idx & 7), ct = (xcd & 1) * 4 + (idx >> 3);
  const int head = 4 * xcd + (idx & 3), qb = idx >> 2;
  const int ch = (5 * head) >> 1, odd = head & 1;
  const Ctx c = {x, cg < 10 ? wqkv : w1, b1, w2, wo, ws, lds0, b, rt, cg, (rt + 3 * cg) % 10, ct, qb, ch, w, lane};
  const size_t orow = (size_t)(64 * mt + 16 * (w & 3) + l15) * 1280 + (80 * ct + 4 * g) * 2;
  u32x2 xv[3];
  f32x4 acc[4][5];
  const unsigned va = g * 1024 + l15 * 16;
  bf16x8 wpre[5][5];
  const unsigned wb = w < 4 ? OFF_W2F + (ct * 48 + 12 * w) * 5120 : OFF_WOF + (ct * 20 + 5 * (w - 4)) * 5120;

  if (__builtin_expect(w >= 4, 1)) {
    __builtin_amdgcn_s_waitcnt(0);
    if (cg < 10) load_phase<3>(c, rs, tag, wpre, wb); else load_phase<4>(c, rs, tag, wpre, wb);
    STAMP(2);
    const int lw = w - 4, lt = tid - 256;
    const f32x4 z4 = {0.f, 0.f, 0.f, 0.f};
    {
      const u32x4 one4 = {0x3f803f80u, 0x3f803f80u, 0x3f803f80u, 0x3f803f80u};
      *(LAS u32x4*)(smem + V_OFF + 3 * VP + lt * 16) = one4;
      if (lt < 40) *(LAS u32x4*)(smem + V_OFF + 3 * VP + 4096 + lt * 16) = one4;
      *(LAS u32x4*)(smem + K_OFF + 3 * KP + lt * 16) = u32x4{qb == 0 ? 0xff80u : 0u, 0u, 0u, 0u};
      *(LAS u32x4*)(smem + K_OFF + 3 * KP + 4096 + lt * 16) = u32x4{0u, 0u, 0u, 0u};
      const int ty = lane < 24 ? lane >> 3 : 0, rr0 = 2 * qb - 2 + ((lane >> 1) & 3);
      const int rr = rr0 < 0 ? 0 : (ty == 0 && rr0 < 2 * qb ? 2 * qb : rr0);
      poll_flags(ws + FL_A + wg_of(rr, (80 * ty + ch + (lane & 1) * 2) / 24) * 128, true, tag);
    }
    STAMP(11);
#pragma unroll
    for (int i = 0; i < 2; ++i) {
      const int seg = 2 * lw + i;
      int tb = 4 * qb - 4 + seg; tb = tb < 0 ? 0 : tb;
      dma_kv(ws + OFF_QKV + (size_t)(tb * 240 + 80 + ch) * 1024, lane * 16, lane * 16 + 81920, lds0 + seg * 1024);
    }
    const int l31 = lane & 31, hh = lane >> 5;
    u32x4 qraw[2][2];
#pragma unroll
    for (int a = 0; a < 2; ++a)
#pragma unroll
      for (int ks = 0; ks < 2; ++ks)
        qraw[a][ks] = __builtin_amdgcn_raw_buffer_load_b128(rs, (ks ? 2 : hh) * 1024 + (32 * a + l31) * 16,
                                                            OFF_QKV + ((4 * qb + lw) * 240 + ch) * 1024, SC1);
    lds_arrive(sync + 5);
    bf16x8 qf[2][2];
#pragma unroll
    for (int a = 0; a < 2; ++a) {
      u32x4 v0 = qraw[a][0], v1 = qraw[a][1];
      if (odd && hh == 0) { v0[0] = 0u; v0[1] = 0u; }
      if (hh) v1 = u32x4{0x3f80u, 0u, 0u, 0u};
      else if (!odd) { v1[2] = 0u; v1[3] = 0u; }
      qf[a][0] = __builtin_bit_cast(bf16x8, v0);
      qf[a][1] = __builtin_bit_cast(bf16x8, v1);
    }
    f32x16 mF, z16;
#pragma unroll
    for (int e = 0; e < 16; ++e) {
      mF[e] = (e & 3) + 8 * (e >> 2) + 4 * hh > l31 ? 0.f : -INFINITY;
      z16[e] = 0.f;
    }
    asm volatile("" : "+v"(mF), "+v"(qf[0][0]), "+v"(qf[0][1]), "+v"(qf[1][0]), "+v"(qf[1][1]));
    WAIT_VM(0);
    WAIT_LGKM0();
    lds_arrive(sync);
    lds_wait(sync, 4);
    __builtin_amdgcn_s_setprio(2);
    STAMP(5);
    unsigned kp = lds0 + K_OFF + hh * KP + (64 * lw + l31) * 16;
    const bool ones = (g & 1) && (p4 >> 1);
    unsigned vp = lds0 + V_OFF + (2 * (g & 1) + (p4 >> 1)) * VP + ((ones ? 0 : 64 * lw) + 4 * hh + q4) * 16 + (p4 & 1) * 8;
    unsigned ov = l31 * 16 + hh * 8;
    bf16x8 q0 = qf[0][0], q1 = qf[0][1];
#define LDK(T, ks) (*(LAS bf16x8*)(size_t)(kp + (T) * 512 + (ks) * 2 * KP))
#define MF32(a, b, c) __builtin_amdgcn_mfma_f32_32x32x16_bf16(a, b, c, 0, 0, 0)
#define EX2(s_, e) pk2(__builtin_amdgcn_exp2f((s_)[e]), __builtin_amdgcn_exp2f((s_)[(e) + 1]))
#pragma unroll 1
    for (int un = 0; un < 2; ++un) {
      float m0 = -INFINITY, m1 = -INFINITY;
      {
        bf16x8 kf[9][2];
#pragma unroll
        for (int T = 0; T < 3; ++T) { kf[T][0] = LDK(T, 0); kf[T][1] = LDK(T, 1); }
        f32x16 sp = z16;
#pragma unroll
        for (int T = 0; T < 10; ++T) {
          f32x16 sn = z16;
          if (T + 3 < 9) { kf[T + 3][0] = LDK(T + 3, 0); kf[T + 3][1] = LDK(T + 3, 1); }
          if (T < 9) sn = MF32(kf[T][1], q1, MF32(kf[T][0], q0, T == 0 ? mF : z16));
          if (T == 9) {
#pragma unroll
            for (int e = 0; e < 16; ++e) sp[e] = mF[e] == 0.f ? -INFINITY : sp[e];
          }
          if (T > 0) {
#pragma unroll
            for (int e = 0; e < 16; e += 4) { m0 = MAX3(m0, sp[e], sp[e + 1]); m1 = MAX3(m1, sp[e + 2], sp[e + 3]); }
          }
          SGB(0x100, 2); SGB(0x8, 1); SGB(0x2, 4); SGB(0x8, 1); SGB(0x2, 4);
          asm volatile("" :: "v"(sn), "v"(m0), "v"(m1));
          SB();
          sp = sn;
        }
      }
      float mx = fmaxf(m0, m1);
      mx = fmaxf(mx, __shfl_xor(mx, 32));
      f32x16 ca;
#pragma unroll
      for (int e = 0; e < 16; ++e) ca[e] = -mx;
      f32x16 oa = z16;
      {
        bf16x8 kc[2][2], vf[2][2];
        f32x16 sc[2];
        u32x4 pa[2] = {{0u, 0u, 0u, 0u}, {0u, 0u, 0u, 0u}};
        kc[0][0] = LDK(0, 0); kc[0][1] = LDK(0, 1); kc[1][0] = LDK(1, 0); kc[1][1] = LDK(1, 1);
        sc[0] = MF32(kc[0][1], q1, MF32(kc[0][0], q0, ca + mF));
        sc[1] = z16;
        asm volatile("" :: "v"(sc[0]));
        SB();
#pragma unroll
        for (int T = 0; T < 10; ++T) {
          const int c = T & 1, n = c ^ 1;
          if (T < 9) {
#pragma unroll
            for (int ks = 0; ks < 2; ++ks) vf[c][ks] = cat44(trread(vp + T * 512 + ks * 256), trread(vp + T * 512 + ks * 256 + 128));
          }
          if (T < 8) sc[n] = MF32(kc[n][1], q1, MF32(kc[n][0], q0, ca));
          if (T < 7) { kc[c][0] = LDK(T + 2, 0); kc[c][1] = LDK(T + 2, 1); }
          if (T > 0) oa = MF32(vf[n][1], __builtin_bit_cast(bf16x8, pa[1]), MF32(vf[n][0], __builtin_bit_cast(bf16x8, pa[0]), oa));
          if (T == 8) {
#pragma unroll
            for (int e = 0; e < 16; ++e) sc[c][e] = mF[e] == 0.f ? -INFINITY : sc[c][e];
          }
          if (T < 9) {
#pragma unroll
            for (int e = 0; e < 4; ++e) { pa[0][e] = EX2(sc[c], 2 * e); pa[1][e] = EX2(sc[c], 8 + 2 * e); }
          }
          SGB(0x100, 6);
          if (T == 8) SGB(0x2, 16);
          SGB(0x8, 1); SGB(0x400, 4); SGB(0x2, 1);
#pragma unroll
          for (int k = 0; k < 3; ++k) { SGB(0x8, 1); SGB(0x400, 2); SGB(0x2, 1); SGB(0x400, 2); SGB(0x2, 1); }
          SGB(0x2, 1);
          asm volatile("" :: "v"(sc[n]), "v"(oa), "v"(pa[0]), "v"(pa[1]));
          SB();
        }
      }
      const float il = __builtin_amdgcn_rcpf(oa[12]);
#pragma unroll
      for (int pl = 0; pl < 3; ++pl) {
        const bool ok = pl == 1 || (pl == 0 ? !(odd && hh == 0) : (odd || hh == 0));
        if (ok) {
          const u32x2 pkd = {pk2(oa[4 * pl] * il, oa[4 * pl + 1] * il), pk2(oa[4 * pl + 2] * il, oa[4 * pl + 3] * il)};
          __builtin_amdgcn_raw_buffer_store_b64(pkd, rs, pl * 1024 + ov, OFF_O + ((4 * qb + lw) * 80 + ch) * 1024, SC1);
        }
      }
      kp += 512; vp += ones ? 0 : 512; ov += 512;
      q0 = qf[1][0]; q1 = qf[1][1];
    }
    __builtin_amdgcn_s_setprio(0);
    STAMP(6);
#pragma unroll
    for (int k = WOPRE; k < 5; ++k)
#pragma unroll
      for (int j = 0; j < 5; ++j) wpre[k][j] = ld16(rs, lane * 16, wb + k * 5120 + j * 1024);
#pragma unroll
    for (int j = 0; j < 2; ++j) xv[j] = *(const u32x2*)(x + orow + (3 + j) * 32);
    wait_vm<5 * (5 - WOPRE) + 2>();
    const bool last_o = lds_arrive(sync + 1) == 3;
    if (last_o && lane == 0) __hip_atomic_store((GAS u64*)(ws + FL_O + b * 128), tag, RLX_AGENT);
    const unsigned ab = OFF_O + (mt * 80 + 20 * lw) * 1024;
    {
      const int hd = 8 * lw + (lane & 7);
      poll_flags(ws + FL_O + (((((mt >> 2) << 2) | (hd & 3)) << 3) | (hd >> 2)) * 128, lane < 8, tag);
    }
    STAMP(7);
    bf16x8 af[3][4];
#pragma unroll
    for (int k = 0; k < 3; ++k)
#pragma unroll
      for (int i = 0; i < 4; ++i) af[k][i] = ld16(rs, va + i * 256, ab + k * 4096);
    SB();
#pragma unroll
    for (int i = 0; i < 4; ++i)
#pragma unroll
      for (int j = 0; j < 5; ++j) acc[i][j] = z4;
#pragma unroll
    for (int k = 0; k < 5; ++k) {
#pragma unroll
      for (int i = 0; i < 4; ++i)
#pragma unroll
        for (int j = 0; j < 5; ++j) acc[i][j] = mfma16(wpre[k][j], af[k % 3][i], acc[i][j]);
      SB();
      if (k < 2) {
#pragma unroll
        for (int i = 0; i < 4; ++i) af[k][i] = ld16(rs, va + i * 256, ab + (k + 3) * 4096);
        SB();
      }
    }
    STAMP(8);
    lds_wait(sync + 1, 4);
    lds_wait(sync + 6, 4);
#pragma unroll
    for (int i = 0; i < 4; ++i) {
#pragma unroll
      for (int j = 0; j < 3; ++j) *(LAS f32x4*)(smem + REDS1_OFF + ((i * 4 + lw) * 3 + j) * 1024 + lane * 16) = acc[i][j];
      if (i != lw) {
#pragma unroll
        for (int j = 0; j < 2; ++j)
          *(LAS f32x4*)(smem + REDS2_OFF + ((i * 3 + lw - (lw > i ? 1 : 0)) * 2 + j) * 1024 + lane * 16) = acc[i][3 + j];
      }
    }
    __syncthreads();
    STAMP(9);
#pragma unroll
    for (int j = 0; j < 2; ++j) {
      f32x4 sv = lw == 0 ? acc[0][3 + j] : (lw == 1 ? acc[1][3 + j] : (lw == 2 ? acc[2][3 + j] : acc[3][3 + j]));
#pragma unroll
      for (int k = 0; k < 3; ++k) sv += *(LAS f32x4*)(smem + REDS2_OFF + ((lw * 3 + k) * 2 + j) * 1024 + lane * 16);
      const f32x4 gt = *(LAS f32x4*)(smem + GX_OFF + (lw * 2 + j) * 1024 + lane * 16);
      *(u32x2*)(out + orow + (3 + j) * 32) = u32x2{pk2(bflo(xv[j][0]) + sv[0] * gt[0], bfhi(xv[j][0]) + sv[1] * gt[1]),
                                                   pk2(bflo(xv[j][1]) + sv[2] * gt[2], bfhi(xv[j][1]) + sv[3] * gt[3])};
    }
    if (b == 0 && w == 4) {
#pragma unroll
      for (int i = 0; i < 4; ++i) poll_flags(ws + FL_W + (lane + 64 * i) * 128, true, tag);
      if (lane == 0) __hip_atomic_store((GAS u64*)(ws + OFF_FL), tag, RLX_AGENT);
    }
  } else {
    if (idx * 4 + w < CODE_KB) {
      const u32x4 t0 = *(const u32x4*)(pc0 + (idx * 4 + w) * 1024 + lane * 16);
      asm volatile("" :: "v"(t0));
    }
    {
      const char* p = ws;
      asm volatile("" : "+s"(p));
      tag = *(const CAS u64*)(p + OFF_FL) + 1;
    }
    if (cg < 10) mfma_phase<3>(c, rs); else mfma_phase<4>(c, rs);
    STAMP(3);
    WAIT_VM(0);
    if (lds_arrive(sync + 2) == 3 && lane == 0) __hip_atomic_store((GAS u64*)(ws + FL_A + b * 128), tag, RLX_AGENT);
    STAMP(4);
#pragma unroll
    for (int k = 0; k < 4; ++k)
#pragma unroll
      for (int j = 0; j < 5; ++j) wpre[k][j] = ld16(rs, lane * 16, wb + k * 5120 + j * 1024);
    const unsigned wl_ = lds0 + SLAB_OFF + w * 24576;
    const unsigned ab = OFF_H + (mt * 192 + 48 * w) * 1024;
    poll_flags(ws + FL_A + wg_of(mt >> 1, 10 + (24 * w + (lane & 1) * 23) / 16) * 128, lane < 2, tag);
    lds_wait(sync + 5, 4);
    STAMP(7);
    bf16x8 af[3][4];
#pragma unroll
    for (int k = 0; k < 3; ++k)
#pragma unroll
      for (int i = 0; i < 4; ++i) af[k][i] = ld16(rs, va + i * 256, ab + k * 4096);
    SB();
#pragma unroll
    for (int i = 0; i < 4; ++i)
#pragma unroll
      for (int j = 0; j < 5; ++j) acc[i][j] = f32x4{0.f, 0.f, 0.f, 0.f};
#pragma unroll
    for (int k = 0; k < 12; ++k) {
      const int cs = k < 4 ? k : (k < 8 ? 4 - (k & 1) : (k < 11 ? k - 8 : 4));
      if (k >= 3 && k < 7) {
#pragma unroll
        for (int j = 0; j < 5; ++j) wpre[4 - ((k + 1) & 1)][j] = *(LAS bf16x8*)(size_t)(wl_ + (k - 3) * 5120 + j * 1024 + lane * 16);
      }
#pragma unroll
      for (int i = 0; i < 4; ++i)
#pragma unroll
        for (int j = 0; j < 5; ++j) acc[i][j] = mfma16(wpre[cs][j], af[k % 3][i], acc[i][j]);
      SB();
      if (k + 3 < 12) {
#pragma unroll
        for (int i = 0; i < 4; ++i) af[k % 3][i] = ld16(rs, va + i * 256, ab + (k + 3) * 4096);
      }
      if (k < 3 || k == 6) {
#pragma unroll
        for (int j = 0; j < 5; ++j) wpre[k < 3 ? k : 4][j] = ld16(rs, lane * 16, wb + (k < 3 ? k + 8 : 11) * 5120 + j * 1024);
      }
      SB();
    }
    lds_arrive(sync + 6);
    lds_wait(sync + 6, 4);
    STAMP(8);
    red_write(smem + REDG_OFF, w, lane, acc);
    u32x2 bb[5];
#pragma unroll
    for (int j = 0; j < 5; ++j) {
      if (j < 3) xv[j] = *(const u32x2*)(x + orow + j * 32);
      bb[j] = *(const u32x2*)(b2 + (80 * ct + 16 * j + 4 * g) * 2);
    }
    WAIT_LGKM0();
    lds_arrive(sync + 3);
    lds_wait(sync + 3, 4);
    f32x4 gt[5];
    red_sum(smem + REDG_OFF, w, lane, acc, gt);
#pragma unroll
    for (int j = 0; j < 5; ++j) {
      const float bv[4] = {bflo(bb[j][0]), bfhi(bb[j][0]), bflo(bb[j][1]), bfhi(bb[j][1])};
#pragma unroll
      for (int e = 0; e < 4; ++e) {
        const float t = gt[j][e] + bv[e];
        gt[j][e] = t * __builtin_amdgcn_rcpf(1.f + __expf(-t));
      }
      if (j >= 3) *(LAS f32x4*)(smem + GX_OFF + (w * 2 + j - 3) * 1024 + lane * 16) = gt[j];
    }
    __syncthreads();
    STAMP(9);
#pragma unroll
    for (int j = 0; j < 3; ++j) {
      f32x4 sv = *(LAS f32x4*)(smem + REDS1_OFF + ((w * 4) * 3 + j) * 1024 + lane * 16);
#pragma unroll
      for (int k = 1; k < 4; ++k) sv += *(LAS f32x4*)(smem + REDS1_OFF + ((w * 4 + k) * 3 + j) * 1024 + lane * 16);
      *(u32x2*)(out + orow + j * 32) = u32x2{pk2(bflo(xv[j][0]) + sv[0] * gt[j][0], bfhi(xv[j][0]) + sv[1] * gt[j][1]),
                                             pk2(bflo(xv[j][1]) + sv[2] * gt[j][2], bfhi(xv[j][1]) + sv[3] * gt[j][3])};
    }
  }
  STAMP(10);
}

extern "C" void kernel_launch(void* const* d_in, const int* in_sizes, int n_in, void* d_out, int out_size, void* d_ws,
                              size_t ws_size, hipStream_t stream) {
  (void)hipFuncSetAttribute((const void*)k_all, hipFuncAttributeMaxDynamicSharedMemorySize, LDS_ALL);
  k_all<<<256, 512, LDS_ALL, stream>>>((const char*)d_in[0], (const char*)d_in[1], (const char*)d_in[4], (const char*)d_in[5],
                                       (const char*)d_in[6], (const char*)d_in[2], (const char*)d_in[7], (char*)d_ws, (char*)d_out);
}
```

```cpp
#include <hip/hip_runtime.h>
#include <stdint.h>

#define LAS __attribute__((address_space(3)))
#define GAS __attribute__((address_space(1)))
#define CAS __attribute__((address_space(4)))
typedef short bf16x8 __attribute__((ext_vector_type(8)));
typedef short s16x4 __attribute__((ext_vector_type(4)));
typedef float f32x4 __attribute__((ext_vector_type(4)));
typedef unsigned u32x4 __attribute__((ext_vector_type(4)));
typedef unsigned u32x2 __attribute__((ext_vector_type(2)));
typedef __bf16 bf16x2_t __attribute__((ext_vector_type(2)));
typedef float f32x2 __attribute__((ext_vector_type(2)));
typedef float f32x16 __attribute__((ext_vector_type(16)));
typedef unsigned long long u64;

constexpr unsigned OFF_FL = 0;
constexpr unsigned FL_W = 1024, FL_A = FL_W + 32768, FL_O = FL_A + 32768;
constexpr unsigned OFF_RS = 131072;
constexpr unsigned OFF_CS = OFF_RS + 16 * 10 * 2048;
constexpr unsigned OFF_QKV = OFF_CS + 16 * 10 * 2048;
constexpr unsigned OFF_H = OFF_QKV + 32 * 240 * 1024;
constexpr unsigned OFF_O = OFF_H + 32 * 192 * 1024;
constexpr unsigned OFF_W2F = OFF_O + 32 * 80 * 1024;
constexpr unsigned OFF_WOF = OFF_W2F + 8 * 48 * 5120;
constexpr unsigned WS_USED = OFF_WOF + 8 * 20 * 5120;

__device__ __forceinline__ f32x4 mfma16(bf16x8 a, bf16x8 b, f32x4 c) {
  return __builtin_amdgcn_mfma_f32_16x16x32_bf16(a, b, c, 0, 0, 0);
}
__device__ __forceinline__ unsigned pk2(float a, float b) {
  f32x2 v = {a, b};
  bf16x2_t r = __builtin_convertvector(v, bf16x2_t);
  return __builtin_bit_cast(unsigned, r);
}
__device__ __forceinline__ float rbf(float a) { return __builtin_bit_cast(float, pk2(0.f, a) & 0xffff0000u); }
__device__ __forceinline__ float bflo(unsigned u) { return __builtin_bit_cast(float, u << 16); }
__device__ __forceinline__ float bfhi(unsigned u) { return __builtin_bit_cast(float, u & 0xffff0000u); }
__device__ __forceinline__ u32x2 fin4(u32x2 xv, f32x4 sv, f32x4 gt) {
  return u32x2{pk2(bflo(xv[0]) + rbf(rbf(sv[0]) * gt[0]), bfhi(xv[0]) + rbf(rbf(sv[1]) * gt[1])),
               pk2(bflo(xv[1]) + rbf(rbf(sv[2]) * gt[2]), bfhi(xv[1]) + rbf(rbf(sv[3]) * gt[3]))};
}
__device__ __forceinline__ s16x4 trread(unsigned a) {
  return __builtin_amdgcn_ds_read_tr16_b64_v4i16((LAS s16x4*)(size_t)a);
}
__device__ __forceinline__ bf16x8 cat44(s16x4 a, s16x4 b) {
  return __builtin_shufflevector(a, b, 0, 1, 2, 3, 4, 5, 6, 7);
}
#define WAIT_VM(n) asm volatile("s_waitcnt vmcnt(" #n ")" ::: "memory")
#define WAIT_LGKM0() __builtin_amdgcn_s_waitcnt(0xC07F)
#define BAR() __builtin_amdgcn_s_barrier()
#define SB() __builtin_amdgcn_sched_barrier(0)
#define SGB(m, n) __builtin_amdgcn_sched_group_barrier(m, n, 0)
#define MAX3(a, b, c) __builtin_fmaxf(__builtin_fmaxf(a, b), c)
#define RLX_AGENT __ATOMIC_RELAXED, __HIP_MEMORY_SCOPE_AGENT
constexpr int SC1 = 16;
constexpr int CODE_KB = 48;
template <int N> struct IC { static constexpr int value = N; };

#define STAMP(i)
#define CLK(v)

__device__ __forceinline__ void dma1(const char* sbase, unsigned voff, unsigned lds) {
  unsigned keep;
  asm volatile("s_mov_b32 %0, m0\n\ts_mov_b32 m0, %3\n\ts_nop 0\n\tglobal_load_lds_dwordx4 %1, %2\n\ts_mov_b32 m0, %0"
               : "=&s"(keep) : "v"(voff), "s"(sbase), "s"(lds) : "memory");
}
#define DMA_W2(ofs) "s_add_u32 m0, m0, 7040\n\ts_nop 0\n\tglobal_load_lds_dwordx4 %8, %7 offset:" #ofs "\n\t" \
                    "s_add_u32 m0, m0, 1024\n\ts_nop 0\n\tglobal_load_lds_dwordx4 %9, %7 offset:" #ofs "\n\t"
#define DMA_HEAD "s_mov_b32 %0, m0\n\ts_mov_b32 m0, %10\n\ts_nop 0\n\tglobal_load_lds_dwordx4 %8, %7\n\t" \
                 "s_add_u32 m0, m0, 1024\n\ts_nop 0\n\tglobal_load_lds_dwordx4 %9, %7\n\t" DMA_W2(128) DMA_W2(256)
#define DMA_X "s_mov_b32 m0, %6\n\ts_nop 0\n\tglobal_load_lds_dwordx4 %2, %1\n\t" \
              "s_add_u32 m0, m0, 1024\n\ts_nop 0\n\tglobal_load_lds_dwordx4 %3, %1\n\t" \
              "s_add_u32 m0, m0, 1024\n\ts_nop 0\n\tglobal_load_lds_dwordx4 %4, %1\n\t" \
              "s_add_u32 m0, m0, 1024\n\ts_nop 0\n\tglobal_load_lds_dwordx4 %5, %1\n\ts_mov_b32 m0, %0"
#define DMA_OPS : "=&s"(keep) : "s"(sa), "v"(va0), "v"(va1), "v"(va2), "v"(va3), "s"(la), "s"(sb), "v"(vb0), "v"(vb1), "s"(lb) : "memory", "scc"
template <int NP>
__device__ __forceinline__ void dma_stage(const char* sa, unsigned va0, unsigned va1, unsigned va2, unsigned va3, unsigned la,
                                          const char* sb, unsigned vb0, unsigned vb1, unsigned lb) {
  unsigned keep;
  if constexpr (NP == 3) asm volatile(DMA_HEAD DMA_X DMA_OPS);
  else asm volatile(DMA_HEAD DMA_W2(384) DMA_X DMA_OPS);
}
__device__ __forceinline__ void dma_kv(const char* sb, unsigned vk, unsigned vv, unsigned l) {
  unsigned keep;
  asm volatile("s_mov_b32 %0, m0\n\ts_mov_b32 m0, %4\n\ts_nop 0\n\tglobal_load_lds_dwordx4 %2, %1 sc1\n\t"
               "s_add_u32 m0, m0, 7168\n\ts_nop 0\n\tglobal_load_lds_dwordx4 %2, %1 offset:1024 sc1\n\t"
               "s_add_u32 m0, m0, 7168\n\ts_nop 0\n\tglobal_load_lds_dwordx4 %2, %1 offset:2048 sc1\n\t"
               "s_add_u32 m0, m0, 18432\n\ts_nop 0\n\tglobal_load_lds_dwordx4 %3, %1 sc1\n\t"
               "s_add_u32 m0, m0, 7232\n\ts_nop 0\n\tglobal_load_lds_dwordx4 %3, %1 offset:1024 sc1\n\t"
               "s_add_u32 m0, m0, 7232\n\ts_nop 0\n\tglobal_load_lds_dwordx4 %3, %1 offset:2048 sc1\n\t"
               "s_mov_b32 m0, %0"
               : "=&s"(keep) : "s"(sb), "v"(vk), "v"(vv), "s"(l) : "memory", "scc");
}
__device__ __forceinline__ void dma5_sc1(const char* sb, unsigned v, unsigned l) {
  unsigned keep;
  asm volatile("s_mov_b32 %0, m0\n\ts_mov_b32 m0, %3\n\ts_nop 0\n\tglobal_load_lds_dwordx4 %2, %1 sc1\n\t"
               "global_load_lds_dwordx4 %2, %1 offset:1024 sc1\n\tglobal_load_lds_dwordx4 %2, %1 offset:2048 sc1\n\t"
               "global_load_lds_dwordx4 %2, %1 offset:3072 sc1\n\ts_add_u32 m0, m0, 1024\n\ts_nop 0\n\t"
               "global_load_lds_dwordx4 %4, %1 offset:3072 sc1\n\ts_mov_b32 m0, %0"
               : "=&s"(keep) : "s"(sb), "v"(v), "s"(l), "v"(v + 1024) : "memory", "scc");
}
__device__ __forceinline__ u32x2 aload8(const char* sbase, unsigned voff) {
  u32x2 r;
  asm volatile("global_load_dwordx2 %0, %1, %2 sc1" : "=v"(r) : "v"(voff), "s"(sbase) : "memory");
  return r;
}
__device__ __forceinline__ u32x4 aload16(const char* sbase, unsigned voff) {
  u32x4 r;
  asm volatile("global_load_dwordx4 %0, %1, %2 sc1" : "=v"(r) : "v"(voff), "s"(sbase) : "memory");
  return r;
}
#define PIN(x) asm volatile("" : "+v"(x))
template <int N> __device__ __forceinline__ void wait_vm() { asm volatile("s_waitcnt vmcnt(%0)" :: "i"(N) : "memory"); }
__device__ __forceinline__ void poll_flags(const char* flag, bool need, u64 tag) {
#pragma unroll 1
  for (int spins = 0; spins < (1 << 20); ++spins) {
    const u64 v = need ? __hip_atomic_load((const GAS u64*)flag, RLX_AGENT) : tag;
    if (__all(v == tag)) break;
    __builtin_amdgcn_s_sleep(1);
  }
  __builtin_amdgcn_fence(__ATOMIC_ACQUIRE, "wavefront");
}
__device__ __forceinline__ unsigned lds_arrive(LAS unsigned* c) {
  unsigned r = 0;
  if ((threadIdx.x & 63) == 0) r = __hip_atomic_fetch_add(c, 1u, __ATOMIC_RELAXED, __HIP_MEMORY_SCOPE_WORKGROUP);
  return __builtin_amdgcn_readfirstlane(r);
}
__device__ __forceinline__ void lds_wait_sleep(LAS unsigned* c, unsigned n) {
#pragma unroll 1
  for (int spins = 0; spins < (1 << 22); ++spins) {
    if (__builtin_amdgcn_readfirstlane(__hip_atomic_load(c, __ATOMIC_RELAXED, __HIP_MEMORY_SCOPE_WORKGROUP)) >= n) break;
    __builtin_amdgcn_s_sleep(2);
  }
  __builtin_amdgcn_fence(__ATOMIC_ACQUIRE, "wavefront");
}
__device__ __forceinline__ void lds_wait(LAS unsigned* c, unsigned n) {
#pragma unroll 1
  for (int spins = 0; spins < (1 << 22); ++spins) {
    if (__builtin_amdgcn_readfirstlane(__hip_atomic_load(c, __ATOMIC_RELAXED, __HIP_MEMORY_SCOPE_WORKGROUP)) >= n) break;
  }
  __builtin_amdgcn_fence(__ATOMIC_ACQUIRE, "wavefront");
}

#define WOPRE 4
constexpr int STG = 49152, RT_OFF = 3 * STG, LDS_ALL = 163840;
constexpr int ST_OFF = 160768;
constexpr int CS_OFF = ST_OFF + 1024, SYNC_OFF = ST_OFF + 2048;
constexpr int B1_OFF = 157696;
constexpr int KP = 8192, VP = 8192 + 64;
constexpr int K_OFF = 0, V_OFF = 4 * KP, SLAB_OFF = 62464;
constexpr int REDG_OFF = SLAB_OFF, GX_OFF = REDG_OFF + 61440, REDS1_OFF = 0, REDS2_OFF = GX_OFF + 8192;

__device__ __forceinline__ void st16(u32x4 v, __amdgpu_buffer_rsrc_t rs, unsigned voff, unsigned soff) {
  __builtin_amdgcn_raw_buffer_store_b128(v, rs, voff + soff, 0, SC1);
}
__device__ __forceinline__ bf16x8 ld16(__amdgpu_buffer_rsrc_t rs, unsigned voff, unsigned soff) {
  return __builtin_bit_cast(bf16x8, __builtin_amdgcn_raw_buffer_load_b128(rs, voff, soff, SC1));
}
__device__ __forceinline__ void red_write(LAS char* red, int kw, int lane, const f32x4 (&acc)[4][5]) {
#pragma unroll
  for (int i = 0; i < 4; ++i) {
    if (i != kw) {
      const int slot = kw - (kw > i ? 1 : 0);
#pragma unroll
      for (int j = 0; j < 5; ++j) *(LAS f32x4*)(red + ((i * 3 + slot) * 5 + j) * 1024 + lane * 16) = acc[i][j];
    }
  }
}
__device__ __forceinline__ void red_sum(LAS char* red, int kw, int lane, const f32x4 (&acc)[4][5], f32x4 (&o)[5]) {
#pragma unroll
  for (int j = 0; j < 5; ++j) {
    f32x4 a = kw == 0 ? acc[0][j] : (kw == 1 ? acc[1][j] : (kw == 2 ? acc[2][j] : acc[3][j]));
#pragma unroll
    for (int s = 0; s < 3; ++s) a += *(LAS f32x4*)(red + ((kw * 3 + s) * 5 + j) * 1024 + lane * 16);
    o[j] = a;
  }
}
__device__ __forceinline__ int wg_of(int rt, int cg) { return (((cg & 3) * 8 + (rt & 7)) << 3) | ((cg >> 2) << 1) | (rt >> 3); }
template <int N>
__device__ __forceinline__ bool sum_pairs(const u32x4 (&v)[N], unsigned last_on, unsigned tg, float& a, float& b) {
  bool ok = true;
  a = 0.f; b = 0.f;
#pragma unroll
  for (int k = 0; k < N; ++k) {
    ok = ok && v[k][1] == tg && v[k][3] == tg;
    const unsigned m = k == N - 1 ? last_on : 0xffffffffu;
    a += __uint_as_float(v[k][0] & m);
    b += __uint_as_float(v[k][2] & m);
  }
  return __all(ok);
}
template <int N>
__device__ __forceinline__ void regather(__amdgpu_buffer_rsrc_t rs, const unsigned (&vo)[N], unsigned last_on, unsigned tg, float& a, float& b) {
#pragma unroll 1
  for (int spins = 0; spins < (1 << 20); ++spins) {
    u32x4 v[N];
#pragma unroll
    for (int k = 0; k < N; ++k) v[k] = __builtin_amdgcn_raw_buffer_load_b128(rs, vo[k], 0, SC1);
    if (sum_pairs<N>(v, last_on, tg, a, b)) break;
    __builtin_amdgcn_s_sleep(2);
  }
}

#define STG_STAMP(s_)

struct Ctx {
  const char *x, *W, *b1, *w2, *wo;
  char* ws;
  unsigned lds0;
  int b, rt, cg, rot, ct, qb, ch, w, lane;
};

template <int NP>
__device__ __forceinline__ void mfma_phase(const Ctx& c, __amdgpu_buffer_rsrc_t rs) {
  constexpr int NT = 2 * NP;
  constexpr bool ISH = NP == 4;
  char* ws = c.ws;
  const int lane = c.lane, g = lane >> 4, l15 = lane & 15, q4 = l15 >> 2, p4 = lane & 3;
  const int mw = c.w & 1, nw = c.w >> 1;
  const int sigl = ((q4 >> 1) & 1) | ((g & 1) << 1), xsw = g ^ (l15 >> 1);
  unsigned xa[2], wa[NT];
  xa[0] = c.lds0 + (64 * mw + l15) * 128 + (xsw << 4);
  xa[1] = c.lds0 + (64 * mw + l15) * 128 + ((xsw ^ 4) << 4);
  f32x4 acc[4][NT];
#pragma unroll
  for (int j = 0; j < NT; ++j) {
    const int tl = NT * nw + j;
    wa[j] = c.lds0 + 16384 + (tl >> 2) * 8192 + (8 * g + q4) * 128 + (((tl & 3) ^ sigl) << 5) + p4 * 8;
#pragma unroll
    for (int i = 0; i < 4; ++i) acc[i][j] = f32x4{0.f, 0.f, 0.f, 0.f};
  }
  bf16x8 xf[2][4], wf[2][NT];
#define LDX(set, i, ks) xf[set][i] = *(LAS bf16x8*)(size_t)(xa[ks] + (i) * 2048)
#define LDW(set, j, ks) wf[set][j] = cat44(trread(wa[j] + (ks) * 4096), trread(wa[j] + (ks) * 4096 + 512))
#define HALF(set, ks) do { \
    _Pragma("unroll") for (int j = 0; j < NT; ++j) { \
      if (j == 0) { _Pragma("unroll") for (int i = 0; i < 4; ++i) LDX((set) ^ 1, i, ks); } \
      else if (2 * j - 2 < NT) { LDW((set) ^ 1, 2 * j - 2, ks); LDW((set) ^ 1, 2 * j - 1, ks); } \
      _Pragma("unroll") for (int i = 0; i < 4; ++i) acc[i][j] = mfma16(wf[set][j], xf[set][i], acc[i][j]); \
      SGB(0x8, 1); SGB(0x100, 1); SGB(0x8, 1); SGB(0x100, 1); SGB(0x8, 1); SGB(0x100, 1); SGB(0x8, 1); SGB(0x100, 1); \
      asm volatile("" :: "v"(acc[0][j]), "v"(acc[1][j]), "v"(acc[2][j]), "v"(acc[3][j])); \
      SB(); \
    } } while (0)
  BAR();
  STAMP(1);
#pragma unroll
  for (int i = 0; i < 4; ++i) LDX(0, i, 0);
#pragma unroll
  for (int j = 0; j < NT; ++j) LDW(0, j, 0);
  WAIT_LGKM0();
  int buf = 0;
#pragma unroll 1
  for (int t = 0; t < 10; ++t) {
    HALF(0, 1);
    {
      const int d = buf == 2 ? -2 * STG : STG;
      buf = buf == 2 ? 0 : buf + 1;
      xa[0] += d; xa[1] += d;
#pragma unroll
      for (int j = 0; j < NT; ++j) wa[j] += d;
    }
    WAIT_LGKM0();
    BAR();
    STG_STAMP(t);
    SB();
    HALF(1, 0);
    WAIT_LGKM0();
  }
  STAMP(2);
  lds_wait((LAS unsigned*)(size_t)(c.lds0 + SYNC_OFF + 28), 4);
  float mean[4], rstd[4];
#pragma unroll
  for (int i = 0; i < 4; ++i) {
    const f32x2 st = *(LAS f32x2*)(size_t)(c.lds0 + ST_OFF + (64 * mw + 16 * i + l15) * 8);
    mean[i] = st[0]; rstd[i] = st[1];
  }
  const int u0 = ISH ? 16 * (c.cg - 10) + NT * nw : 12 * c.cg + NT * nw;
  const unsigned so = (ISH ? OFF_H + (2 * c.rt + mw) * 192 * 1024 : OFF_QKV + (2 * c.rt + mw) * 240 * 1024) + 2 * u0 * 1024;
  const unsigned vo = (g >> 1) * 1024 + (16 * (g & 1) + l15) * 16;
#pragma unroll
  for (int j = 0; j < NT; ++j) {
    const f32x4 c4 = *(LAS f32x4*)(size_t)(c.lds0 + CS_OFF + ((NT * nw + j) * 16 + 4 * g) * 4);
    f32x4 bv = {0.f, 0.f, 0.f, 0.f};
    if (ISH) {
      const u32x2 bb = *(LAS u32x2*)(size_t)(c.lds0 + B1_OFF + ((NT * nw + j) * 16 + 4 * g) * 2);
      bv = f32x4{bflo(bb[0]), bfhi(bb[0]), bflo(bb[1]), bfhi(bb[1])};
    }
    const float qs = u0 + j < 40 ? 0.22360679774997896f * 1.4426950408889634f : 1.f;
#pragma unroll
    for (int pp = 0; pp < 2; ++pp) {
      float v[2][4];
#pragma unroll
      for (int i = 0; i < 2; ++i)
#pragma unroll
        for (int r = 0; r < 4; ++r) {
          float t = rstd[2 * pp + i] * (acc[2 * pp + i][j][r] - mean[2 * pp + i] * c4[r]);
          if (ISH) { t += bv[r]; t = t > 0.f ? t : 0.f; t = t * t; } else t *= qs;
          v[i][r] = t;
        }
      const unsigned x0 = pk2(v[0][0], v[0][1]), x1 = pk2(v[0][2], v[0][3]);
      const unsigned y0 = pk2(v[1][0], v[1][1]), y1 = pk2(v[1][2], v[1][3]);
      const auto s0 = __builtin_amdgcn_permlane16_swap(x0, y0, false, false);
      const auto s1 = __builtin_amdgcn_permlane16_swap(x1, y1, false, false);
      const u32x4 o = {s0[0], s1[0], s0[1], s1[1]};
      st16(o, rs, vo + pp * 512, so + 2 * j * 1024);
    }
  }
}

template <int NP>
__device__ __forceinline__ void load_phase(const Ctx& c, __amdgpu_buffer_rsrc_t rs, u64& tag, bf16x8 (&wpre)[5][5], unsigned wb) {
  constexpr int D = 4 + 2 * NP, LDW_ = NP == 4 ? 3072 : 3840;
  char* ws = c.ws;
  const unsigned lds0 = c.lds0;
  const int lane = c.lane, g = lane >> 4, l15 = lane & 15, q4 = l15 >> 2, p4 = lane & 3;
  const int lw = c.w - 4, lr = lane >> 3, ls = lane & 7, lt = lw * 64 + lane;
  const int bk = 16 * lw + lr;
  const int bs0 = (bk >> 1) & 1, bs1 = bs0 ^ 2;
  const int col0 = NP == 4 ? (c.cg - 10) * 512 : c.cg * 384;
  const unsigned vb0 = bk * LDW_ + col0 + ((ls ^ (bs0 << 1)) << 4), vb1 = (bk + 8) * LDW_ + col0 + ((ls ^ (bs1 << 1)) << 4);
  const unsigned vA = (c.rt * 128 + 32 * lw + lr) * 1280 + ((ls ^ (lr >> 1)) << 4);
  int ktn = c.rot;
  auto issue = [&](auto S) {
    constexpr int s = decltype(S)::value;
    const unsigned dst = lds0 + (s % 3) * STG;
    dma_stage<NP>(c.x + ktn * 128, vA, (vA + 10240) ^ 64u, vA + 20480, (vA + 30720) ^ 64u, dst + lw * 4096,
                  c.W + (size_t)ktn * (64 * LDW_), vb0, vb1, dst + 16384 + lw * 2048);
    ktn = ktn == 9 ? 0 : ktn + 1;
  };
  issue(IC<0>{});
  const bool do_rt = c.b < 170;
  const int rkb = c.b / 5, rnb = c.b - 5 * rkb;
  if (do_rt) {
    const char* Wr = rkb < 24 ? c.w2 + (size_t)rkb * 64 * 1280 : c.wo + (size_t)(rkb - 24) * 64 * 1280;
    const unsigned r0 = bk * 1280 + rnb * 256 + ((ls ^ (bs0 << 1)) << 4), r1 = (bk + 8) * 1280 + rnb * 256 + ((ls ^ (bs1 << 1)) << 4);
    dma1(Wr, r0, lds0 + RT_OFF + lw * 2048);
    dma1(Wr, r1, lds0 + RT_OFF + lw * 2048 + 1024);
    dma1(Wr + 128, r0, lds0 + RT_OFF + 8192 + lw * 2048);
    dma1(Wr + 128, r1, lds0 + RT_OFF + 8192 + lw * 2048 + 1024);
  }
  issue(IC<1>{}); issue(IC<2>{});
  {
    const char* p = ws;
    asm volatile("" : "+s"(p));
    tag = *(const CAS u64*)(p + OFF_FL) + 1;
  }
  const unsigned tg = (unsigned)tag;
  wait_vm<2 * D>();
  BAR();
  STAMP(1);
  const int sigl = ((q4 >> 1) & 1) | ((g & 1) << 1), xsw = g ^ (l15 >> 1);
  const bf16x8 ONE8 = {0x3f80, 0x3f80, 0x3f80, 0x3f80, 0x3f80, 0x3f80, 0x3f80, 0x3f80};
  const f32x4 z4 = {0.f, 0.f, 0.f, 0.f};
  bf16x8 sw_[NP][2], sx_[2][2];
  u32x4 gr[5], gc[5];
  u32x2 fl;
  const int fkb = lane < 12 ? 6 * lw + (lane >> 1) : 24 + ((5 * lw) >> 1) + (lane < 18 ? (lane - 12) >> 1 : 0);
  const unsigned fW = FL_W + (fkb * 5 + ((5 * c.ct + (lane & 1) * 4) >> 3)) * 128;
  unsigned ro[5], co[5];
#pragma unroll
  for (int k = 0; k < 5; ++k) {
    ro[k] = OFF_RS + (c.rt * 10 + 2 * k + (lane >> 5)) * 2048 + (32 * lw + (lane & 31)) * 16;
    co[k] = OFF_CS + (c.cg * 10 + 2 * k + (lane >> 5)) * 2048 + (32 * (lw < NP ? lw : 0) + (lane & 31)) * 16;
  }
  auto stage = [&](auto S) {
    constexpr int s = decltype(S)::value;
    if constexpr (s == 0) {
#pragma unroll
      for (int i = 0; i < NP; ++i) {
        const int tl = NP * lw + i;
        const unsigned wq = lds0 + 16384 + (tl >> 2) * 8192 + (8 * g + q4) * 128 + (((tl & 3) ^ sigl) << 5) + p4 * 8;
        sw_[i][0] = cat44(trread(wq), trread(wq + 512));
        sw_[i][1] = cat44(trread(wq + 4096), trread(wq + 4608));
      }
#pragma unroll
      for (int i = 0; i < 2; ++i) {
        const unsigned xq = lds0 + (32 * lw + 16 * i + l15) * 128;
        sx_[i][0] = *(LAS bf16x8*)(size_t)(xq + (xsw << 4));
        sx_[i][1] = *(LAS bf16x8*)(size_t)(xq + ((xsw ^ 4) << 4));
      }
    }
    if constexpr (s < 9) wait_vm<s == 6 ? D + 1 : (s == 7 ? D + 10 : (s == 8 ? 10 + 5 * WOPRE : D))>();
    WAIT_LGKM0();
    BAR();
    if constexpr (s < 7) issue(IC<s + 3>{});
    if constexpr (s == 2) {
      u32x4 cu = {0u, 0u, 0u, 0u};
#pragma unroll
      for (int i = 0; i < NP; ++i) {
        const u32x4 ci = __builtin_bit_cast(u32x4, mfma16(sw_[i][1], ONE8, mfma16(sw_[i][0], ONE8, z4)));
        if (l15 == i) cu = ci;
      }
      if (l15 < NP) {
        const unsigned o = OFF_CS + (c.cg * 10 + c.rot) * 2048 + NP * lw * 128;
        st16(u32x4{cu[0], tg, cu[1], tg}, rs, l15 * 128 + g * 32, o);
        st16(u32x4{cu[2], tg, cu[3], tg}, rs, l15 * 128 + g * 32 + 16, o);
      }
    }
    if constexpr (s == 3) {
#pragma unroll
      for (int i = 0; i < 2; ++i) {
        const f32x4 sx = mfma16(ONE8, sx_[i][1], mfma16(ONE8, sx_[i][0], z4));
        const f32x4 sq = mfma16(sx_[i][1], sx_[i][1], mfma16(sx_[i][0], sx_[i][0], z4));
        const int r = l15 & 3;
        const float sxx = r == 0 ? sq[0] : (r == 1 ? sq[1] : (r == 2 ? sq[2] : sq[3]));
        if (q4 == g)
          st16(u32x4{__float_as_uint(sx[0]), tg, __float_as_uint(sxx), tg}, rs,
                                                 (32 * lw + 16 * i + l15) * 16, OFF_RS + (c.rt * 10 + c.rot) * 2048);
      }
    }
    if constexpr (s < 2) {
      if (do_rt) {
        const unsigned dstb = rkb < 24 ? OFF_W2F : OFF_WOF;
        const int nks = rkb < 24 ? 48 : 20, kb = rkb < 24 ? rkb : rkb - 24;
        {
          const int nl = 2 * lw + s, nt = 8 * rnb + nl, c5 = nt / 5, jj = nt - 5 * c5;
          const unsigned rp = lds0 + RT_OFF + (nl >> 2) * 8192 + (8 * g + q4) * 128 + (((nl & 3) ^ sigl) << 5) + p4 * 8;
#pragma unroll
          for (int ks = 0; ks < 2; ++ks) {
            const bf16x8 f = cat44(trread(rp + ks * 4096), trread(rp + ks * 4096 + 512));
            st16(__builtin_bit_cast(u32x4, f), rs, lane * 16,
                                                   dstb + ((c5 * nks + 2 * kb + ks) * 5 + jj) * 1024);
          }
        }
      }
    }
    if constexpr (s == 2) {
      if (lt < 8) *(LAS unsigned*)(size_t)(lds0 + SYNC_OFF + lt * 4) = 0u;
      if (NP == 4 && lt < 32) dma1(c.b1 + (c.cg - 10) * 512, lane * 16, lds0 + B1_OFF);
    }
    if constexpr (s == 3) {
      if (lt == 0) __hip_atomic_store((GAS u64*)(ws + FL_W + c.b * 128), tag, RLX_AGENT);
    }
    if constexpr (s == 6) {
#pragma unroll
      for (int k = 0; k < 5; ++k) { gr[k] = aload16(ws, ro[k]); gc[k] = aload16(ws, co[k]); }
    }
    if constexpr (s == 5) fl = aload8(ws, fW);
    if constexpr (s == 7) {
      PIN(fl);
      if (!__all(fl[0] == tg && fl[1] == (unsigned)(tag >> 32))) poll_flags(ws + fW, true, tag);
#pragma unroll
      for (int k = 0; k < WOPRE; ++k)
#pragma unroll
        for (int j = 0; j < 5; ++j) wpre[k][j] = ld16(rs, lane * 16, wb + k * 5120 + j * 1024);
    }
    if constexpr (s == 8) {
#pragma unroll
      for (int k = 0; k < 4; ++k)
        dma5_sc1(ws + OFF_W2F + (c.ct * 48 + 12 * lw + 4 + k) * 5120, lane * 16, lds0 + SLAB_OFF + lw * 24576 + k * 5120);
    }
  };
  stage(IC<0>{}); stage(IC<1>{}); stage(IC<2>{}); stage(IC<3>{}); stage(IC<4>{});
  stage(IC<5>{}); stage(IC<6>{}); stage(IC<7>{}); stage(IC<8>{}); stage(IC<9>{});
  wait_vm<5 * WOPRE + 20>();
  {
#pragma unroll
      for (int k = 0; k < 5; ++k) { PIN(gr[k]); PIN(gc[k]); }
      float sx, sxx, c0, c1;
      if (!sum_pairs<5>(gr, 0xffffffffu, tg, sx, sxx)) regather<5>(rs, ro, 0xffffffffu, tg, sx, sxx);
      if (!sum_pairs<5>(gc, 0xffffffffu, tg, c0, c1)) regather<5>(rs, co, 0xffffffffu, tg, c0, c1);
      sx += __shfl_xor(sx, 32); sxx += __shfl_xor(sxx, 32);
      c0 += __shfl_xor(c0, 32); c1 += __shfl_xor(c1, 32);
      const float mean = sx * (1.f / 640.f);
      float var = sxx * (1.f / 640.f) - mean * mean;
      var = var < 0.f ? 0.f : var;
      if (lane < 32) {
        *(LAS f32x2*)(size_t)(lds0 + ST_OFF + (32 * lw + lane) * 8) = f32x2{mean, rsqrtf(var + 1e-5f)};
        *(LAS f32x2*)(size_t)(lds0 + CS_OFF + (32 * lw + lane) * 8) = f32x2{c0, c1};
      }
    }
  WAIT_LGKM0();
  lds_arrive((LAS unsigned*)(size_t)(lds0 + SYNC_OFF + 28));
}

__global__ __launch_bounds__(512, 2) void k_all(const char* __restrict__ x, const char* __restrict__ wqkv, const char* __restrict__ w1,
                                                const char* __restrict__ b1, const char* __restrict__ w2, const char* __restrict__ wo,
                                                const char* __restrict__ b2, char* ws, char* __restrict__ out) {
  extern __shared__ __attribute__((aligned(16))) char smem_[];
  LAS char* smem = (LAS char*)smem_;
  const int tid = threadIdx.x, lane = tid & 63, w = __builtin_amdgcn_readfirstlane(tid >> 6);
  const int b = blockIdx.x, xcd = b & 7, idx = b >> 3;
  const int g = lane >> 4, l15 = lane & 15, q4 = l15 >> 2, p4 = lane & 3;
  const unsigned lds0 = (unsigned)(size_t)smem;
  asm volatile("" :: "s"(x), "s"(wqkv), "s"(w1), "s"(b1), "s"(w2), "s"(wo), "s"(ws));
  u64 pc_;
  asm volatile("s_getpc_b64 %0" : "=s"(pc_));
  const char* pc0 = (const char*)(pc_ & ~(u64)63);
  const u64 rt0_ = __builtin_amdgcn_s_memrealtime();
  u64 tag;
  const __amdgpu_buffer_rsrc_t rs = __builtin_amdgcn_make_buffer_rsrc(ws, 0, WS_USED, 0x00020000);
  LAS unsigned* sync = (LAS unsigned*)(smem + SYNC_OFF);
  STAMP(0);
  const int rt = (xcd & 1) * 8 + (idx & 7), cg = (xcd >> 1) * 4 + (idx >> 3);
  const int mt = (xcd >> 1) * 8 + (idx & 7), ct = (xcd & 1) * 4 + (idx >> 3);
  const int head = 4 * xcd + (idx & 3), qb = idx >> 2;
  const int ch = (5 * head) >> 1, odd = head & 1;
  const Ctx c = {x, cg < 10 ? wqkv : w1, b1, w2, wo, ws, lds0, b, rt, cg, (rt + 3 * cg) % 10, ct, qb, ch, w, lane};
  const size_t orow = (size_t)(64 * mt + 16 * (w & 3) + l15) * 1280 + (80 * ct + 4 * g) * 2;
  u32x2 xv[3];
  f32x4 acc[4][5];
  const unsigned va = g * 1024 + l15 * 16;
  bf16x8 wpre[5][5];
  const unsigned wb = w < 4 ? OFF_W2F + (ct * 48 + 12 * w) * 5120 : OFF_WOF + (ct * 20 + 5 * (w - 4)) * 5120;

  if (__builtin_expect(w >= 4, 1)) {
    __builtin_amdgcn_s_waitcnt(0);
    __builtin_amdgcn_s_setprio(3);
    if (cg < 10) load_phase<3>(c, rs, tag, wpre, wb); else load_phase<4>(c, rs, tag, wpre, wb);
    STAMP(2);
    const int lw = w - 4, lt = tid - 256;
    const f32x4 z4 = {0.f, 0.f, 0.f, 0.f};
    {
      const u32x4 one4 = {0x3f803f80u, 0x3f803f80u, 0x3f803f80u, 0x3f803f80u};
      *(LAS u32x4*)(smem + V_OFF + 3 * VP + lt * 16) = one4;
      if (lt < 40) *(LAS u32x4*)(smem + V_OFF + 3 * VP + 4096 + lt * 16) = one4;
      *(LAS u32x4*)(smem + K_OFF + 3 * KP + lt * 16) = u32x4{qb == 0 ? 0xff80u : 0u, 0u, 0u, 0u};
      *(LAS u32x4*)(smem + K_OFF + 3 * KP + 4096 + lt * 16) = u32x4{0u, 0u, 0u, 0u};
      const int ty = lane < 24 ? lane >> 3 : 0, rr0 = 2 * qb - 2 + ((lane >> 1) & 3);
      const int rr = rr0 < 0 ? 0 : (ty == 0 && rr0 < 2 * qb ? 2 * qb : rr0);
      poll_flags(ws + FL_A + wg_of(rr, (80 * ty + ch + (lane & 1) * 2) / 24) * 128, true, tag);
    }
    STAMP(11);
#pragma unroll
    for (int i = 0; i < 2; ++i) {
      const int seg = 2 * lw + i;
      int tb = 4 * qb - 4 + seg; tb = tb < 0 ? 0 : tb;
      dma_kv(ws + OFF_QKV + (size_t)(tb * 240 + 80 + ch) * 1024, lane * 16, lane * 16 + 81920, lds0 + seg * 1024);
    }
    const int l31 = lane & 31, hh = lane >> 5;
    u32x4 qraw[2][2];
#pragma unroll
    for (int a = 0; a < 2; ++a)
#pragma unroll
      for (int ks = 0; ks < 2; ++ks)
        qraw[a][ks] = __builtin_amdgcn_raw_buffer_load_b128(rs, (ks ? 2 : hh) * 1024 + (32 * a + l31) * 16,
                                                            OFF_QKV + ((4 * qb + lw) * 240 + ch) * 1024, SC1);
    lds_arrive(sync + 5);
    bf16x8 qf[2][2];
#pragma unroll
    for (int a = 0; a < 2; ++a) {
      u32x4 v0 = qraw[a][0], v1 = qraw[a][1];
      if (odd && hh == 0) { v0[0] = 0u; v0[1] = 0u; }
      if (hh) v1 = u32x4{0x3f80u, 0u, 0u, 0u};
      else if (!odd) { v1[2] = 0u; v1[3] = 0u; }
      qf[a][0] = __builtin_bit_cast(bf16x8, v0);
      qf[a][1] = __builtin_bit_cast(bf16x8, v1);
    }
    f32x16 mF, z16;
#pragma unroll
    for (int e = 0; e < 16; ++e) {
      mF[e] = (e & 3) + 8 * (e >> 2) + 4 * hh > l31 ? 0.f : -INFINITY;
      z16[e] = 0.f;
    }
    asm volatile("" : "+v"(mF), "+v"(qf[0][0]), "+v"(qf[0][1]), "+v"(qf[1][0]), "+v"(qf[1][1]));
    WAIT_VM(0);
    WAIT_LGKM0();
    lds_arrive(sync);
    lds_wait(sync, 4);
    __builtin_amdgcn_s_setprio(2);
    STAMP(5);
    unsigned kp = lds0 + K_OFF + hh * KP + (64 * lw + l31) * 16;
    const bool ones = (g & 1) && (p4 >> 1);
    unsigned vp = lds0 + V_OFF + (2 * (g & 1) + (p4 >> 1)) * VP + ((ones ? 0 : 64 * lw) + 4 * hh + q4) * 16 + (p4 & 1) * 8;
    unsigned ov = l31 * 16 + hh * 8;
    bf16x8 q0 = qf[0][0], q1 = qf[0][1];
#define LDK(T, ks) (*(LAS bf16x8*)(size_t)(kp + (T) * 512 + (ks) * 2 * KP))
#define MF32(a, b, c) __builtin_amdgcn_mfma_f32_32x32x16_bf16(a, b, c, 0, 0, 0)
#define EX2(s_, e) pk2(__builtin_amdgcn_exp2f((s_)[e]), __builtin_amdgcn_exp2f((s_)[(e) + 1]))
#pragma unroll 1
    for (int un = 0; un < 2; ++un) {
      float m0 = -INFINITY, m1 = -INFINITY;
      {
        bf16x8 kf[9][2];
#pragma unroll
        for (int T = 0; T < 3; ++T) { kf[T][0] = LDK(T, 0); kf[T][1] = LDK(T, 1); }
        f32x16 sp = z16;
#pragma unroll
        for (int T = 0; T < 10; ++T) {
          f32x16 sn = z16;
          if (T + 3 < 9) { kf[T + 3][0] = LDK(T + 3, 0); kf[T + 3][1] = LDK(T + 3, 1); }
          if (T < 9) sn = MF32(kf[T][1], q1, MF32(kf[T][0], q0, T == 0 ? mF : z16));
          if (T == 9) {
#pragma unroll
            for (int e = 0; e < 16; ++e) sp[e] = mF[e] == 0.f ? -INFINITY : sp[e];
          }
          if (T > 0) {
#pragma unroll
            for (int e = 0; e < 16; e += 4) { m0 = MAX3(m0, sp[e], sp[e + 1]); m1 = MAX3(m1, sp[e + 2], sp[e + 3]); }
          }
          SGB(0x100, 2); SGB(0x8, 1); SGB(0x2, 4); SGB(0x8, 1); SGB(0x2, 4);
          asm volatile("" :: "v"(sn), "v"(m0), "v"(m1));
          SB();
          sp = sn;
        }
      }
      float mx = fmaxf(m0, m1);
      mx = fmaxf(mx, __shfl_xor(mx, 32));
      f32x16 ca;
#pragma unroll
      for (int e = 0; e < 16; ++e) ca[e] = -mx;
      f32x16 oa = z16;
      {
        bf16x8 kc[2][2], vf[2][2];
        f32x16 sc[2];
        u32x4 pa[2] = {{0u, 0u, 0u, 0u}, {0u, 0u, 0u, 0u}};
        kc[0][0] = LDK(0, 0); kc[0][1] = LDK(0, 1); kc[1][0] = LDK(1, 0); kc[1][1] = LDK(1, 1);
        sc[0] = MF32(kc[0][1], q1, MF32(kc[0][0], q0, ca + mF));
        sc[1] = z16;
        asm volatile("" :: "v"(sc[0]));
        SB();
#pragma unroll
        for (int T = 0; T < 10; ++T) {
          const int c = T & 1, n = c ^ 1;
          if (T < 9) {
#pragma unroll
            for (int ks = 0; ks < 2; ++ks) vf[c][ks] = cat44(trread(vp + T * 512 + ks * 256), trread(vp + T * 512 + ks * 256 + 128));
          }
          if (T < 8) sc[n] = MF32(kc[n][1], q1, MF32(kc[n][0], q0, ca));
          if (T < 7) { kc[c][0] = LDK(T + 2, 0); kc[c][1] = LDK(T + 2, 1); }
          if (T > 0) oa = MF32(vf[n][1], __builtin_bit_cast(bf16x8, pa[1]), MF32(vf[n][0], __builtin_bit_cast(bf16x8, pa[0]), oa));
          if (T == 8) {
#pragma unroll
            for (int e = 0; e < 16; ++e) sc[c][e] = mF[e] == 0.f ? -INFINITY : sc[c][e];
          }
          if (T < 9) {
#pragma unroll
            for (int e = 0; e < 4; ++e) { pa[0][e] = EX2(sc[c], 2 * e); pa[1][e] = EX2(sc[c], 8 + 2 * e); }
          }
          SGB(0x100, 6);
          if (T == 8) SGB(0x2, 16);
          SGB(0x8, 1); SGB(0x400, 4); SGB(0x2, 1);
#pragma unroll
          for (int k = 0; k < 3; ++k) { SGB(0x8, 1); SGB(0x400, 2); SGB(0x2, 1); SGB(0x400, 2); SGB(0x2, 1); }
          SGB(0x2, 1);
          asm volatile("" :: "v"(sc[n]), "v"(oa), "v"(pa[0]), "v"(pa[1]));
          SB();
        }
      }
      const float il = __builtin_amdgcn_rcpf(oa[12]);
#pragma unroll
      for (int pl = 0; pl < 3; ++pl) {
        const bool ok = pl == 1 || (pl == 0 ? !(odd && hh == 0) : (odd || hh == 0));
        if (ok) {
          const u32x2 pkd = {pk2(oa[4 * pl] * il, oa[4 * pl + 1] * il), pk2(oa[4 * pl + 2] * il, oa[4 * pl + 3] * il)};
          __builtin_amdgcn_raw_buffer_store_b64(pkd, rs, pl * 1024 + ov, OFF_O + ((4 * qb + lw) * 80 + ch) * 1024, SC1);
        }
      }
      kp += 512; vp += ones ? 0 : 512; ov += 512;
      q0 = qf[1][0]; q1 = qf[1][1];
    }
    __builtin_amdgcn_s_setprio(0);
    lds_arrive(sync + 4);
    STAMP(6);
#pragma unroll
    for (int k = WOPRE; k < 5; ++k)
#pragma unroll
      for (int j = 0; j < 5; ++j) wpre[k][j] = ld16(rs, lane * 16, wb + k * 5120 + j * 1024);
#pragma unroll
    for (int j = 0; j < 2; ++j) xv[j] = *(const u32x2*)(x + orow + (3 + j) * 32);
    wait_vm<5 * (5 - WOPRE) + 2>();
    const bool last_o = lds_arrive(sync + 1) == 3;
    if (last_o && lane == 0) __hip_atomic_store((GAS u64*)(ws + FL_O + b * 128), tag, RLX_AGENT);
    const unsigned ab = OFF_O + (mt * 80 + 20 * lw) * 1024;
    {
      const int hd = 8 * lw + (lane & 7);
      poll_flags(ws + FL_O + (((((mt >> 2) << 2) | (hd & 3)) << 3) | (hd >> 2)) * 128, lane < 8, tag);
    }
    STAMP(7);
    bf16x8 af[3][4];
#pragma unroll
    for (int k = 0; k < 3; ++k)
#pragma unroll
      for (int i = 0; i < 4; ++i) af[k][i] = ld16(rs, va + i * 256, ab + k * 4096);
    SB();
#pragma unroll
    for (int i = 0; i < 4; ++i)
#pragma unroll
      for (int j = 0; j < 5; ++j) acc[i][j] = z4;
#pragma unroll
    for (int k = 0; k < 5; ++k) {
#pragma unroll
      for (int i = 0; i < 4; ++i)
#pragma unroll
        for (int j = 0; j < 5; ++j) acc[i][j] = mfma16(wpre[k][j], af[k % 3][i], acc[i][j]);
      SB();
      if (k < 2) {
#pragma unroll
        for (int i = 0; i < 4; ++i) af[k][i] = ld16(rs, va + i * 256, ab + (k + 3) * 4096);
        SB();
      }
    }
    STAMP(8);
    lds_wait(sync + 1, 4);
    lds_wait(sync + 6, 4);
#pragma unroll
    for (int i = 0; i < 4; ++i) {
#pragma unroll
      for (int j = 0; j < 3; ++j) *(LAS f32x4*)(smem + REDS1_OFF + ((i * 4 + lw) * 3 + j) * 1024 + lane * 16) = acc[i][j];
      if (i != lw) {
#pragma unroll
        for (int j = 0; j < 2; ++j)
          *(LAS f32x4*)(smem + REDS2_OFF + ((i * 3 + lw - (lw > i ? 1 : 0)) * 2 + j) * 1024 + lane * 16) = acc[i][3 + j];
      }
    }
    __syncthreads();
    STAMP(9);
#pragma unroll
    for (int j = 0; j < 2; ++j) {
      f32x4 sv = lw == 0 ? acc[0][3 + j] : (lw == 1 ? acc[1][3 + j] : (lw == 2 ? acc[2][3 + j] : acc[3][3 + j]));
#pragma unroll
      for (int k = 0; k < 3; ++k) sv += *(LAS f32x4*)(smem + REDS2_OFF + ((lw * 3 + k) * 2 + j) * 1024 + lane * 16);
      const f32x4 gt = *(LAS f32x4*)(smem + GX_OFF + (lw * 2 + j) * 1024 + lane * 16);
      *(u32x2*)(out + orow + (3 + j) * 32) = fin4(xv[j], sv, gt);
    }
    if (b == 0 && w == 4) {
#pragma unroll
      for (int i = 0; i < 4; ++i) poll_flags(ws + FL_W + (lane + 64 * i) * 128, true, tag);
      if (lane == 0) __hip_atomic_store((GAS u64*)(ws + OFF_FL), tag, RLX_AGENT);
    }
  } else {
    if (idx * 4 + w < CODE_KB) {
      const u32x4 t0 = *(const u32x4*)(pc0 + (idx * 4 + w) * 1024 + lane * 16);
      asm volatile("" :: "v"(t0));
    }
    {
      const char* p = ws;
      asm volatile("" : "+s"(p));
      tag = *(const CAS u64*)(p + OFF_FL) + 1;
    }
    if (cg < 10) mfma_phase<3>(c, rs); else mfma_phase<4>(c, rs);
    STAMP(3);
    WAIT_VM(0);
    if (lds_arrive(sync + 2) == 3 && lane == 0) __hip_atomic_store((GAS u64*)(ws + FL_A + b * 128), tag, RLX_AGENT);
    STAMP(4);
#pragma unroll
    for (int k = 0; k < 4; ++k)
#pragma unroll
      for (int j = 0; j < 5; ++j) wpre[k][j] = ld16(rs, lane * 16, wb + k * 5120 + j * 1024);
    const unsigned wl_ = lds0 + SLAB_OFF + w * 24576;
    const unsigned ab = OFF_H + (mt * 192 + 48 * w) * 1024;
    poll_flags(ws + FL_A + wg_of(mt >> 1, 10 + (24 * w + (lane & 1) * 23) / 16) * 128, lane < 2, tag);
    lds_wait(sync + 5, 4);
    STAMP(7);
    bf16x8 af[3][4];
#pragma unroll
    for (int k = 0; k < 3; ++k)
#pragma unroll
      for (int i = 0; i < 4; ++i) af[k][i] = ld16(rs, va + i * 256, ab + k * 4096);
    SB();
#pragma unroll
    for (int i = 0; i < 4; ++i)
#pragma unroll
      for (int j = 0; j < 5; ++j) acc[i][j] = f32x4{0.f, 0.f, 0.f, 0.f};
    STAMP(12);
#pragma unroll
    for (int k = 0; k < 12; ++k) {
      const int cs = k < 4 ? k : (k < 8 ? 4 - (k & 1) : (k < 11 ? k - 8 : 4));
      if (k >= 3 && k < 7) {
#pragma unroll
        for (int j = 0; j < 5; ++j) wpre[4 - ((k + 1) & 1)][j] = *(LAS bf16x8*)(size_t)(wl_ + (k - 3) * 5120 + j * 1024 + lane * 16);
      }
#pragma unroll
      for (int i = 0; i < 4; ++i)
#pragma unroll
        for (int j = 0; j < 5; ++j) acc[i][j] = mfma16(wpre[cs][j], af[k % 3][i], acc[i][j]);
      SB();
      if (k + 3 < 12) {
#pragma unroll
        for (int i = 0; i < 4; ++i) af[k % 3][i] = ld16(rs, va + i * 256, ab + (k + 3) * 4096);
      }
      if (k < 3 || k == 6) {
#pragma unroll
        for (int j = 0; j < 5; ++j) wpre[k < 3 ? k : 4][j] = ld16(rs, lane * 16, wb + (k < 3 ? k + 8 : 11) * 5120 + j * 1024);
      }
      SB();
    }
    lds_arrive(sync + 6);
    lds_wait(sync + 6, 4);
    STAMP(8);
    red_write(smem + REDG_OFF, w, lane, acc);
    u32x2 bb[5];
#pragma unroll
    for (int j = 0; j < 5; ++j) {
      if (j < 3) xv[j] = *(const u32x2*)(x + orow + j * 32);
      bb[j] = *(const u32x2*)(b2 + (80 * ct + 16 * j + 4 * g) * 2);
    }
    WAIT_LGKM0();
    lds_arrive(sync + 3);
    lds_wait(sync + 3, 4);
    f32x4 gt[5];
    red_sum(smem + REDG_OFF, w, lane, acc, gt);
#pragma unroll
    for (int j = 0; j < 5; ++j) {
      const float bv[4] = {bflo(bb[j][0]), bfhi(bb[j][0]), bflo(bb[j][1]), bfhi(bb[j][1])};
#pragma unroll
      for (int e = 0; e < 4; ++e) {
        const float t = rbf(rbf(gt[j][e]) + bv[e]);
        gt[j][e] = rbf(t * __builtin_amdgcn_rcpf(1.f + __expf(-t)));
      }
      if (j >= 3) *(LAS f32x4*)(smem + GX_OFF + (w * 2 + j - 3) * 1024 + lane * 16) = gt[j];
    }
    __syncthreads();
    STAMP(9);
#pragma unroll
    for (int j = 0; j < 3; ++j) {
      f32x4 sv = *(LAS f32x4*)(smem + REDS1_OFF + ((w * 4) * 3 + j) * 1024 + lane * 16);
#pragma unroll
      for (int k = 1; k < 4; ++k) sv += *(LAS f32x4*)(smem + REDS1_OFF + ((w * 4 + k) * 3 + j) * 1024 + lane * 16);
      *(u32x2*)(out + orow + j * 32) = fin4(xv[j], sv, gt[j]);
    }
  }
  STAMP(10);
}

extern "C" void kernel_launch(void* const* d_in, const int* in_sizes, int n_in, void* d_out, int out_size, void* d_ws,
                              size_t ws_size, hipStream_t stream) {
  (void)hipFuncSetAttribute((const void*)k_all, hipFuncAttributeMaxDynamicSharedMemorySize, LDS_ALL);
  k_all<<<256, 512, LDS_ALL, stream>>>((const char*)d_in[0], (const char*)d_in[1], (const char*)d_in[4], (const char*)d_in[5],
                                       (const char*)d_in[6], (const char*)d_in[2], (const char*)d_in[7], (char*)d_ws, (char*)d_out);
}
```

```cpp
#include <hip/hip_runtime.h>
#include <stdint.h>

#define LAS __attribute__((address_space(3)))
#define GAS __attribute__((address_space(1)))
#define CAS __attribute__((address_space(4)))
typedef short bf16x8 __attribute__((ext_vector_type(8)));
typedef short s16x4 __attribute__((ext_vector_type(4)));
typedef float f32x4 __attribute__((ext_vector_type(4)));
typedef unsigned u32x4 __attribute__((ext_vector_type(4)));
typedef unsigned u32x2 __attribute__((ext_vector_type(2)));
typedef __bf16 bf16x2_t __attribute__((ext_vector_type(2)));
typedef float f32x2 __attribute__((ext_vector_type(2)));
typedef float f32x16 __attribute__((ext_vector_type(16)));
typedef unsigned long long u64;

constexpr unsigned OFF_FL = 0;
constexpr unsigned FL_W = 1024, FL_A = FL_W + 32768, FL_O = FL_A + 32768;
constexpr unsigned OFF_RS = 131072;
constexpr unsigned OFF_CS = OFF_RS + 16 * 10 * 2048;
constexpr unsigned OFF_QKV = OFF_CS + 16 * 10 * 2048;
constexpr unsigned OFF_H = OFF_QKV + 32 * 240 * 1024;
constexpr unsigned OFF_O = OFF_H + 32 * 192 * 1024;
constexpr unsigned OFF_W2F = OFF_O + 32 * 80 * 1024;
constexpr unsigned OFF_WOF = OFF_W2F + 8 * 48 * 5120;
constexpr unsigned WS_USED = OFF_WOF + 8 * 20 * 5120;

__device__ __forceinline__ f32x4 mfma16(bf16x8 a, bf16x8 b, f32x4 c) {
  return __builtin_amdgcn_mfma_f32_16x16x32_bf16(a, b, c, 0, 0, 0);
}
__device__ __forceinline__ unsigned pk2(float a, float b) {
  f32x2 v = {a, b};
  bf16x2_t r = __builtin_convertvector(v, bf16x2_t);
  return __builtin_bit_cast(unsigned, r);
}
__device__ __forceinline__ float rbf(float a) { return __builtin_bit_cast(float, pk2(0.f, a) & 0xffff0000u); }
__device__ __forceinline__ float bflo(unsigned u) { return __builtin_bit_cast(float, u << 16); }
__device__ __forceinline__ float bfhi(unsigned u) { return __builtin_bit_cast(float, u & 0xffff0000u); }
__device__ __forceinline__ u32x2 fin4(u32x2 xv, f32x4 sv, f32x4 gt) {
  return u32x2{pk2(bflo(xv[0]) + rbf(rbf(sv[0]) * gt[0]), bfhi(xv[0]) + rbf(rbf(sv[1]) * gt[1])),
               pk2(bflo(xv[1]) + rbf(rbf(sv[2]) * gt[2]), bfhi(xv[1]) + rbf(rbf(sv[3]) * gt[3]))};
}
__device__ __forceinline__ s16x4 trread(unsigned a) {
  return __builtin_amdgcn_ds_read_tr16_b64_v4i16((LAS s16x4*)(size_t)a);
}
__device__ __forceinline__ bf16x8 cat44(s16x4 a, s16x4 b) {
  return __builtin_shufflevector(a, b, 0, 1, 2, 3, 4, 5, 6, 7);
}
#define WAIT_VM(n) asm volatile("s_waitcnt vmcnt(" #n ")" ::: "memory")
#define WAIT_LGKM0() __builtin_amdgcn_s_waitcnt(0xC07F)
#define BAR() __builtin_amdgcn_s_barrier()
#define SB() __builtin_amdgcn_sched_barrier(0)
#define SGB(m, n) __builtin_amdgcn_sched_group_barrier(m, n, 0)
#define MAX3(a, b, c) __builtin_fmaxf(__builtin_fmaxf(a, b), c)
#define RLX_AGENT __ATOMIC_RELAXED, __HIP_MEMORY_SCOPE_AGENT
constexpr int SC1 = 16;
constexpr int CODE_KB = 48;
template <int N> struct IC { static constexpr int value = N; };

#define STAMP(i)
#define CLK(v)

__device__ __forceinline__ void dma1(const char* sbase, unsigned voff, unsigned lds) {
  unsigned keep;
  asm volatile("s_mov_b32 %0, m0\n\ts_mov_b32 m0, %3\n\ts_nop 0\n\tglobal_load_lds_dwordx4 %1, %2\n\ts_mov_b32 m0, %0"
               : "=&s"(keep) : "v"(voff), "s"(sbase), "s"(lds) : "memory");
}
#define DMA_W2(ofs) "s_add_u32 m0, m0, 7040\n\ts_nop 0\n\tglobal_load_lds_dwordx4 %8, %7 offset:" #ofs "\n\t" \
                    "s_add_u32 m0, m0, 1024\n\ts_nop 0\n\tglobal_load_lds_dwordx4 %9, %7 offset:" #ofs "\n\t"
#define DMA_HEAD "s_mov_b32 %0, m0\n\ts_mov_b32 m0, %10\n\ts_nop 0\n\tglobal_load_lds_dwordx4 %8, %7\n\t" \
                 "s_add_u32 m0, m0, 1024\n\ts_nop 0\n\tglobal_load_lds_dwordx4 %9, %7\n\t" DMA_W2(128) DMA_W2(256)
#define DMA_X "s_mov_b32 m0, %6\n\ts_nop 0\n\tglobal_load_lds_dwordx4 %2, %1\n\t" \
              "s_add_u32 m0, m0, 1024\n\ts_nop 0\n\tglobal_load_lds_dwordx4 %3, %1\n\t" \
              "s_add_u32 m0, m0, 1024\n\ts_nop 0\n\tglobal_load_lds_dwordx4 %4, %1\n\t" \
              "s_add_u32 m0, m0, 1024\n\ts_nop 0\n\tglobal_load_lds_dwordx4 %5, %1\n\ts_mov_b32 m0, %0"
#define DMA_OPS : "=&s"(keep) : "s"(sa), "v"(va0), "v"(va1), "v"(va2), "v"(va3), "s"(la), "s"(sb), "v"(vb0), "v"(vb1), "s"(lb) : "memory", "scc"
template <int NP>
__device__ __forceinline__ void dma_stage(const char* sa, unsigned va0, unsigned va1, unsigned va2, unsigned va3, unsigned la,
                                          const char* sb, unsigned vb0, unsigned vb1, unsigned lb) {
  unsigned keep;
  if constexpr (NP == 3) asm volatile(DMA_HEAD DMA_X DMA_OPS);
  else asm volatile(DMA_HEAD DMA_W2(384) DMA_X DMA_OPS);
}
__device__ __forceinline__ void dma_kv(const char* sb, unsigned vk, unsigned vv, unsigned l) {
  unsigned keep;
  asm volatile("s_mov_b32 %0, m0\n\ts_mov_b32 m0, %4\n\ts_nop 0\n\tglobal_load_lds_dwordx4 %2, %1 sc1\n\t"
               "s_add_u32 m0, m0, 7168\n\ts_nop 0\n\tglobal_load_lds_dwordx4 %2, %1 offset:1024 sc1\n\t"
               "s_add_u32 m0, m0, 7168\n\ts_nop 0\n\tglobal_load_lds_dwordx4 %2, %1 offset:2048 sc1\n\t"
               "s_add_u32 m0, m0, 18432\n\ts_nop 0\n\tglobal_load_lds_dwordx4 %3, %1 sc1\n\t"
               "s_add_u32 m0, m0, 7232\n\ts_nop 0\n\tglobal_load_lds_dwordx4 %3, %1 offset:1024 sc1\n\t"
               "s_add_u32 m0, m0, 7232\n\ts_nop 0\n\tglobal_load_lds_dwordx4 %3, %1 offset:2048 sc1\n\t"
               "s_mov_b32 m0, %0"
               : "=&s"(keep) : "s"(sb), "v"(vk), "v"(vv), "s"(l) : "memory", "scc");
}
__device__ __forceinline__ void dma5_sc1(const char* sb, unsigned v, unsigned l) {
  unsigned keep;
  asm volatile("s_mov_b32 %0, m0\n\ts_mov_b32 m0, %3\n\ts_nop 0\n\tglobal_load_lds_dwordx4 %2, %1 sc1\n\t"
               "global_load_lds_dwordx4 %2, %1 offset:1024 sc1\n\tglobal_load_lds_dwordx4 %2, %1 offset:2048 sc1\n\t"
               "global_load_lds_dwordx4 %2, %1 offset:3072 sc1\n\ts_add_u32 m0, m0, 1024\n\ts_nop 0\n\t"
               "global_load_lds_dwordx4 %4, %1 offset:3072 sc1\n\ts_mov_b32 m0, %0"
               : "=&s"(keep) : "s"(sb), "v"(v), "s"(l), "v"(v + 1024) : "memory", "scc");
}
__device__ __forceinline__ u32x2 aload8(const char* sbase, unsigned voff) {
  u32x2 r;
  asm volatile("global_load_dwordx2 %0, %1, %2 sc1" : "=v"(r) : "v"(voff), "s"(sbase) : "memory");
  return r;
}
__device__ __forceinline__ u32x4 aload16(const char* sbase, unsigned voff) {
  u32x4 r;
  asm volatile("global_load_dwordx4 %0, %1, %2 sc1" : "=v"(r) : "v"(voff), "s"(sbase) : "memory");
  return r;
}
#define PIN(x) asm volatile("" : "+v"(x))
template <int N> __device__ __forceinline__ void wait_vm() { asm volatile("s_waitcnt vmcnt(%0)" :: "i"(N) : "memory"); }
__device__ __forceinline__ void poll_flags(const char* flag, bool need, u64 tag) {
#pragma unroll 1
  for (int spins = 0; spins < (1 << 20); ++spins) {
    const u64 v = need ? __hip_atomic_load((const GAS u64*)flag, RLX_AGENT) : tag;
    if (__all(v == tag)) break;
    __builtin_amdgcn_s_sleep(1);
  }
  __builtin_amdgcn_fence(__ATOMIC_ACQUIRE, "wavefront");
}
__device__ __forceinline__ unsigned lds_arrive(LAS unsigned* c) {
  unsigned r = 0;
  if ((threadIdx.x & 63) == 0) r = __hip_atomic_fetch_add(c, 1u, __ATOMIC_RELAXED, __HIP_MEMORY_SCOPE_WORKGROUP);
  return __builtin_amdgcn_readfirstlane(r);
}
__device__ __forceinline__ void lds_wait_sleep(LAS unsigned* c, unsigned n) {
#pragma unroll 1
  for (int spins = 0; spins < (1 << 22); ++spins) {
    if (__builtin_amdgcn_readfirstlane(__hip_atomic_load(c, __ATOMIC_RELAXED, __HIP_MEMORY_SCOPE_WORKGROUP)) >= n) break;
    __builtin_amdgcn_s_sleep(2);
  }
  __builtin_amdgcn_fence(__ATOMIC_ACQUIRE, "wavefront");
}
__device__ __forceinline__ void lds_wait(LAS unsigned* c, unsigned n) {
#pragma unroll 1
  for (int spins = 0; spins < (1 << 22); ++spins) {
    if (__builtin_amdgcn_readfirstlane(__hip_atomic_load(c, __ATOMIC_RELAXED, __HIP_MEMORY_SCOPE_WORKGROUP)) >= n) break;
  }
  __builtin_amdgcn_fence(__ATOMIC_ACQUIRE, "wavefront");
}

#define WOPRE 4
constexpr int STG = 49152, RT_OFF = 3 * STG, LDS_ALL = 163840;
constexpr int ST_OFF = 160768;
constexpr int CS_OFF = ST_OFF + 1024, SYNC_OFF = ST_OFF + 2048;
constexpr int B1_OFF = 157696;
constexpr int KP = 8192, VP = 8192 + 64;
constexpr int K_OFF = 0, V_OFF = 4 * KP, SLAB_OFF = 62464;
constexpr int REDG_OFF = SLAB_OFF, GX_OFF = REDG_OFF + 61440, REDS1_OFF = 0, REDS2_OFF = GX_OFF + 8192;

__device__ __forceinline__ void st16(u32x4 v, __amdgpu_buffer_rsrc_t rs, unsigned voff, unsigned soff) {
  __builtin_amdgcn_raw_buffer_store_b128(v, rs, voff + soff, 0, SC1);
}
__device__ __forceinline__ bf16x8 ld16(__amdgpu_buffer_rsrc_t rs, unsigned voff, unsigned soff) {
  return __builtin_bit_cast(bf16x8, __builtin_amdgcn_raw_buffer_load_b128(rs, voff, soff, SC1));
}
__device__ __forceinline__ void red_write(LAS char* red, int kw, int lane, const f32x4 (&acc)[4][5]) {
#pragma unroll
  for (int i = 0; i < 4; ++i) {
    if (i != kw) {
      const int slot = kw - (kw > i ? 1 : 0);
#pragma unroll
      for (int j = 0; j < 5; ++j) *(LAS f32x4*)(red + ((i * 3 + slot) * 5 + j) * 1024 + lane * 16) = acc[i][j];
    }
  }
}
__device__ __forceinline__ void red_sum(LAS char* red, int kw, int lane, const f32x4 (&acc)[4][5], f32x4 (&o)[5]) {
#pragma unroll
  for (int j = 0; j < 5; ++j) {
    f32x4 a = kw == 0 ? acc[0][j] : (kw == 1 ? acc[1][j] : (kw == 2 ? acc[2][j] : acc[3][j]));
#pragma unroll
    for (int s = 0; s < 3; ++s) a += *(LAS f32x4*)(red + ((kw * 3 + s) * 5 + j) * 1024 + lane * 16);
    o[j] = a;
  }
}
__device__ __forceinline__ int wg_of(int rt, int cg) { return (((cg & 3) * 8 + (rt & 7)) << 3) | ((cg >> 2) << 1) | (rt >> 3); }
template <int N>
__device__ __forceinline__ bool sum_pairs(const u32x4 (&v)[N], unsigned last_on, unsigned tg, float& a, float& b) {
  bool ok = true;
  a = 0.f; b = 0.f;
#pragma unroll
  for (int k = 0; k < N; ++k) {
    ok = ok && v[k][1] == tg && v[k][3] == tg;
    const unsigned m = k == N - 1 ? last_on : 0xffffffffu;
    a += __uint_as_float(v[k][0] & m);
    b += __uint_as_float(v[k][2] & m);
  }
  return __all(ok);
}
template <int N>
__device__ __forceinline__ void regather(__amdgpu_buffer_rsrc_t rs, const unsigned (&vo)[N], unsigned last_on, unsigned tg, float& a, float& b) {
#pragma unroll 1
  for (int spins = 0; spins < (1 << 20); ++spins) {
    u32x4 v[N];
#pragma unroll
    for (int k = 0; k < N; ++k) v[k] = __builtin_amdgcn_raw_buffer_load_b128(rs, vo[k], 0, SC1);
    if (sum_pairs<N>(v, last_on, tg, a, b)) break;
    __builtin_amdgcn_s_sleep(2);
  }
}

#define STG_STAMP(s_)

struct Ctx {
  const char *x, *W, *b1, *w2, *wo;
  char* ws;
  unsigned lds0;
  int b, rt, cg, rot, ct, qb, ch, w, lane;
};

template <int NP>
__device__ __forceinline__ void mfma_phase(const Ctx& c, __amdgpu_buffer_rsrc_t rs) {
  constexpr int NT = 2 * NP;
  constexpr bool ISH = NP == 4;
  char* ws = c.ws;
  const int lane = c.lane, g = lane >> 4, l15 = lane & 15, q4 = l15 >> 2, p4 = lane & 3;
  const int mw = c.w & 1, nw = c.w >> 1;
  const int sigl = ((q4 >> 1) & 1) | ((g & 1) << 1), xsw = g ^ (l15 >> 1);
  unsigned xa[2], wa[NT];
  xa[0] = c.lds0 + (64 * mw + l15) * 128 + (xsw << 4);
  xa[1] = c.lds0 + (64 * mw + l15) * 128 + ((xsw ^ 4) << 4);
  f32x4 acc[4][NT];
#pragma unroll
  for (int j = 0; j < NT; ++j) {
    const int tl = NT * nw + j;
    wa[j] = c.lds0 + 16384 + (tl >> 2) * 8192 + (8 * g + q4) * 128 + (((tl & 3) ^ sigl) << 5) + p4 * 8;
#pragma unroll
    for (int i = 0; i < 4; ++i) acc[i][j] = f32x4{0.f, 0.f, 0.f, 0.f};
  }
  bf16x8 xf[2][4], wf[2][NT];
#define LDX(set, i, ks) xf[set][i] = *(LAS bf16x8*)(size_t)(xa[ks] + (i) * 2048)
#define LDW(set, j, ks) wf[set][j] = cat44(trread(wa[j] + (ks) * 4096), trread(wa[j] + (ks) * 4096 + 512))
#define HALF(set, ks) do { \
    _Pragma("unroll") for (int j = 0; j < NT; ++j) { \
      if (j == 0) { _Pragma("unroll") for (int i = 0; i < 4; ++i) LDX((set) ^ 1, i, ks); } \
      else if (2 * j - 2 < NT) { LDW((set) ^ 1, 2 * j - 2, ks); LDW((set) ^ 1, 2 * j - 1, ks); } \
      _Pragma("unroll") for (int i = 0; i < 4; ++i) acc[i][j] = mfma16(wf[set][j], xf[set][i], acc[i][j]); \
      SGB(0x8, 1); SGB(0x100, 1); SGB(0x8, 1); SGB(0x100, 1); SGB(0x8, 1); SGB(0x100, 1); SGB(0x8, 1); SGB(0x100, 1); \
      asm volatile("" :: "v"(acc[0][j]), "v"(acc[1][j]), "v"(acc[2][j]), "v"(acc[3][j])); \
      SB(); \
    } } while (0)
  BAR();
  STAMP(1);
#pragma unroll
  for (int i = 0; i < 4; ++i) LDX(0, i, 0);
#pragma unroll
  for (int j = 0; j < NT; ++j) LDW(0, j, 0);
  WAIT_LGKM0();
  int buf = 0;
#pragma unroll 1
  for (int t = 0; t < 10; ++t) {
    HALF(0, 1);
    {
      const int d = buf == 2 ? -2 * STG : STG;
      buf = buf == 2 ? 0 : buf + 1;
      xa[0] += d; xa[1] += d;
#pragma unroll
      for (int j = 0; j < NT; ++j) wa[j] += d;
    }
    WAIT_LGKM0();
    BAR();
    STG_STAMP(t);
    SB();
    HALF(1, 0);
    WAIT_LGKM0();
  }
  STAMP(2);
  lds_wait((LAS unsigned*)(size_t)(c.lds0 + SYNC_OFF + 28), 4);
  float mean[4], rstd[4];
#pragma unroll
  for (int i = 0; i < 4; ++i) {
    const f32x2 st = *(LAS f32x2*)(size_t)(c.lds0 + ST_OFF + (64 * mw + 16 * i + l15) * 8);
    mean[i] = st[0]; rstd[i] = st[1];
  }
  const int u0 = ISH ? 16 * (c.cg - 10) + NT * nw : 12 * c.cg + NT * nw;
  const unsigned so = (ISH ? OFF_H + (2 * c.rt + mw) * 192 * 1024 : OFF_QKV + (2 * c.rt + mw) * 240 * 1024) + 2 * u0 * 1024;
  const unsigned vo = (g >> 1) * 1024 + (16 * (g & 1) + l15) * 16;
#pragma unroll
  for (int j = 0; j < NT; ++j) {
    const f32x4 c4 = *(LAS f32x4*)(size_t)(c.lds0 + CS_OFF + ((NT * nw + j) * 16 + 4 * g) * 4);
    f32x4 bv = {0.f, 0.f, 0.f, 0.f};
    if (ISH) {
      const u32x2 bb = *(LAS u32x2*)(size_t)(c.lds0 + B1_OFF + ((NT * nw + j) * 16 + 4 * g) * 2);
      bv = f32x4{bflo(bb[0]), bfhi(bb[0]), bflo(bb[1]), bfhi(bb[1])};
    }
    const float qs = u0 + j < 40 ? 0.22360679774997896f * 1.4426950408889634f : 1.f;
#pragma unroll
    for (int pp = 0; pp < 2; ++pp) {
      float v[2][4];
#pragma unroll
      for (int i = 0; i < 2; ++i)
#pragma unroll
        for (int r = 0; r < 4; ++r) {
          float t = rstd[2 * pp + i] * (acc[2 * pp + i][j][r] - mean[2 * pp + i] * c4[r]);
          if (ISH) { t += bv[r]; t = t > 0.f ? t : 0.f; t = t * t; } else t *= qs;
          v[i][r] = t;
        }
      const unsigned x0 = pk2(v[0][0], v[0][1]), x1 = pk2(v[0][2], v[0][3]);
      const unsigned y0 = pk2(v[1][0], v[1][1]), y1 = pk2(v[1][2], v[1][3]);
      const auto s0 = __builtin_amdgcn_permlane16_swap(x0, y0, false, false);
      const auto s1 = __builtin_amdgcn_permlane16_swap(x1, y1, false, false);
      const u32x4 o = {s0[0], s1[0], s0[1], s1[1]};
      st16(o, rs, vo + pp * 512, so + 2 * j * 1024);
    }
  }
}

template <int NP>
__device__ __forceinline__ void load_phase(const Ctx& c, __amdgpu_buffer_rsrc_t rs, u64& tag, bf16x8 (&wpre)[5][5], unsigned wb) {
  constexpr int D = 4 + 2 * NP, LDW_ = NP == 4 ? 3072 : 3840;
  char* ws = c.ws;
  const unsigned lds0 = c.lds0;
  const int lane = c.lane, g = lane >> 4, l15 = lane & 15, q4 = l15 >> 2, p4 = lane & 3;
  const int lw = c.w - 4, lr = lane >> 3, ls = lane & 7, lt = lw * 64 + lane;
  const int bk = 16 * lw + lr;
  const int bs0 = (bk >> 1) & 1, bs1 = bs0 ^ 2;
  const int col0 = NP == 4 ? (c.cg - 10) * 512 : c.cg * 384;
  const unsigned vb0 = bk * LDW_ + col0 + ((ls ^ (bs0 << 1)) << 4), vb1 = (bk + 8) * LDW_ + col0 + ((ls ^ (bs1 << 1)) << 4);
  const unsigned vA = (c.rt * 128 + 32 * lw + lr) * 1280 + ((ls ^ (lr >> 1)) << 4);
  int ktn = c.rot;
  auto issue = [&](auto S) {
    constexpr int s = decltype(S)::value;
    const unsigned dst = lds0 + (s % 3) * STG;
    dma_stage<NP>(c.x + ktn * 128, vA, (vA + 10240) ^ 64u, vA + 20480, (vA + 30720) ^ 64u, dst + lw * 4096,
                  c.W + (size_t)ktn * (64 * LDW_), vb0, vb1, dst + 16384 + lw * 2048);
    ktn = ktn == 9 ? 0 : ktn + 1;
  };
  issue(IC<0>{});
  const bool do_rt = c.b < 170;
  const int rkb = c.b / 5, rnb = c.b - 5 * rkb;
  if (do_rt) {
    const char* Wr = rkb < 24 ? c.w2 + (size_t)rkb * 64 * 1280 : c.wo + (size_t)(rkb - 24) * 64 * 1280;
    const unsigned r0 = bk * 1280 + rnb * 256 + ((ls ^ (bs0 << 1)) << 4), r1 = (bk + 8) * 1280 + rnb * 256 + ((ls ^ (bs1 << 1)) << 4);
    dma1(Wr, r0, lds0 + RT_OFF + lw * 2048);
    dma1(Wr, r1, lds0 + RT_OFF + lw * 2048 + 1024);
    dma1(Wr + 128, r0, lds0 + RT_OFF + 8192 + lw * 2048);
    dma1(Wr + 128, r1, lds0 + RT_OFF + 8192 + lw * 2048 + 1024);
  }
  issue(IC<1>{}); issue(IC<2>{});
  {
    const char* p = ws;
    asm volatile("" : "+s"(p));
    tag = *(const CAS u64*)(p + OFF_FL) + 1;
  }
  const unsigned tg = (unsigned)tag;
  wait_vm<2 * D>();
  BAR();
  STAMP(1);
  const int sigl = ((q4 >> 1) & 1) | ((g & 1) << 1), xsw = g ^ (l15 >> 1);
  const bf16x8 ONE8 = {0x3f80, 0x3f80, 0x3f80, 0x3f80, 0x3f80, 0x3f80, 0x3f80, 0x3f80};
  const f32x4 z4 = {0.f, 0.f, 0.f, 0.f};
  bf16x8 sw_[NP][2], sx_[2][2];
  u32x4 gr[5], gc[5];
  u32x2 fl;
  const int fkb = lane < 12 ? 6 * lw + (lane >> 1) : 24 + ((5 * lw) >> 1) + (lane < 18 ? (lane - 12) >> 1 : 0);
  const unsigned fW = FL_W + (fkb * 5 + ((5 * c.ct + (lane & 1) * 4) >> 3)) * 128;
  unsigned ro[5], co[5];
#pragma unroll
  for (int k = 0; k < 5; ++k) {
    ro[k] = OFF_RS + (c.rt * 10 + 2 * k + (lane >> 5)) * 2048 + (32 * lw + (lane & 31)) * 16;
    co[k] = OFF_CS + (c.cg * 10 + 2 * k + (lane >> 5)) * 2048 + (32 * (lw < NP ? lw : 0) + (lane & 31)) * 16;
  }
  auto stage = [&](auto S) {
    constexpr int s = decltype(S)::value;
    if constexpr (s == 0) {
#pragma unroll
      for (int i = 0; i < NP; ++i) {
        const int tl = NP * lw + i;
        const unsigned wq = lds0 + 16384 + (tl >> 2) * 8192 + (8 * g + q4) * 128 + (((tl & 3) ^ sigl) << 5) + p4 * 8;
        sw_[i][0] = cat44(trread(wq), trread(wq + 512));
        sw_[i][1] = cat44(trread(wq + 4096), trread(wq + 4608));
      }
#pragma unroll
      for (int i = 0; i < 2; ++i) {
        const unsigned xq = lds0 + (32 * lw + 16 * i + l15) * 128;
        sx_[i][0] = *(LAS bf16x8*)(size_t)(xq + (xsw << 4));
        sx_[i][1] = *(LAS bf16x8*)(size_t)(xq + ((xsw ^ 4) << 4));
      }
    }
    if constexpr (s < 9) wait_vm<s == 6 ? D + 1 : (s == 7 ? D + 10 : (s == 8 ? 10 + 5 * WOPRE : D))>();
    WAIT_LGKM0();
    BAR();
    if constexpr (s < 7) issue(IC<s + 3>{});
    if constexpr (s == 2) {
      u32x4 cu = {0u, 0u, 0u, 0u};
#pragma unroll
      for (int i = 0; i < NP; ++i) {
        const u32x4 ci = __builtin_bit_cast(u32x4, mfma16(sw_[i][1], ONE8, mfma16(sw_[i][0], ONE8, z4)));
        if (l15 == i) cu = ci;
      }
      if (l15 < NP) {
        const unsigned o = OFF_CS + (c.cg * 10 + c.rot) * 2048 + NP * lw * 128;
        st16(u32x4{cu[0], tg, cu[1], tg}, rs, l15 * 128 + g * 32, o);
        st16(u32x4{cu[2], tg, cu[3], tg}, rs, l15 * 128 + g * 32 + 16, o);
      }
    }
    if constexpr (s == 3) {
#pragma unroll
      for (int i = 0; i < 2; ++i) {
        const f32x4 sx = mfma16(ONE8, sx_[i][1], mfma16(ONE8, sx_[i][0], z4));
        const f32x4 sq = mfma16(sx_[i][1], sx_[i][1], mfma16(sx_[i][0], sx_[i][0], z4));
        const int r = l15 & 3;
        const float sxx = r == 0 ? sq[0] : (r == 1 ? sq[1] : (r == 2 ? sq[2] : sq[3]));
        if (q4 == g)
          st16(u32x4{__float_as_uint(sx[0]), tg, __float_as_uint(sxx), tg}, rs,
                                                 (32 * lw + 16 * i + l15) * 16, OFF_RS + (c.rt * 10 + c.rot) * 2048);
      }
    }
    if constexpr (s < 2) {
      if (do_rt) {
        const unsigned dstb = rkb < 24 ? OFF_W2F : OFF_WOF;
        const int nks = rkb < 24 ? 48 : 20, kb = rkb < 24 ? rkb : rkb - 24;
        {
          const int nl = 2 * lw + s, nt = 8 * rnb + nl, c5 = nt / 5, jj = nt - 5 * c5;
          const unsigned rp = lds0 + RT_OFF + (nl >> 2) * 8192 + (8 * g + q4) * 128 + (((nl & 3) ^ sigl) << 5) + p4 * 8;
#pragma unroll
          for (int ks = 0; ks < 2; ++ks) {
            const bf16x8 f = cat44(trread(rp + ks * 4096), trread(rp + ks * 4096 + 512));
            st16(__builtin_bit_cast(u32x4, f), rs, lane * 16,
                                                   dstb + ((c5 * nks + 2 * kb + ks) * 5 + jj) * 1024);
          }
        }
      }
    }
    if constexpr (s == 2) {
      if (lt < 8) *(LAS unsigned*)(size_t)(lds0 + SYNC_OFF + lt * 4) = 0u;
      if (NP == 4 && lt < 32) dma1(c.b1 + (c.cg - 10) * 512, lane * 16, lds0 + B1_OFF);
    }
    if constexpr (s == 3) {
      if (lt == 0) __hip_atomic_store((GAS u64*)(ws + FL_W + c.b * 128), tag, RLX_AGENT);
    }
    if constexpr (s == 6) {
#pragma unroll
      for (int k = 0; k < 5; ++k) { gr[k] = aload16(ws, ro[k]); gc[k] = aload16(ws, co[k]); }
    }
    if constexpr (s == 5) fl = aload8(ws, fW);
    if constexpr (s == 7) {
      PIN(fl);
      if (!__all(fl[0] == tg && fl[1] == (unsigned)(tag >> 32))) poll_flags(ws + fW, true, tag);
#pragma unroll
      for (int k = 0; k < WOPRE; ++k)
#pragma unroll
        for (int j = 0; j < 5; ++j) wpre[k][j] = ld16(rs, lane * 16, wb + k * 5120 + j * 1024);
    }
    if constexpr (s == 8) {
#pragma unroll
      for (int k = 0; k < 4; ++k)
        dma5_sc1(ws + OFF_W2F + (c.ct * 48 + 12 * lw + 4 + k) * 5120, lane * 16, lds0 + SLAB_OFF + lw * 24576 + k * 5120);
    }
  };
  stage(IC<0>{}); stage(IC<1>{}); stage(IC<2>{}); stage(IC<3>{}); stage(IC<4>{});
  stage(IC<5>{}); stage(IC<6>{}); stage(IC<7>{}); stage(IC<8>{}); stage(IC<9>{});
  wait_vm<5 * WOPRE + 20>();
  {
#pragma unroll
      for (int k = 0; k < 5; ++k) { PIN(gr[k]); PIN(gc[k]); }
      float sx, sxx, c0, c1;
      if (!sum_pairs<5>(gr, 0xffffffffu, tg, sx, sxx)) regather<5>(rs, ro, 0xffffffffu, tg, sx, sxx);
      if (!sum_pairs<5>(gc, 0xffffffffu, tg, c0, c1)) regather<5>(rs, co, 0xffffffffu, tg, c0, c1);
      sx += __shfl_xor(sx, 32); sxx += __shfl_xor(sxx, 32);
      c0 += __shfl_xor(c0, 32); c1 += __shfl_xor(c1, 32);
      const float mean = sx * (1.f / 640.f);
      float var = sxx * (1.f / 640.f) - mean * mean;
      var = var < 0.f ? 0.f : var;
      if (lane < 32) {
        *(LAS f32x2*)(size_t)(lds0 + ST_OFF + (32 * lw + lane) * 8) = f32x2{mean, rsqrtf(var + 1e-5f)};
        *(LAS f32x2*)(size_t)(lds0 + CS_OFF + (32 * lw + lane) * 8) = f32x2{c0, c1};
      }
    }
  WAIT_LGKM0();
  lds_arrive((LAS unsigned*)(size_t)(lds0 + SYNC_OFF + 28));
}

__global__ __launch_bounds__(512, 2) void k_all(const char* __restrict__ x, const char* __restrict__ wqkv, const char* __restrict__ w1,
                                                const char* __restrict__ b1, const char* __restrict__ w2, const char* __restrict__ wo,
                                                const char* __restrict__ b2, char* ws, char* __restrict__ out) {
  extern __shared__ __attribute__((aligned(16))) char smem_[];
  LAS char* smem = (LAS char*)smem_;
  const int tid = threadIdx.x, lane = tid & 63, w = __builtin_amdgcn_readfirstlane(tid >> 6);
  const int b = blockIdx.x, xcd = b & 7, idx = b >> 3;
  const int g = lane >> 4, l15 = lane & 15, q4 = l15 >> 2, p4 = lane & 3;
  const unsigned lds0 = (unsigned)(size_t)smem;
  asm volatile("" :: "s"(x), "s"(wqkv), "s"(w1), "s"(b1), "s"(w2), "s"(wo), "s"(ws));
  u64 pc_;
  asm volatile("s_getpc_b64 %0" : "=s"(pc_));
  const char* pc0 = (const char*)(pc_ & ~(u64)63);
  u64 tag;
  const __amdgpu_buffer_rsrc_t rs = __builtin_amdgcn_make_buffer_rsrc(ws, 0, WS_USED, 0x00020000);
  LAS unsigned* sync = (LAS unsigned*)(smem + SYNC_OFF);
  STAMP(0);
  const int rt = (xcd & 1) * 8 + (idx & 7), cg = (xcd >> 1) * 4 + (idx >> 3);
  const int mt = (xcd >> 1) * 8 + (idx & 7), ct = (xcd & 1) * 4 + (idx >> 3);
  const int head = 4 * xcd + (idx & 3), qb = idx >> 2;
  const int ch = (5 * head) >> 1, odd = head & 1;
  const Ctx c = {x, cg < 10 ? wqkv : w1, b1, w2, wo, ws, lds0, b, rt, cg, (rt + 3 * cg) % 10, ct, qb, ch, w, lane};
  const size_t orow = (size_t)(64 * mt + 16 * (w & 3) + l15) * 1280 + (80 * ct + 4 * g) * 2;
  u32x2 xv[3];
  f32x4 acc[4][5];
  const unsigned va = g * 1024 + l15 * 16;
  bf16x8 wpre[5][5];
  const unsigned wb = w < 4 ? OFF_W2F + (ct * 48 + 12 * w) * 5120 : OFF_WOF + (ct * 20 + 5 * (w - 4)) * 5120;

  if (__builtin_expect(w >= 4, 1)) {
    __builtin_amdgcn_s_waitcnt(0);
    __builtin_amdgcn_s_setprio(3);
    if (cg < 10) load_phase<3>(c, rs, tag, wpre, wb); else load_phase<4>(c, rs, tag, wpre, wb);
    STAMP(2);
    const int lw = w - 4, lt = tid - 256;
    const f32x4 z4 = {0.f, 0.f, 0.f, 0.f};
    {
      const u32x4 one4 = {0x3f803f80u, 0x3f803f80u, 0x3f803f80u, 0x3f803f80u};
      *(LAS u32x4*)(smem + V_OFF + 3 * VP + lt * 16) = one4;
      if (lt < 40) *(LAS u32x4*)(smem + V_OFF + 3 * VP + 4096 + lt * 16) = one4;
      *(LAS u32x4*)(smem + K_OFF + 3 * KP + lt * 16) = u32x4{qb == 0 ? 0xff80u : 0u, 0u, 0u, 0u};
      *(LAS u32x4*)(smem + K_OFF + 3 * KP + 4096 + lt * 16) = u32x4{0u, 0u, 0u, 0u};
      const int ty = lane < 24 ? lane >> 3 : 0, rr0 = 2 * qb - 2 + ((lane >> 1) & 3);
      const int rr = rr0 < 0 ? 0 : (ty == 0 && rr0 < 2 * qb ? 2 * qb : rr0);
      poll_flags(ws + FL_A + wg_of(rr, (80 * ty + ch + (lane & 1) * 2) / 24) * 128, true, tag);
    }
    STAMP(11);
#pragma unroll
    for (int i = 0; i < 2; ++i) {
      const int seg = 2 * lw + i;
      int tb = 4 * qb - 4 + seg; tb = tb < 0 ? 0 : tb;
      dma_kv(ws + OFF_QKV + (size_t)(tb * 240 + 80 + ch) * 1024, lane * 16, lane * 16 + 81920, lds0 + seg * 1024);
    }
    const int l31 = lane & 31, hh = lane >> 5;
    u32x4 qraw[2][2];
#pragma unroll
    for (int a = 0; a < 2; ++a)
#pragma unroll
      for (int ks = 0; ks < 2; ++ks)
        qraw[a][ks] = __builtin_amdgcn_raw_buffer_load_b128(rs, (ks ? 2 : hh) * 1024 + (32 * a + l31) * 16,
                                                            OFF_QKV + ((4 * qb + lw) * 240 + ch) * 1024, SC1);
    lds_arrive(sync + 5);
    bf16x8 qf[2][2];
#pragma unroll
    for (int a = 0; a < 2; ++a) {
      u32x4 v0 = qraw[a][0], v1 = qraw[a][1];
      if (odd && hh == 0) { v0[0] = 0u; v0[1] = 0u; }
      if (hh) v1 = u32x4{0x3f80u, 0u, 0u, 0u};
      else if (!odd) { v1[2] = 0u; v1[3] = 0u; }
      qf[a][0] = __builtin_bit_cast(bf16x8, v0);
      qf[a][1] = __builtin_bit_cast(bf16x8, v1);
    }
    f32x16 mF, z16;
#pragma unroll
    for (int e = 0; e < 16; ++e) {
      mF[e] = (e & 3) + 8 * (e >> 2) + 4 * hh > l31 ? 0.f : -INFINITY;
      z16[e] = 0.f;
    }
    asm volatile("" : "+v"(mF), "+v"(qf[0][0]), "+v"(qf[0][1]), "+v"(qf[1][0]), "+v"(qf[1][1]));
    WAIT_VM(0);
    WAIT_LGKM0();
    lds_arrive(sync);
    lds_wait(sync, 4);
    __builtin_amdgcn_s_setprio(2);
    STAMP(5);
    unsigned kp = lds0 + K_OFF + hh * KP + (64 * lw + l31) * 16;
    const bool ones = (g & 1) && (p4 >> 1);
    unsigned vp = lds0 + V_OFF + (2 * (g & 1) + (p4 >> 1)) * VP + ((ones ? 0 : 64 * lw) + 4 * hh + q4) * 16 + (p4 & 1) * 8;
    unsigned ov = l31 * 16 + hh * 8;
    bf16x8 q0 = qf[0][0], q1 = qf[0][1];
#define LDK(T, ks) (*(LAS bf16x8*)(size_t)(kp + (T) * 512 + (ks) * 2 * KP))
#define MF32(a, b, c) __builtin_amdgcn_mfma_f32_32x32x16_bf16(a, b, c, 0, 0, 0)
#define EX2(s_, e) pk2(__builtin_amdgcn_exp2f((s_)[e]), __builtin_amdgcn_exp2f((s_)[(e) + 1]))
#pragma unroll 1
    for (int un = 0; un < 2; ++un) {
      float m0 = -INFINITY, m1 = -INFINITY;
      {
        bf16x8 kf[9][2];
#pragma unroll
        for (int T = 0; T < 3; ++T) { kf[T][0] = LDK(T, 0); kf[T][1] = LDK(T, 1); }
        f32x16 sp = z16;
#pragma unroll
        for (int T = 0; T < 10; ++T) {
          f32x16 sn = z16;
          if (T + 3 < 9) { kf[T + 3][0] = LDK(T + 3, 0); kf[T + 3][1] = LDK(T + 3, 1); }
          if (T < 9) sn = MF32(kf[T][1], q1, MF32(kf[T][0], q0, T == 0 ? mF : z16));
          if (T == 9) {
#pragma unroll
            for (int e = 0; e < 16; ++e) sp[e] = mF[e] == 0.f ? -INFINITY : sp[e];
          }
          if (T > 0) {
#pragma unroll
            for (int e = 0; e < 16; e += 4) { m0 = MAX3(m0, sp[e], sp[e + 1]); m1 = MAX3(m1, sp[e + 2], sp[e + 3]); }
          }
          SGB(0x100, 2); SGB(0x8, 1); SGB(0x2, 4); SGB(0x8, 1); SGB(0x2, 4);
          asm volatile("" :: "v"(sn), "v"(m0), "v"(m1));
          SB();
          sp = sn;
        }
      }
      float mx = fmaxf(m0, m1);
      mx = fmaxf(mx, __shfl_xor(mx, 32));
      f32x16 ca;
#pragma unroll
      for (int e = 0; e < 16; ++e) ca[e] = -mx;
      f32x16 oa = z16;
      {
        bf16x8 kc[2][2], vf[2][2];
        f32x16 sc[2];
        u32x4 pa[2] = {{0u, 0u, 0u, 0u}, {0u, 0u, 0u, 0u}};
        kc[0][0] = LDK(0, 0); kc[0][1] = LDK(0, 1); kc[1][0] = LDK(1, 0); kc[1][1] = LDK(1, 1);
        sc[0] = MF32(kc[0][1], q1, MF32(kc[0][0], q0, ca + mF));
        sc[1] = z16;
        asm volatile("" :: "v"(sc[0]));
        SB();
#pragma unroll
        for (int T = 0; T < 10; ++T) {
          const int c = T & 1, n = c ^ 1;
          if (T < 9) {
#pragma unroll
            for (int ks = 0; ks < 2; ++ks) vf[c][ks] = cat44(trread(vp + T * 512 + ks * 256), trread(vp + T * 512 + ks * 256 + 128));
          }
          if (T < 8) sc[n] = MF32(kc[n][1], q1, MF32(kc[n][0], q0, ca));
          if (T < 7) { kc[c][0] = LDK(T + 2, 0); kc[c][1] = LDK(T + 2, 1); }
          if (T > 0) oa = MF32(vf[n][1], __builtin_bit_cast(bf16x8, pa[1]), MF32(vf[n][0], __builtin_bit_cast(bf16x8, pa[0]), oa));
          if (T == 8) {
#pragma unroll
            for (int e = 0; e < 16; ++e) sc[c][e] = mF[e] == 0.f ? -INFINITY : sc[c][e];
          }
          if (T < 9) {
#pragma unroll
            for (int e = 0; e < 4; ++e) { pa[0][e] = EX2(sc[c], 2 * e); pa[1][e] = EX2(sc[c], 8 + 2 * e); }
          }
          SGB(0x100, 6);
          if (T == 8) SGB(0x2, 16);
          SGB(0x8, 1); SGB(0x400, 4); SGB(0x2, 1);
#pragma unroll
          for (int k = 0; k < 3; ++k) { SGB(0x8, 1); SGB(0x400, 2); SGB(0x2, 1); SGB(0x400, 2); SGB(0x2, 1); }
          SGB(0x2, 1);
          asm volatile("" :: "v"(sc[n]), "v"(oa), "v"(pa[0]), "v"(pa[1]));
          SB();
        }
      }
      const float il = __builtin_amdgcn_rcpf(oa[12]);
#pragma unroll
      for (int pl = 0; pl < 3; ++pl) {
        const bool ok = pl == 1 || (pl == 0 ? !(odd && hh == 0) : (odd || hh == 0));
        if (ok) {
          const u32x2 pkd = {pk2(oa[4 * pl] * il, oa[4 * pl + 1] * il), pk2(oa[4 * pl + 2] * il, oa[4 * pl + 3] * il)};
          __builtin_amdgcn_raw_buffer_store_b64(pkd, rs, pl * 1024 + ov, OFF_O + ((4 * qb + lw) * 80 + ch) * 1024, SC1);
        }
      }
      kp += 512; vp += ones ? 0 : 512; ov += 512;
      q0 = qf[1][0]; q1 = qf[1][1];
    }
    __builtin_amdgcn_s_setprio(0);
    lds_arrive(sync + 4);
    STAMP(6);
#pragma unroll
    for (int k = WOPRE; k < 5; ++k)
#pragma unroll
      for (int j = 0; j < 5; ++j) wpre[k][j] = ld16(rs, lane * 16, wb + k * 5120 + j * 1024);
#pragma unroll
    for (int j = 0; j < 2; ++j) xv[j] = *(const u32x2*)(x + orow + (3 + j) * 32);
    wait_vm<5 * (5 - WOPRE) + 2>();
    const bool last_o = lds_arrive(sync + 1) == 3;
    if (last_o && lane == 0) __hip_atomic_store((GAS u64*)(ws + FL_O + b * 128), tag, RLX_AGENT);
    const unsigned ab = OFF_O + (mt * 80 + 20 * lw) * 1024;
    {
      const int hd = 8 * lw + (lane & 7);
      poll_flags(ws + FL_O + (((((mt >> 2) << 2) | (hd & 3)) << 3) | (hd >> 2)) * 128, lane < 8, tag);
    }
    STAMP(7);
    bf16x8 af[3][4];
#pragma unroll
    for (int k = 0; k < 3; ++k)
#pragma unroll
      for (int i = 0; i < 4; ++i) af[k][i] = ld16(rs, va + i * 256, ab + k * 4096);
    SB();
#pragma unroll
    for (int i = 0; i < 4; ++i)
#pragma unroll
      for (int j = 0; j < 5; ++j) acc[i][j] = z4;
#pragma unroll
    for (int k = 0; k < 5; ++k) {
#pragma unroll
      for (int i = 0; i < 4; ++i)
#pragma unroll
        for (int j = 0; j < 5; ++j) acc[i][j] = mfma16(wpre[k][j], af[k % 3][i], acc[i][j]);
      SB();
      if (k < 2) {
#pragma unroll
        for (int i = 0; i < 4; ++i) af[k][i] = ld16(rs, va + i * 256, ab + (k + 3) * 4096);
        SB();
      }
    }
    STAMP(8);
    lds_wait(sync + 1, 4);
    lds_wait(sync + 6, 4);
#pragma unroll
    for (int i = 0; i < 4; ++i) {
#pragma unroll
      for (int j = 0; j < 3; ++j) *(LAS f32x4*)(smem + REDS1_OFF + ((i * 4 + lw) * 3 + j) * 1024 + lane * 16) = acc[i][j];
      if (i != lw) {
#pragma unroll
        for (int j = 0; j < 2; ++j)
          *(LAS f32x4*)(smem + REDS2_OFF + ((i * 3 + lw - (lw > i ? 1 : 0)) * 2 + j) * 1024 + lane * 16) = acc[i][3 + j];
      }
    }
    __syncthreads();
    STAMP(9);
#pragma unroll
    for (int j = 0; j < 2; ++j) {
      f32x4 sv = lw == 0 ? acc[0][3 + j] : (lw == 1 ? acc[1][3 + j] : (lw == 2 ? acc[2][3 + j] : acc[3][3 + j]));
#pragma unroll
      for (int k = 0; k < 3; ++k) sv += *(LAS f32x4*)(smem + REDS2_OFF + ((lw * 3 + k) * 2 + j) * 1024 + lane * 16);
      const f32x4 gt = *(LAS f32x4*)(smem + GX_OFF + (lw * 2 + j) * 1024 + lane * 16);
      *(u32x2*)(out + orow + (3 + j) * 32) = fin4(xv[j], sv, gt);
    }

  } else {
    if (idx * 4 + w < CODE_KB) {
      const u32x4 t0 = *(const u32x4*)(pc0 + (idx * 4 + w) * 1024 + lane * 16);
      asm volatile("" :: "v"(t0));
    }
    {
      const char* p = ws;
      asm volatile("" : "+s"(p));
      tag = *(const CAS u64*)(p + OFF_FL) + 1;
    }
    if (cg < 10) mfma_phase<3>(c, rs); else mfma_phase<4>(c, rs);
    STAMP(3);
    WAIT_VM(0);
    if (lds_arrive(sync + 2) == 3 && lane == 0) __hip_atomic_store((GAS u64*)(ws + FL_A + b * 128), tag, RLX_AGENT);
    if (b == 0 && w == 0) {
#pragma unroll 1
      for (int spins = 0; spins < (1 << 20); ++spins) {
        bool ok = true;
#pragma unroll
        for (int i = 0; i < 4; ++i) ok = ok && __hip_atomic_load((const GAS u64*)(ws + FL_W + (lane + 64 * i) * 128), RLX_AGENT) == tag;
        if (__all(ok)) break;
        __builtin_amdgcn_s_sleep(1);
      }
      if (lane == 0) __hip_atomic_store((GAS u64*)(ws + OFF_FL), tag, RLX_AGENT);
    }
    STAMP(4);
#pragma unroll
    for (int k = 0; k < 4; ++k)
#pragma unroll
      for (int j = 0; j < 5; ++j) wpre[k][j] = ld16(rs, lane * 16, wb + k * 5120 + j * 1024);
    const unsigned wl_ = lds0 + SLAB_OFF + w * 24576;
    const unsigned ab = OFF_H + (mt * 192 + 48 * w) * 1024;
    poll_flags(ws + FL_A + wg_of(mt >> 1, 10 + (24 * w + (lane & 1) * 23) / 16) * 128, lane < 2, tag);
    lds_wait(sync + 5, 4);
    STAMP(7);
    bf16x8 af[3][4];
#pragma unroll
    for (int k = 0; k < 3; ++k)
#pragma unroll
      for (int i = 0; i < 4; ++i) af[k][i] = ld16(rs, va + i * 256, ab + k * 4096);
    SB();
#pragma unroll
    for (int i = 0; i < 4; ++i)
#pragma unroll
      for (int j = 0; j < 5; ++j) acc[i][j] = f32x4{0.f, 0.f, 0.f, 0.f};
    STAMP(12);
#pragma unroll
    for (int k = 0; k < 12; ++k) {
      const int cs = k < 4 ? k : (k < 8 ? 4 - (k & 1) : (k < 11 ? k - 8 : 4));
      if (k >= 3 && k < 7) {
#pragma unroll
        for (int j = 0; j < 5; ++j) wpre[4 - ((k + 1) & 1)][j] = *(LAS bf16x8*)(size_t)(wl_ + (k - 3) * 5120 + j * 1024 + lane * 16);
      }
#pragma unroll
      for (int i = 0; i < 4; ++i)
#pragma unroll
        for (int j = 0; j < 5; ++j) acc[i][j] = mfma16(wpre[cs][j], af[k % 3][i], acc[i][j]);
      SB();
      if (k + 3 < 12) {
#pragma unroll
        for (int i = 0; i < 4; ++i) af[k % 3][i] = ld16(rs, va + i * 256, ab + (k + 3) * 4096);
      }
      if (k < 3 || k == 6) {
#pragma unroll
        for (int j = 0; j < 5; ++j) wpre[k < 3 ? k : 4][j] = ld16(rs, lane * 16, wb + (k < 3 ? k + 8 : 11) * 5120 + j * 1024);
      }
      SB();
    }
    lds_arrive(sync + 6);
    lds_wait(sync + 6, 4);
    STAMP(8);
    red_write(smem + REDG_OFF, w, lane, acc);
    u32x2 bb[5];
#pragma unroll
    for (int j = 0; j < 5; ++j) {
      if (j < 3) xv[j] = *(const u32x2*)(x + orow + j * 32);
      bb[j] = *(const u32x2*)(b2 + (80 * ct + 16 * j + 4 * g) * 2);
    }
    WAIT_LGKM0();
    lds_arrive(sync + 3);
    lds_wait(sync + 3, 4);
    f32x4 gt[5];
    red_sum(smem + REDG_OFF, w, lane, acc, gt);
#pragma unroll
    for (int j = 0; j < 5; ++j) {
      const float bv[4] = {bflo(bb[j][0]), bfhi(bb[j][0]), bflo(bb[j][1]), bfhi(bb[j][1])};
#pragma unroll
      for (int e = 0; e < 4; ++e) {
        const float t = rbf(rbf(gt[j][e]) + bv[e]);
        gt[j][e] = rbf(t * __builtin_amdgcn_rcpf(1.f + __expf(-t)));
      }
      if (j >= 3) *(LAS f32x4*)(smem + GX_OFF + (w * 2 + j - 3) * 1024 + lane * 16) = gt[j];
    }
    __syncthreads();
    STAMP(9);
#pragma unroll
    for (int j = 0; j < 3; ++j) {
      f32x4 sv = *(LAS f32x4*)(smem + REDS1_OFF + ((w * 4) * 3 + j) * 1024 + lane * 16);
#pragma unroll
      for (int k = 1; k < 4; ++k) sv += *(LAS f32x4*)(smem + REDS1_OFF + ((w * 4 + k) * 3 + j) * 1024 + lane * 16);
      *(u32x2*)(out + orow + j * 32) = fin4(xv[j], sv, gt[j]);
    }
  }
  STAMP(10);
}

extern "C" void kernel_launch(void* const* d_in, const int* in_sizes, int n_in, void* d_out, int out_size, void* d_ws,
                              size_t ws_size, hipStream_t stream) {
  (void)hipFuncSetAttribute((const void*)k_all, hipFuncAttributeMaxDynamicSharedMemorySize, LDS_ALL);
  k_all<<<256, 512, LDS_ALL, stream>>>((const char*)d_in[0], (const char*)d_in[1], (const char*)d_in[4], (const char*)d_in[5],
                                       (const char*)d_in[6], (const char*)d_in[2], (const char*)d_in[7], (char*)d_ws, (char*)d_out);
}
```

```cpp
#include <hip/hip_runtime.h>
#include <stdint.h>

#define LAS __attribute__((address_space(3)))
#define GAS __attribute__((address_space(1)))
#define CAS __attribute__((address_space(4)))
typedef short bf16x8 __attribute__((ext_vector_type(8)));
typedef short s16x4 __attribute__((ext_vector_type(4)));
typedef float f32x4 __attribute__((ext_vector_type(4)));
typedef unsigned u32x4 __attribute__((ext_vector_type(4)));
typedef unsigned u32x2 __attribute__((ext_vector_type(2)));
typedef __bf16 bf16x2_t __attribute__((ext_vector_type(2)));
typedef float f32x2 __attribute__((ext_vector_type(2)));
typedef float f32x16 __attribute__((ext_vector_type(16)));
typedef unsigned long long u64;

constexpr unsigned OFF_FL = 0;
constexpr unsigned FL_W = 1024, FL_A = FL_W + 32768, FL_O = FL_A + 32768;
constexpr unsigned OFF_RS = 131072;
constexpr unsigned OFF_CS = OFF_RS + 16 * 10 * 2048;
constexpr unsigned OFF_QKV = OFF_CS + 16 * 10 * 2048;
constexpr unsigned OFF_H = OFF_QKV + 32 * 240 * 1024;
constexpr unsigned OFF_O = OFF_H + 32 * 192 * 1024;
constexpr unsigned OFF_W2F = OFF_O + 32 * 80 * 1024;
constexpr unsigned OFF_WOF = OFF_W2F + 8 * 48 * 5120;
constexpr unsigned WS_USED = OFF_WOF + 8 * 20 * 5120;

__device__ __forceinline__ f32x4 mfma16(bf16x8 a, bf16x8 b, f32x4 c) {
  return __builtin_amdgcn_mfma_f32_16x16x32_bf16(a, b, c, 0, 0, 0);
}
__device__ __forceinline__ unsigned pk2(float a, float b) {
  f32x2 v = {a, b};
  bf16x2_t r = __builtin_convertvector(v, bf16x2_t);
  return __builtin_bit_cast(unsigned, r);
}
__device__ __forceinline__ float rbf(float a) { return __builtin_bit_cast(float, pk2(0.f, a) & 0xffff0000u); }
__device__ __forceinline__ float bflo(unsigned u) { return __builtin_bit_cast(float, u << 16); }
__device__ __forceinline__ float bfhi(unsigned u) { return __builtin_bit_cast(float, u & 0xffff0000u); }
__device__ __forceinline__ u32x2 fin4(u32x2 xv, f32x4 sv, f32x4 gt) {
  return u32x2{pk2(bflo(xv[0]) + rbf(rbf(sv[0]) * gt[0]), bfhi(xv[0]) + rbf(rbf(sv[1]) * gt[1])),
               pk2(bflo(xv[1]) + rbf(rbf(sv[2]) * gt[2]), bfhi(xv[1]) + rbf(rbf(sv[3]) * gt[3]))};
}
__device__ __forceinline__ s16x4 trread(unsigned a) {
  return __builtin_amdgcn_ds_read_tr16_b64_v4i16((LAS s16x4*)(size_t)a);
}
__device__ __forceinline__ bf16x8 cat44(s16x4 a, s16x4 b) {
  return __builtin_shufflevector(a, b, 0, 1, 2, 3, 4, 5, 6, 7);
}
#define WAIT_VM(n) asm volatile("s_waitcnt vmcnt(" #n ")" ::: "memory")
#define WAIT_LGKM0() __builtin_amdgcn_s_waitcnt(0xC07F)
#define BAR() __builtin_amdgcn_s_barrier()
#define SB() __builtin_amdgcn_sched_barrier(0)
#define SGB(m, n) __builtin_amdgcn_sched_group_barrier(m, n, 0)
#define MAX3(a, b, c) __builtin_fmaxf(__builtin_fmaxf(a, b), c)
#define RLX_AGENT __ATOMIC_RELAXED, __HIP_MEMORY_SCOPE_AGENT
constexpr int SC1 = 16;
constexpr int CODE_KB = 48;
template <int N> struct IC { static constexpr int value = N; };

#define STAMP(i)
#define CLK(v)

__device__ __forceinline__ void dma1(const char* sbase, unsigned voff, unsigned lds) {
  unsigned keep;
  asm volatile("s_mov_b32 %0, m0\n\ts_mov_b32 m0, %3\n\ts_nop 0\n\tglobal_load_lds_dwordx4 %1, %2\n\ts_mov_b32 m0, %0"
               : "=&s"(keep) : "v"(voff), "s"(sbase), "s"(lds) : "memory");
}
#define DMA_W2(ofs) "s_add_u32 m0, m0, 7040\n\ts_nop 0\n\tglobal_load_lds_dwordx4 %8, %7 offset:" #ofs "\n\t" \
                    "s_add_u32 m0, m0, 1024\n\ts_nop 0\n\tglobal_load_lds_dwordx4 %9, %7 offset:" #ofs "\n\t"
#define DMA_HEAD "s_mov_b32 %0, m0\n\ts_mov_b32 m0, %10\n\ts_nop 0\n\tglobal_load_lds_dwordx4 %8, %7\n\t" \
                 "s_add_u32 m0, m0, 1024\n\ts_nop 0\n\tglobal_load_lds_dwordx4 %9, %7\n\t" DMA_W2(128) DMA_W2(256)
#define DMA_X "s_mov_b32 m0, %6\n\ts_nop 0\n\tglobal_load_lds_dwordx4 %2, %1\n\t" \
              "s_add_u32 m0, m0, 1024\n\ts_nop 0\n\tglobal_load_lds_dwordx4 %3, %1\n\t" \
              "s_add_u32 m0, m0, 1024\n\ts_nop 0\n\tglobal_load_lds_dwordx4 %4, %1\n\t" \
              "s_add_u32 m0, m0, 1024\n\ts_nop 0\n\tglobal_load_lds_dwordx4 %5, %1\n\ts_mov_b32 m0, %0"
#define DMA_OPS : "=&s"(keep) : "s"(sa), "v"(va0), "v"(va1), "v"(va2), "v"(va3), "s"(la), "s"(sb), "v"(vb0), "v"(vb1), "s"(lb) : "memory", "scc"
template <int NP>
__device__ __forceinline__ void dma_stage(const char* sa, unsigned va0, unsigned va1, unsigned va2, unsigned va3, unsigned la,
                                          const char* sb, unsigned vb0, unsigned vb1, unsigned lb) {
  unsigned keep;
  if constexpr (NP == 3) asm volatile(DMA_HEAD DMA_X DMA_OPS);
  else asm volatile(DMA_HEAD DMA_W2(384) DMA_X DMA_OPS);
}
__device__ __forceinline__ void dma_stage_rt(const char* sa, unsigned va0, unsigned va1, unsigned va2, unsigned va3, unsigned la,
                                             const char* sb, unsigned vb0, unsigned vb1, unsigned lb, int cg) {
  unsigned keep;
  asm volatile(DMA_HEAD "s_cmp_lt_u32 %11, 10\n\ts_cbranch_scc1 .Lx%=\n\t" DMA_W2(384) ".Lx%=:\n\t" DMA_X
               : "=&s"(keep) : "s"(sa), "v"(va0), "v"(va1), "v"(va2), "v"(va3), "s"(la), "s"(sb), "v"(vb0), "v"(vb1), "s"(lb), "s"(cg)
               : "memory", "scc");
}
__device__ __forceinline__ void dma_kv(const char* sb, unsigned vk, unsigned vv, unsigned l) {
  unsigned keep;
  asm volatile("s_mov_b32 %0, m0\n\ts_mov_b32 m0, %4\n\ts_nop 0\n\tglobal_load_lds_dwordx4 %2, %1 sc1\n\t"
               "s_add_u32 m0, m0, 7168\n\ts_nop 0\n\tglobal_load_lds_dwordx4 %2, %1 offset:1024 sc1\n\t"
               "s_add_u32 m0, m0, 7168\n\ts_nop 0\n\tglobal_load_lds_dwordx4 %2, %1 offset:2048 sc1\n\t"
               "s_add_u32 m0, m0, 18432\n\ts_nop 0\n\tglobal_load_lds_dwordx4 %3, %1 sc1\n\t"
               "s_add_u32 m0, m0, 7232\n\ts_nop 0\n\tglobal_load_lds_dwordx4 %3, %1 offset:1024 sc1\n\t"
               "s_add_u32 m0, m0, 7232\n\ts_nop 0\n\tglobal_load_lds_dwordx4 %3, %1 offset:2048 sc1\n\t"
               "s_mov_b32 m0, %0"
               : "=&s"(keep) : "s"(sb), "v"(vk), "v"(vv), "s"(l) : "memory", "scc");
}
__device__ __forceinline__ void dma5_sc1(const char* sb, unsigned v, unsigned l) {
  unsigned keep;
  asm volatile("s_mov_b32 %0, m0\n\ts_mov_b32 m0, %3\n\ts_nop 0\n\tglobal_load_lds_dwordx4 %2, %1 sc1\n\t"
               "global_load_lds_dwordx4 %2, %1 offset:1024 sc1\n\tglobal_load_lds_dwordx4 %2, %1 offset:2048 sc1\n\t"
               "global_load_lds_dwordx4 %2, %1 offset:3072 sc1\n\ts_add_u32 m0, m0, 1024\n\ts_nop 0\n\t"
               "global_load_lds_dwordx4 %4, %1 offset:3072 sc1\n\ts_mov_b32 m0, %0"
               : "=&s"(keep) : "s"(sb), "v"(v), "s"(l), "v"(v + 1024) : "memory", "scc");
}
__device__ __forceinline__ u32x2 aload8(const char* sbase, unsigned voff) {
  u32x2 r;
  asm volatile("global_load_dwordx2 %0, %1, %2 sc1" : "=v"(r) : "v"(voff), "s"(sbase) : "memory");
  return r;
}
__device__ __forceinline__ u32x4 aload16(const char* sbase, unsigned voff) {
  u32x4 r;
  asm volatile("global_load_dwordx4 %0, %1, %2 sc1" : "=v"(r) : "v"(voff), "s"(sbase) : "memory");
  return r;
}
#define PIN(x) asm volatile("" : "+v"(x))
template <int N> __device__ __forceinline__ void wait_vm() { asm volatile("s_waitcnt vmcnt(%0)" :: "i"(N) : "memory"); }
__device__ __forceinline__ void poll_flags(const char* flag, bool need, u64 tag) {
#pragma unroll 1
  for (int spins = 0; spins < (1 << 20); ++spins) {
    const u64 v = need ? __hip_atomic_load((const GAS u64*)flag, RLX_AGENT) : tag;
    if (__all(v == tag)) break;
    __builtin_amdgcn_s_sleep(1);
  }
  __builtin_amdgcn_fence(__ATOMIC_ACQUIRE, "wavefront");
}
__device__ __forceinline__ unsigned lds_arrive(LAS unsigned* c) {
  unsigned r = 0;
  if ((threadIdx.x & 63) == 0) r = __hip_atomic_fetch_add(c, 1u, __ATOMIC_RELAXED, __HIP_MEMORY_SCOPE_WORKGROUP);
  return __builtin_amdgcn_readfirstlane(r);
}
__device__ __forceinline__ void lds_wait_sleep(LAS unsigned* c, unsigned n) {
#pragma unroll 1
  for (int spins = 0; spins < (1 << 22); ++spins) {
    if (__builtin_amdgcn_readfirstlane(__hip_atomic_load(c, __ATOMIC_RELAXED, __HIP_MEMORY_SCOPE_WORKGROUP)) >= n) break;
    __builtin_amdgcn_s_sleep(2);
  }
  __builtin_amdgcn_fence(__ATOMIC_ACQUIRE, "wavefront");
}
__device__ __forceinline__ void lds_wait(LAS unsigned* c, unsigned n) {
#pragma unroll 1
  for (int spins = 0; spins < (1 << 22); ++spins) {
    if (__builtin_amdgcn_readfirstlane(__hip_atomic_load(c, __ATOMIC_RELAXED, __HIP_MEMORY_SCOPE_WORKGROUP)) >= n) break;
  }
  __builtin_amdgcn_fence(__ATOMIC_ACQUIRE, "wavefront");
}

#define WOPRE 4
constexpr int STG = 49152, RT_OFF = 3 * STG, LDS_ALL = 163840;
constexpr int ST_OFF = 160768;
constexpr int CS_OFF = ST_OFF + 1024, SYNC_OFF = ST_OFF + 2048;
constexpr int B1_OFF = 157696;
constexpr int KP = 8192, VP = 8192 + 64;
constexpr int K_OFF = 0, V_OFF = 4 * KP, SLAB_OFF = 62464;
constexpr int REDG_OFF = SLAB_OFF, GX_OFF = REDG_OFF + 61440, REDS1_OFF = 0, REDS2_OFF = GX_OFF + 8192;

__device__ __forceinline__ void st16(u32x4 v, __amdgpu_buffer_rsrc_t rs, unsigned voff, unsigned soff) {
  __builtin_amdgcn_raw_buffer_store_b128(v, rs, voff + soff, 0, SC1);
}
__device__ __forceinline__ bf16x8 ld16(__amdgpu_buffer_rsrc_t rs, unsigned voff, unsigned soff) {
  return __builtin_bit_cast(bf16x8, __builtin_amdgcn_raw_buffer_load_b128(rs, voff, soff, SC1));
}
__device__ __forceinline__ void red_write(LAS char* red, int kw, int lane, const f32x4 (&acc)[4][5]) {
#pragma unroll
  for (int i = 0; i < 4; ++i) {
    if (i != kw) {
      const int slot = kw - (kw > i ? 1 : 0);
#pragma unroll
      for (int j = 0; j < 5; ++j) *(LAS f32x4*)(red + ((i * 3 + slot) * 5 + j) * 1024 + lane * 16) = acc[i][j];
    }
  }
}
__device__ __forceinline__ void red_sum(LAS char* red, int kw, int lane, const f32x4 (&acc)[4][5], f32x4 (&o)[5]) {
#pragma unroll
  for (int j = 0; j < 5; ++j) {
    f32x4 a = kw == 0 ? acc[0][j] : (kw == 1 ? acc[1][j] : (kw == 2 ? acc[2][j] : acc[3][j]));
#pragma unroll
    for (int s = 0; s < 3; ++s) a += *(LAS f32x4*)(red + ((kw * 3 + s) * 5 + j) * 1024 + lane * 16);
    o[j] = a;
  }
}
__device__ __forceinline__ int wg_of(int rt, int cg) { return (((cg & 3) * 8 + (rt & 7)) << 3) | ((cg >> 2) << 1) | (rt >> 3); }
template <int N>
__device__ __forceinline__ bool sum_pairs(const u32x4 (&v)[N], unsigned last_on, unsigned tg, float& a, float& b) {
  bool ok = true;
  a = 0.f; b = 0.f;
#pragma unroll
  for (int k = 0; k < N; ++k) {
    ok = ok && v[k][1] == tg && v[k][3] == tg;
    const unsigned m = k == N - 1 ? last_on : 0xffffffffu;
    a += __uint_as_float(v[k][0] & m);
    b += __uint_as_float(v[k][2] & m);
  }
  return __all(ok);
}
template <int N>
__device__ __forceinline__ void regather(__amdgpu_buffer_rsrc_t rs, const unsigned (&vo)[N], unsigned last_on, unsigned tg, float& a, float& b) {
#pragma unroll 1
  for (int spins = 0; spins < (1 << 20); ++spins) {
    u32x4 v[N];
#pragma unroll
    for (int k = 0; k < N; ++k) v[k] = __builtin_amdgcn_raw_buffer_load_b128(rs, vo[k], 0, SC1);
    if (sum_pairs<N>(v, last_on, tg, a, b)) break;
    __builtin_amdgcn_s_sleep(2);
  }
}

#define STG_STAMP(s_)

struct Ctx {
  const char *x, *W, *b1, *w2, *wo;
  char* ws;
  unsigned lds0, vA, vb0, vb1;
  int b, rt, cg, rot, ct, qb, ch, w, lane;
};

template <int NP>
__device__ __forceinline__ void mfma_phase(const Ctx& c, __amdgpu_buffer_rsrc_t rs) {
  constexpr int NT = 2 * NP;
  constexpr bool ISH = NP == 4;
  char* ws = c.ws;
  const int lane = c.lane, g = lane >> 4, l15 = lane & 15, q4 = l15 >> 2, p4 = lane & 3;
  const int mw = c.w & 1, nw = c.w >> 1;
  const int sigl = ((q4 >> 1) & 1) | ((g & 1) << 1), xsw = g ^ (l15 >> 1);
  unsigned xa[2], wa[NT];
  xa[0] = c.lds0 + (64 * mw + l15) * 128 + (xsw << 4);
  xa[1] = c.lds0 + (64 * mw + l15) * 128 + ((xsw ^ 4) << 4);
  f32x4 acc[4][NT];
#pragma unroll
  for (int j = 0; j < NT; ++j) {
    const int tl = NT * nw + j;
    wa[j] = c.lds0 + 16384 + (tl >> 2) * 8192 + (8 * g + q4) * 128 + (((tl & 3) ^ sigl) << 5) + p4 * 8;
#pragma unroll
    for (int i = 0; i < 4; ++i) acc[i][j] = f32x4{0.f, 0.f, 0.f, 0.f};
  }
  bf16x8 xf[2][4], wf[2][NT];
#define LDX(set, i, ks) xf[set][i] = *(LAS bf16x8*)(size_t)(xa[ks] + (i) * 2048)
#define LDW(set, j, ks) wf[set][j] = cat44(trread(wa[j] + (ks) * 4096), trread(wa[j] + (ks) * 4096 + 512))
#define HALF(set, ks) do { \
    _Pragma("unroll") for (int j = 0; j < NT; ++j) { \
      if (j == 0) { _Pragma("unroll") for (int i = 0; i < 4; ++i) LDX((set) ^ 1, i, ks); } \
      else if (2 * j - 2 < NT) { LDW((set) ^ 1, 2 * j - 2, ks); LDW((set) ^ 1, 2 * j - 1, ks); } \
      _Pragma("unroll") for (int i = 0; i < 4; ++i) acc[i][j] = mfma16(wf[set][j], xf[set][i], acc[i][j]); \
      SGB(0x8, 1); SGB(0x100, 1); SGB(0x8, 1); SGB(0x100, 1); SGB(0x8, 1); SGB(0x100, 1); SGB(0x8, 1); SGB(0x100, 1); \
      asm volatile("" :: "v"(acc[0][j]), "v"(acc[1][j]), "v"(acc[2][j]), "v"(acc[3][j])); \
      SB(); \
    } } while (0)
  BAR();
  STAMP(1);
#pragma unroll
  for (int i = 0; i < 4; ++i) LDX(0, i, 0);
#pragma unroll
  for (int j = 0; j < NT; ++j) LDW(0, j, 0);
  WAIT_LGKM0();
  int buf = 0;
#pragma unroll 1
  for (int t = 0; t < 10; ++t) {
    HALF(0, 1);
    {
      const int d = buf == 2 ? -2 * STG : STG;
      buf = buf == 2 ? 0 : buf + 1;
      xa[0] += d; xa[1] += d;
#pragma unroll
      for (int j = 0; j < NT; ++j) wa[j] += d;
    }
    WAIT_LGKM0();
    BAR();
    STG_STAMP(t);
    SB();
    HALF(1, 0);
    WAIT_LGKM0();
  }
  STAMP(2);
  lds_wait((LAS unsigned*)(size_t)(c.lds0 + SYNC_OFF + 28), 4);
  float mean[4], rstd[4];
#pragma unroll
  for (int i = 0; i < 4; ++i) {
    const f32x2 st = *(LAS f32x2*)(size_t)(c.lds0 + ST_OFF + (64 * mw + 16 * i + l15) * 8);
    mean[i] = st[0]; rstd[i] = st[1];
  }
  const int u0 = ISH ? 16 * (c.cg - 10) + NT * nw : 12 * c.cg + NT * nw;
  const unsigned so = (ISH ? OFF_H + (2 * c.rt + mw) * 192 * 1024 : OFF_QKV + (2 * c.rt + mw) * 240 * 1024) + 2 * u0 * 1024;
  const unsigned vo = (g >> 1) * 1024 + (16 * (g & 1) + l15) * 16;
#pragma unroll
  for (int j = 0; j < NT; ++j) {
    const f32x4 c4 = *(LAS f32x4*)(size_t)(c.lds0 + CS_OFF + ((NT * nw + j) * 16 + 4 * g) * 4);
    f32x4 bv = {0.f, 0.f, 0.f, 0.f};
    if (ISH) {
      const u32x2 bb = *(LAS u32x2*)(size_t)(c.lds0 + B1_OFF + ((NT * nw + j) * 16 + 4 * g) * 2);
      bv = f32x4{bflo(bb[0]), bfhi(bb[0]), bflo(bb[1]), bfhi(bb[1])};
    }
    const float qs = u0 + j < 40 ? 0.22360679774997896f * 1.4426950408889634f : 1.f;
#pragma unroll
    for (int pp = 0; pp < 2; ++pp) {
      float v[2][4];
#pragma unroll
      for (int i = 0; i < 2; ++i)
#pragma unroll
        for (int r = 0; r < 4; ++r) {
          float t = rstd[2 * pp + i] * (acc[2 * pp + i][j][r] - mean[2 * pp + i] * c4[r]);
          if (ISH) { t += bv[r]; t = t > 0.f ? t : 0.f; t = t * t; } else t *= qs;
          v[i][r] = t;
        }
      const unsigned x0 = pk2(v[0][0], v[0][1]), x1 = pk2(v[0][2], v[0][3]);
      const unsigned y0 = pk2(v[1][0], v[1][1]), y1 = pk2(v[1][2], v[1][3]);
      const auto s0 = __builtin_amdgcn_permlane16_swap(x0, y0, false, false);
      const auto s1 = __builtin_amdgcn_permlane16_swap(x1, y1, false, false);
      const u32x4 o = {s0[0], s1[0], s0[1], s1[1]};
      st16(o, rs, vo + pp * 512, so + 2 * j * 1024);
    }
  }
}

template <int NP>
__device__ __forceinline__ void load_phase(const Ctx& c, __amdgpu_buffer_rsrc_t rs, u64& tag, bf16x8 (&wpre)[5][5], unsigned wb) {
  constexpr int D = 4 + 2 * NP, LDW_ = NP == 4 ? 3072 : 3840;
  char* ws = c.ws;
  const unsigned lds0 = c.lds0;
  const int lane = c.lane, g = lane >> 4, l15 = lane & 15, q4 = l15 >> 2, p4 = lane & 3;
  const int lw = c.w - 4, lt = lw * 64 + lane;
  const unsigned vb0 = c.vb0, vb1 = c.vb1, vA = c.vA;
  int ktn = c.rot + 3 >= 10 ? c.rot - 7 : c.rot + 3;
  auto issue = [&](auto S) {
    constexpr int s = decltype(S)::value;
    const unsigned dst = lds0 + (s % 3) * STG;
    dma_stage<NP>(c.x + ktn * 128, vA, (vA + 10240) ^ 64u, vA + 20480, (vA + 30720) ^ 64u, dst + lw * 4096,
                  c.W + (size_t)ktn * (64 * LDW_), vb0, vb1, dst + 16384 + lw * 2048);
    ktn = ktn == 9 ? 0 : ktn + 1;
  };
  const bool do_rt = c.b < 170;
  const int rkb = c.b / 5, rnb = c.b - 5 * rkb;
  {
    const char* p = ws;
    asm volatile("" : "+s"(p));
    tag = *(const CAS u64*)(p + OFF_FL) + 1;
  }
  const unsigned tg = (unsigned)tag;
  wait_vm<2 * D>();
  BAR();
  STAMP(1);
  const int sigl = ((q4 >> 1) & 1) | ((g & 1) << 1), xsw = g ^ (l15 >> 1);
  const bf16x8 ONE8 = {0x3f80, 0x3f80, 0x3f80, 0x3f80, 0x3f80, 0x3f80, 0x3f80, 0x3f80};
  const f32x4 z4 = {0.f, 0.f, 0.f, 0.f};
  bf16x8 sw_[NP][2], sx_[2][2];
  u32x4 gr[5], gc[5];
  u32x2 fl;
  const int fkb = lane < 12 ? 6 * lw + (lane >> 1) : 24 + ((5 * lw) >> 1) + (lane < 18 ? (lane - 12) >> 1 : 0);
  const unsigned fW = FL_W + (fkb * 5 + ((5 * c.ct + (lane & 1) * 4) >> 3)) * 128;
  unsigned ro[5], co[5];
#pragma unroll
  for (int k = 0; k < 5; ++k) {
    ro[k] = OFF_RS + (c.rt * 10 + 2 * k + (lane >> 5)) * 2048 + (32 * lw + (lane & 31)) * 16;
    co[k] = OFF_CS + (c.cg * 10 + 2 * k + (lane >> 5)) * 2048 + (32 * (lw < NP ? lw : 0) + (lane & 31)) * 16;
  }
  auto stage = [&](auto S) {
    constexpr int s = decltype(S)::value;
    if constexpr (s == 0) {
#pragma unroll
      for (int i = 0; i < NP; ++i) {
        const int tl = NP * lw + i;
        const unsigned wq = lds0 + 16384 + (tl >> 2) * 8192 + (8 * g + q4) * 128 + (((tl & 3) ^ sigl) << 5) + p4 * 8;
        sw_[i][0] = cat44(trread(wq), trread(wq + 512));
        sw_[i][1] = cat44(trread(wq + 4096), trread(wq + 4608));
      }
#pragma unroll
      for (int i = 0; i < 2; ++i) {
        const unsigned xq = lds0 + (32 * lw + 16 * i + l15) * 128;
        sx_[i][0] = *(LAS bf16x8*)(size_t)(xq + (xsw << 4));
        sx_[i][1] = *(LAS bf16x8*)(size_t)(xq + ((xsw ^ 4) << 4));
      }
    }
    if constexpr (s < 9) wait_vm<s == 6 ? D + 1 : (s == 7 ? D + 10 : (s == 8 ? 10 + 5 * WOPRE : D))>();
    WAIT_LGKM0();
    BAR();
    if constexpr (s < 7) issue(IC<s + 3>{});
    if constexpr (s == 2) {
      u32x4 cu = {0u, 0u, 0u, 0u};
#pragma unroll
      for (int i = 0; i < NP; ++i) {
        const u32x4 ci = __builtin_bit_cast(u32x4, mfma16(sw_[i][1], ONE8, mfma16(sw_[i][0], ONE8, z4)));
        if (l15 == i) cu = ci;
      }
      if (l15 < NP) {
        const unsigned o = OFF_CS + (c.cg * 10 + c.rot) * 2048 + NP * lw * 128;
        st16(u32x4{cu[0], tg, cu[1], tg}, rs, l15 * 128 + g * 32, o);
        st16(u32x4{cu[2], tg, cu[3], tg}, rs, l15 * 128 + g * 32 + 16, o);
      }
    }
    if constexpr (s == 3) {
#pragma unroll
      for (int i = 0; i < 2; ++i) {
        const f32x4 sx = mfma16(ONE8, sx_[i][1], mfma16(ONE8, sx_[i][0], z4));
        const f32x4 sq = mfma16(sx_[i][1], sx_[i][1], mfma16(sx_[i][0], sx_[i][0], z4));
        const int r = l15 & 3;
        const float sxx = r == 0 ? sq[0] : (r == 1 ? sq[1] : (r == 2 ? sq[2] : sq[3]));
        if (q4 == g)
          st16(u32x4{__float_as_uint(sx[0]), tg, __float_as_uint(sxx), tg}, rs,
                                                 (32 * lw + 16 * i + l15) * 16, OFF_RS + (c.rt * 10 + c.rot) * 2048);
      }
    }
    if constexpr (s < 2) {
      if (do_rt) {
        const unsigned dstb = rkb < 24 ? OFF_W2F : OFF_WOF;
        const int nks = rkb < 24 ? 48 : 20, kb = rkb < 24 ? rkb : rkb - 24;
        {
          const int nl = 2 * lw + s, nt = 8 * rnb + nl, c5 = nt / 5, jj = nt - 5 * c5;
          const unsigned rp = lds0 + RT_OFF + (nl >> 2) * 8192 + (8 * g + q4) * 128 + (((nl & 3) ^ sigl) << 5) + p4 * 8;
#pragma unroll
          for (int ks = 0; ks < 2; ++ks) {
            const bf16x8 f = cat44(trread(rp + ks * 4096), trread(rp + ks * 4096 + 512));
            st16(__builtin_bit_cast(u32x4, f), rs, lane * 16,
                                                   dstb + ((c5 * nks + 2 * kb + ks) * 5 + jj) * 1024);
          }
        }
      }
    }
    if constexpr (s == 2) {
      if (lt < 8) *(LAS unsigned*)(size_t)(lds0 + SYNC_OFF + lt * 4) = 0u;
      if (NP == 4 && lt < 32) dma1(c.b1 + (c.cg - 10) * 512, lane * 16, lds0 + B1_OFF);
    }
    if constexpr (s == 3) {
      if (lt == 0) __hip_atomic_store((GAS u64*)(ws + FL_W + c.b * 128), tag, RLX_AGENT);
    }
    if constexpr (s == 6) {
#pragma unroll
      for (int k = 0; k < 5; ++k) { gr[k] = aload16(ws, ro[k]); gc[k] = aload16(ws, co[k]); }
    }
    if constexpr (s == 5) fl = aload8(ws, fW);
    if constexpr (s == 7) {
      PIN(fl);
      if (!__all(fl[0] == tg && fl[1] == (unsigned)(tag >> 32))) poll_flags(ws + fW, true, tag);
#pragma unroll
      for (int k = 0; k < WOPRE; ++k)
#pragma unroll
        for (int j = 0; j < 5; ++j) wpre[k][j] = ld16(rs, lane * 16, wb + k * 5120 + j * 1024);
    }
    if constexpr (s == 8) {
#pragma unroll
      for (int k = 0; k < 4; ++k)
        dma5_sc1(ws + OFF_W2F + (c.ct * 48 + 12 * lw + 4 + k) * 5120, lane * 16, lds0 + SLAB_OFF + lw * 24576 + k * 5120);
    }
  };
  stage(IC<0>{}); stage(IC<1>{}); stage(IC<2>{}); stage(IC<3>{}); stage(IC<4>{});
  stage(IC<5>{}); stage(IC<6>{}); stage(IC<7>{}); stage(IC<8>{}); stage(IC<9>{});
  wait_vm<5 * WOPRE + 20>();
  {
#pragma unroll
      for (int k = 0; k < 5; ++k) { PIN(gr[k]); PIN(gc[k]); }
      float sx, sxx, c0, c1;
      if (!sum_pairs<5>(gr, 0xffffffffu, tg, sx, sxx)) regather<5>(rs, ro, 0xffffffffu, tg, sx, sxx);
      if (!sum_pairs<5>(gc, 0xffffffffu, tg, c0, c1)) regather<5>(rs, co, 0xffffffffu, tg, c0, c1);
      sx += __shfl_xor(sx, 32); sxx += __shfl_xor(sxx, 32);
      c0 += __shfl_xor(c0, 32); c1 += __shfl_xor(c1, 32);
      const float mean = sx * (1.f / 640.f);
      float var = sxx * (1.f / 640.f) - mean * mean;
      var = var < 0.f ? 0.f : var;
      if (lane < 32) {
        *(LAS f32x2*)(size_t)(lds0 + ST_OFF + (32 * lw + lane) * 8) = f32x2{mean, rsqrtf(var + 1e-5f)};
        *(LAS f32x2*)(size_t)(lds0 + CS_OFF + (32 * lw + lane) * 8) = f32x2{c0, c1};
      }
    }
  WAIT_LGKM0();
  lds_arrive((LAS unsigned*)(size_t)(lds0 + SYNC_OFF + 28));
}

__global__ __launch_bounds__(512, 2) void k_all(const char* __restrict__ x, const char* __restrict__ wqkv, const char* __restrict__ w1,
                                                const char* __restrict__ b1, const char* __restrict__ w2, const char* __restrict__ wo,
                                                const char* __restrict__ b2, char* ws, char* __restrict__ out) {
  extern __shared__ __attribute__((aligned(16))) char smem_[];
  LAS char* smem = (LAS char*)smem_;
  const int tid = threadIdx.x, lane = tid & 63, w = __builtin_amdgcn_readfirstlane(tid >> 6);
  const int b = blockIdx.x, xcd = b & 7, idx = b >> 3;
  const unsigned lds0 = (unsigned)(size_t)smem;
  asm volatile("" :: "s"(x), "s"(wqkv), "s"(w1), "s"(b1), "s"(w2), "s"(wo), "s"(ws));
  u64 pc_;
  asm volatile("s_getpc_b64 %0" : "=s"(pc_));
  const int rt = (xcd & 1) * 8 + (idx & 7), cg = (xcd >> 1) * 4 + (idx >> 3);
  const int rot = (rt + 3 * cg) % 10;
  const char* W = cg < 10 ? wqkv : w1;
  unsigned vA = 0, vb0 = 0, vb1 = 0;
  if (w >= 4) {
    const int lw = w - 4, lr = lane >> 3, ls = lane & 7;
    const int bk = 16 * lw + lr, sw0 = (ls ^ (bk & 2)) << 4, sw1 = sw0 ^ 64;
    const int ldw = cg < 10 ? 3840 : 3072, col0 = cg < 10 ? cg * 384 : (cg - 10) * 512;
    vb0 = bk * ldw + col0 + sw0;
    vb1 = (bk + 8) * ldw + col0 + sw1;
    vA = (rt * 128 + 32 * lw + lr) * 1280 + ((ls ^ (lr >> 1)) << 4);
    const unsigned a1 = (vA + 10240) ^ 64u, a2 = vA + 20480, a3 = (vA + 30720) ^ 64u;
    dma_stage_rt(x + rot * 128, vA, a1, a2, a3, lds0 + lw * 4096, W + (size_t)rot * 64 * ldw, vb0, vb1, lds0 + 16384 + lw * 2048, cg);
    if (b < 170) {
      const int rkb = b / 5, rnb = b - 5 * rkb;
      const char* Wr = rkb < 24 ? w2 + (size_t)rkb * 64 * 1280 : wo + (size_t)(rkb - 24) * 64 * 1280;
      const unsigned r0 = bk * 1280 + rnb * 256 + sw0, r1 = (bk + 8) * 1280 + rnb * 256 + sw1;
      dma1(Wr, r0, lds0 + RT_OFF + lw * 2048);
      dma1(Wr, r1, lds0 + RT_OFF + lw * 2048 + 1024);
      dma1(Wr + 128, r0, lds0 + RT_OFF + 8192 + lw * 2048);
      dma1(Wr + 128, r1, lds0 + RT_OFF + 8192 + lw * 2048 + 1024);
    }
#pragma unroll
    for (int s = 1; s < 3; ++s) {
      const int kt = rot + s >= 10 ? rot + s - 10 : rot + s;
      dma_stage_rt(x + kt * 128, vA, a1, a2, a3, lds0 + s * STG + lw * 4096, W + (size_t)kt * 64 * ldw, vb0, vb1,
                   lds0 + s * STG + 16384 + lw * 2048, cg);
    }
  }
  SB();
  const int g = lane >> 4, l15 = lane & 15, q4 = l15 >> 2, p4 = lane & 3;
  const char* pc0 = (const char*)(pc_ & ~(u64)63);
  u64 tag;
  const __amdgpu_buffer_rsrc_t rs = __builtin_amdgcn_make_buffer_rsrc(ws, 0, WS_USED, 0x00020000);
  LAS unsigned* sync = (LAS unsigned*)(smem + SYNC_OFF);
  STAMP(0);
  const int mt = (xcd >> 1) * 8 + (idx & 7), ct = (xcd & 1) * 4 + (idx >> 3);
  const int head = 4 * xcd + (idx & 3), qb = idx >> 2;
  const int ch = (5 * head) >> 1, odd = head & 1;
  const Ctx c = {x, W, b1, w2, wo, ws, lds0, vA, vb0, vb1, b, rt, cg, rot, ct, qb, ch, w, lane};
  const size_t orow = (size_t)(64 * mt + 16 * (w & 3) + l15) * 1280 + (80 * ct + 4 * g) * 2;
  u32x2 xv[3];
  f32x4 acc[4][5];
  const unsigned va = g * 1024 + l15 * 16;
  bf16x8 wpre[5][5];
  const unsigned wb = w < 4 ? OFF_W2F + (ct * 48 + 12 * w) * 5120 : OFF_WOF + (ct * 20 + 5 * (w - 4)) * 5120;

  if (__builtin_expect(w >= 4, 1)) {
    __builtin_amdgcn_s_waitcnt(0x3F7F | 0xC000);
    if (cg < 10) load_phase<3>(c, rs, tag, wpre, wb); else load_phase<4>(c, rs, tag, wpre, wb);
    STAMP(2);
    const int lw = w - 4, lt = tid - 256;
    const f32x4 z4 = {0.f, 0.f, 0.f, 0.f};
    {
      const u32x4 one4 = {0x3f803f80u, 0x3f803f80u, 0x3f803f80u, 0x3f803f80u};
      *(LAS u32x4*)(smem + V_OFF + 3 * VP + lt * 16) = one4;
      if (lt < 40) *(LAS u32x4*)(smem + V_OFF + 3 * VP + 4096 + lt * 16) = one4;
      *(LAS u32x4*)(smem + K_OFF + 3 * KP + lt * 16) = u32x4{qb == 0 ? 0xff80u : 0u, 0u, 0u, 0u};
      *(LAS u32x4*)(smem + K_OFF + 3 * KP + 4096 + lt * 16) = u32x4{0u, 0u, 0u, 0u};
      const int ty = lane < 24 ? lane >> 3 : 0, rr0 = 2 * qb - 2 + ((lane >> 1) & 3);
      const int rr = rr0 < 0 ? 0 : (ty == 0 && rr0 < 2 * qb ? 2 * qb : rr0);
      poll_flags(ws + FL_A + wg_of(rr, (80 * ty + ch + (lane & 1) * 2) / 24) * 128, true, tag);
    }
    STAMP(11);
#pragma unroll
    for (int i = 0; i < 2; ++i) {
      const int seg = 2 * lw + i;
      int tb = 4 * qb - 4 + seg; tb = tb < 0 ? 0 : tb;
      dma_kv(ws + OFF_QKV + (size_t)(tb * 240 + 80 + ch) * 1024, lane * 16, lane * 16 + 81920, lds0 + seg * 1024);
    }
    const int l31 = lane & 31, hh = lane >> 5;
    u32x4 qraw[2][2];
#pragma unroll
    for (int a = 0; a < 2; ++a)
#pragma unroll
      for (int ks = 0; ks < 2; ++ks)
        qraw[a][ks] = __builtin_amdgcn_raw_buffer_load_b128(rs, (ks ? 2 : hh) * 1024 + (32 * a + l31) * 16,
                                                            OFF_QKV + ((4 * qb + lw) * 240 + ch) * 1024, SC1);
    lds_arrive(sync + 5);
    bf16x8 qf[2][2];
#pragma unroll
    for (int a = 0; a < 2; ++a) {
      u32x4 v0 = qraw[a][0], v1 = qraw[a][1];
      if (odd && hh == 0) { v0[0] = 0u; v0[1] = 0u; }
      if (hh) v1 = u32x4{0x3f80u, 0u, 0u, 0u};
      else if (!odd) { v1[2] = 0u; v1[3] = 0u; }
      qf[a][0] = __builtin_bit_cast(bf16x8, v0);
      qf[a][1] = __builtin_bit_cast(bf16x8, v1);
    }
    f32x16 mF, z16;
#pragma unroll
    for (int e = 0; e < 16; ++e) {
      mF[e] = (e & 3) + 8 * (e >> 2) + 4 * hh > l31 ? 0.f : -INFINITY;
      z16[e] = 0.f;
    }
    asm volatile("" : "+v"(mF), "+v"(qf[0][0]), "+v"(qf[0][1]), "+v"(qf[1][0]), "+v"(qf[1][1]));
    WAIT_VM(0);
    WAIT_LGKM0();
    lds_arrive(sync);
    lds_wait(sync, 4);
    __builtin_amdgcn_s_setprio(2);
    STAMP(5);
    unsigned kp = lds0 + K_OFF + hh * KP + (64 * lw + l31) * 16;
    const bool ones = (g & 1) && (p4 >> 1);
    unsigned vp = lds0 + V_OFF + (2 * (g & 1) + (p4 >> 1)) * VP + ((ones ? 0 : 64 * lw) + 4 * hh + q4) * 16 + (p4 & 1) * 8;
    unsigned ov = l31 * 16 + hh * 8;
    bf16x8 q0 = qf[0][0], q1 = qf[0][1];
#define LDK(T, ks) (*(LAS bf16x8*)(size_t)(kp + (T) * 512 + (ks) * 2 * KP))
#define MF32(a, b, c) __builtin_amdgcn_mfma_f32_32x32x16_bf16(a, b, c, 0, 0, 0)
#define EX2(s_, e) pk2(__builtin_amdgcn_exp2f((s_)[e]), __builtin_amdgcn_exp2f((s_)[(e) + 1]))
#pragma unroll 1
    for (int un = 0; un < 2; ++un) {
      float m0 = -INFINITY, m1 = -INFINITY;
      {
        bf16x8 kf[9][2];
#pragma unroll
        for (int T = 0; T < 3; ++T) { kf[T][0] = LDK(T, 0); kf[T][1] = LDK(T, 1); }
        f32x16 sp = z16;
#pragma unroll
        for (int T = 0; T < 10; ++T) {
          f32x16 sn = z16;
          if (T + 3 < 9) { kf[T + 3][0] = LDK(T + 3, 0); kf[T + 3][1] = LDK(T + 3, 1); }
          if (T < 9) sn = MF32(kf[T][1], q1, MF32(kf[T][0], q0, T == 0 ? mF : z16));
          if (T == 9) {
#pragma unroll
            for (int e = 0; e < 16; ++e) sp[e] = mF[e] == 0.f ? -INFINITY : sp[e];
          }
          if (T > 0) {
#pragma unroll
            for (int e = 0; e < 16; e += 4) { m0 = MAX3(m0, sp[e], sp[e + 1]); m1 = MAX3(m1, sp[e + 2], sp[e + 3]); }
          }
          SGB(0x100, 2); SGB(0x8, 1); SGB(0x2, 4); SGB(0x8, 1); SGB(0x2, 4);
          asm volatile("" :: "v"(sn), "v"(m0), "v"(m1));
          SB();
          sp = sn;
        }
      }
      float mx = fmaxf(m0, m1);
      mx = fmaxf(mx, __shfl_xor(mx, 32));
      f32x16 ca;
#pragma unroll
      for (int e = 0; e < 16; ++e) ca[e] = -mx;
      f32x16 oa = z16;
      {
        bf16x8 kc[2][2], vf[2][2];
        f32x16 sc[2];
        u32x4 pa[2] = {{0u, 0u, 0u, 0u}, {0u, 0u, 0u, 0u}};
        kc[0][0] = LDK(0, 0); kc[0][1] = LDK(0, 1); kc[1][0] = LDK(1, 0); kc[1][1] = LDK(1, 1);
        sc[0] = MF32(kc[0][1], q1, MF32(kc[0][0], q0, ca + mF));
        sc[1] = z16;
        asm volatile("" :: "v"(sc[0]));
        SB();
#pragma unroll
        for (int T = 0; T < 10; ++T) {
          const int c = T & 1, n = c ^ 1;
          if (T < 9) {
#pragma unroll
            for (int ks = 0; ks < 2; ++ks) vf[c][ks] = cat44(trread(vp + T * 512 + ks * 256), trread(vp + T * 512 + ks * 256 + 128));
          }
          if (T < 8) sc[n] = MF32(kc[n][1], q1, MF32(kc[n][0], q0, ca));
          if (T < 7) { kc[c][0] = LDK(T + 2, 0); kc[c][1] = LDK(T + 2, 1); }
          if (T > 0) oa = MF32(vf[n][1], __builtin_bit_cast(bf16x8, pa[1]), MF32(vf[n][0], __builtin_bit_cast(bf16x8, pa[0]), oa));
          if (T == 8) {
#pragma unroll
            for (int e = 0; e < 16; ++e) sc[c][e] = mF[e] == 0.f ? -INFINITY : sc[c][e];
          }
          if (T < 9) {
#pragma unroll
            for (int e = 0; e < 4; ++e) { pa[0][e] = EX2(sc[c], 2 * e); pa[1][e] = EX2(sc[c], 8 + 2 * e); }
          }
          SGB(0x100, 6);
          if (T == 8) SGB(0x2, 16);
          SGB(0x8, 1); SGB(0x400, 4); SGB(0x2, 1);
#pragma unroll
          for (int k = 0; k < 3; ++k) { SGB(0x8, 1); SGB(0x400, 2); SGB(0x2, 1); SGB(0x400, 2); SGB(0x2, 1); }
          SGB(0x2, 1);
          asm volatile("" :: "v"(sc[n]), "v"(oa), "v"(pa[0]), "v"(pa[1]));
          SB();
        }
      }
      const float il = __builtin_amdgcn_rcpf(oa[12]);
#pragma unroll
      for (int pl = 0; pl < 3; ++pl) {
        const bool ok = pl == 1 || (pl == 0 ? !(odd && hh == 0) : (odd || hh == 0));
        if (ok) {
          const u32x2 pkd = {pk2(oa[4 * pl] * il, oa[4 * pl + 1] * il), pk2(oa[4 * pl + 2] * il, oa[4 * pl + 3] * il)};
          __builtin_amdgcn_raw_buffer_store_b64(pkd, rs, pl * 1024 + ov, OFF_O + ((4 * qb + lw) * 80 + ch) * 1024, SC1);
        }
      }
      kp += 512; vp += ones ? 0 : 512; ov += 512;
      q0 = qf[1][0]; q1 = qf[1][1];
    }
    __builtin_amdgcn_s_setprio(0);
    lds_arrive(sync + 4);
    STAMP(6);
#pragma unroll
    for (int k = WOPRE; k < 5; ++k)
#pragma unroll
      for (int j = 0; j < 5; ++j) wpre[k][j] = ld16(rs, lane * 16, wb + k * 5120 + j * 1024);
#pragma unroll
    for (int j = 0; j < 2; ++j) xv[j] = *(const u32x2*)(x + orow + (3 + j) * 32);
    wait_vm<5 * (5 - WOPRE) + 2>();
    const bool last_o = lds_arrive(sync + 1) == 3;
    if (last_o && lane == 0) __hip_atomic_store((GAS u64*)(ws + FL_O + b * 128), tag, RLX_AGENT);
    const unsigned ab = OFF_O + (mt * 80 + 20 * lw) * 1024;
    {
      const int hd = 8 * lw + (lane & 7);
      poll_flags(ws + FL_O + (((((mt >> 2) << 2) | (hd & 3)) << 3) | (hd >> 2)) * 128, lane < 8, tag);
    }
    STAMP(7);
    bf16x8 af[3][4];
#pragma unroll
    for (int k = 0; k < 3; ++k)
#pragma unroll
      for (int i = 0; i < 4; ++i) af[k][i] = ld16(rs, va + i * 256, ab + k * 4096);
    SB();
#pragma unroll
    for (int i = 0; i < 4; ++i)
#pragma unroll
      for (int j = 0; j < 5; ++j) acc[i][j] = z4;
#pragma unroll
    for (int k = 0; k < 5; ++k) {
#pragma unroll
      for (int i = 0; i < 4; ++i)
#pragma unroll
        for (int j = 0; j < 5; ++j) acc[i][j] = mfma16(wpre[k][j], af[k % 3][i], acc[i][j]);
      SB();
      if (k < 2) {
#pragma unroll
        for (int i = 0; i < 4; ++i) af[k][i] = ld16(rs, va + i * 256, ab + (k + 3) * 4096);
        SB();
      }
    }
    STAMP(8);
    lds_wait(sync + 1, 4);
    lds_wait(sync + 6, 4);
#pragma unroll
    for (int i = 0; i < 4; ++i) {
#pragma unroll
      for (int j = 0; j < 3; ++j) *(LAS f32x4*)(smem + REDS1_OFF + ((i * 4 + lw) * 3 + j) * 1024 + lane * 16) = acc[i][j];
      if (i != lw) {
#pragma unroll
        for (int j = 0; j < 2; ++j)
          *(LAS f32x4*)(smem + REDS2_OFF + ((i * 3 + lw - (lw > i ? 1 : 0)) * 2 + j) * 1024 + lane * 16) = acc[i][3 + j];
      }
    }
    __syncthreads();
    STAMP(9);
#pragma unroll
    for (int j = 0; j < 2; ++j) {
      f32x4 sv = lw == 0 ? acc[0][3 + j] : (lw == 1 ? acc[1][3 + j] : (lw == 2 ? acc[2][3 + j] : acc[3][3 + j]));
#pragma unroll
      for (int k = 0; k < 3; ++k) sv += *(LAS f32x4*)(smem + REDS2_OFF + ((lw * 3 + k) * 2 + j) * 1024 + lane * 16);
      const f32x4 gt = *(LAS f32x4*)(smem + GX_OFF + (lw * 2 + j) * 1024 + lane * 16);
      *(u32x2*)(out + orow + (3 + j) * 32) = fin4(xv[j], sv, gt);
    }

  } else {
    if (idx * 4 + w < CODE_KB) {
      const u32x4 t0 = *(const u32x4*)(pc0 + (idx * 4 + w) * 1024 + lane * 16);
      asm volatile("" :: "v"(t0));
    }
    {
      const char* p = ws;
      asm volatile("" : "+s"(p));
      tag = *(const CAS u64*)(p + OFF_FL) + 1;
    }
    if (cg < 10) mfma_phase<3>(c, rs); else mfma_phase<4>(c, rs);
    STAMP(3);
    WAIT_VM(0);
    if (lds_arrive(sync + 2) == 3 && lane == 0) __hip_atomic_store((GAS u64*)(ws + FL_A + b * 128), tag, RLX_AGENT);
    if (b == 0 && w == 0) {
#pragma unroll 1
      for (int spins = 0; spins < (1 << 20); ++spins) {
        bool ok = true;
#pragma unroll
        for (int i = 0; i < 4; ++i) ok = ok && __hip_atomic_load((const GAS u64*)(ws + FL_W + (lane + 64 * i) * 128), RLX_AGENT) == tag;
        if (__all(ok)) break;
        __builtin_amdgcn_s_sleep(1);
      }
      if (lane == 0) __hip_atomic_store((GAS u64*)(ws + OFF_FL), tag, RLX_AGENT);
    }
    STAMP(4);
#pragma unroll
    for (int k = 0; k < 4; ++k)
#pragma unroll
      for (int j = 0; j < 5; ++j) wpre[k][j] = ld16(rs, lane * 16, wb + k * 5120 + j * 1024);
    const unsigned wl_ = lds0 + SLAB_OFF + w * 24576;
    const unsigned ab = OFF_H + (mt * 192 + 48 * w) * 1024;
    poll_flags(ws + FL_A + wg_of(mt >> 1, 10 + (24 * w + (lane & 1) * 23) / 16) * 128, lane < 2, tag);
    lds_wait(sync + 5, 4);
    STAMP(7);
    bf16x8 af[3][4];
#pragma unroll
    for (int k = 0; k < 3; ++k)
#pragma unroll
      for (int i = 0; i < 4; ++i) af[k][i] = ld16(rs, va + i * 256, ab + k * 4096);
    SB();
#pragma unroll
    for (int i = 0; i < 4; ++i)
#pragma unroll
      for (int j = 0; j < 5; ++j) acc[i][j] = f32x4{0.f, 0.f, 0.f, 0.f};
    STAMP(12);
#pragma unroll
    for (int k = 0; k < 12; ++k) {
      const int cs = k < 4 ? k : (k < 8 ? 4 - (k & 1) : (k < 11 ? k - 8 : 4));
      if (k >= 3 && k < 7) {
#pragma unroll
        for (int j = 0; j < 5; ++j) wpre[4 - ((k + 1) & 1)][j] = *(LAS bf16x8*)(size_t)(wl_ + (k - 3) * 5120 + j * 1024 + lane * 16);
      }
#pragma unroll
      for (int i = 0; i < 4; ++i)
#pragma unroll
        for (int j = 0; j < 5; ++j) acc[i][j] = mfma16(wpre[cs][j], af[k % 3][i], acc[i][j]);
      SB();
      if (k + 3 < 12) {
#pragma unroll
        for (int i = 0; i < 4; ++i) af[k % 3][i] = ld16(rs, va + i * 256, ab + (k + 3) * 4096);
      }
      if (k < 3 || k == 6) {
#pragma unroll
        for (int j = 0; j < 5; ++j) wpre[k < 3 ? k : 4][j] = ld16(rs, lane * 16, wb + (k < 3 ? k + 8 : 11) * 5120 + j * 1024);
      }
      SB();
    }
    lds_arrive(sync + 6);
    lds_wait(sync + 6, 4);
    STAMP(8);
    red_write(smem + REDG_OFF, w, lane, acc);
    u32x2 bb[5];
#pragma unroll
    for (int j = 0; j < 5; ++j) {
      if (j < 3) xv[j] = *(const u32x2*)(x + orow + j * 32);
      bb[j] = *(const u32x2*)(b2 + (80 * ct + 16 * j + 4 * g) * 2);
    }
    WAIT_LGKM0();
    lds_arrive(sync + 3);
    lds_wait(sync + 3, 4);
    f32x4 gt[5];
    red_sum(smem + REDG_OFF, w, lane, acc, gt);
#pragma unroll
    for (int j = 0; j < 5; ++j) {
      const float bv[4] = {bflo(bb[j][0]), bfhi(bb[j][0]), bflo(bb[j][1]), bfhi(bb[j][1])};
#pragma unroll
      for (int e = 0; e < 4; ++e) {
        const float t = rbf(rbf(gt[j][e]) + bv[e]);
        gt[j][e] = rbf(t * __builtin_amdgcn_rcpf(1.f + __expf(-t)));
      }
      if (j >= 3) *(LAS f32x4*)(smem + GX_OFF + (w * 2 + j - 3) * 1024 + lane * 16) = gt[j];
    }
    __syncthreads();
    STAMP(9);
#pragma unroll
    for (int j = 0; j < 3; ++j) {
      f32x4 sv = *(LAS f32x4*)(smem + REDS1_OFF + ((w * 4) * 3 + j) * 1024 + lane * 16);
#pragma unroll
      for (int k = 1; k < 4; ++k) sv += *(LAS f32x4*)(smem + REDS1_OFF + ((w * 4 + k) * 3 + j) * 1024 + lane * 16);
      *(u32x2*)(out + orow + j * 32) = fin4(xv[j], sv, gt[j]);
    }
  }
  STAMP(10);
}

extern "C" void kernel_launch(void* const* d_in, const int* in_sizes, int n_in, void* d_out, int out_size, void* d_ws,
                              size_t ws_size, hipStream_t stream) {
  (void)hipFuncSetAttribute((const void*)k_all, hipFuncAttributeMaxDynamicSharedMemorySize, LDS_ALL);
  k_all<<<256, 512, LDS_ALL, stream>>>((const char*)d_in[0], (const char*)d_in[1], (const char*)d_in[4], (const char*)d_in[5],
                                       (const char*)d_in[6], (const char*)d_in[2], (const char*)d_in[7], (char*)d_ws, (char*)d_out);
}
```

```cpp
#include <hip/hip_runtime.h>
#include <stdint.h>

#define LAS __attribute__((address_space(3)))
#define GAS __attribute__((address_space(1)))
#define CAS __attribute__((address_space(4)))
typedef short bf16x8 __attribute__((ext_vector_type(8)));
typedef short s16x4 __attribute__((ext_vector_type(4)));
typedef float f32x4 __attribute__((ext_vector_type(4)));
typedef unsigned u32x4 __attribute__((ext_vector_type(4)));
typedef unsigned u32x2 __attribute__((ext_vector_type(2)));
typedef __bf16 bf16x2_t __attribute__((ext_vector_type(2)));
typedef float f32x2 __attribute__((ext_vector_type(2)));
typedef float f32x16 __attribute__((ext_vector_type(16)));
typedef unsigned long long u64;

constexpr unsigned OFF_FL = 0;
constexpr unsigned FL_W = 1024, FL_Q = FL_W + 32768, FL_H = FL_Q + 32768, FL_O = FL_H + 32768;
constexpr unsigned OFF_RS = 262144;
constexpr unsigned OFF_CS = OFF_RS + 16 * 10 * 2048;
constexpr unsigned OFF_QKV = OFF_CS + 2 * 16 * 10 * 1024;
constexpr unsigned OFF_H = OFF_QKV + 32 * 240 * 1024;
constexpr unsigned OFF_O = OFF_H + 32 * 192 * 1024;
constexpr unsigned OFF_W2F = OFF_O + 32 * 80 * 1024;
constexpr unsigned OFF_WOF = OFF_W2F + 8 * 48 * 5120;
constexpr unsigned WS_USED = OFF_WOF + 8 * 20 * 5120;

__device__ __forceinline__ f32x4 mfma16(bf16x8 a, bf16x8 b, f32x4 c) {
  return __builtin_amdgcn_mfma_f32_16x16x32_bf16(a, b, c, 0, 0, 0);
}
__device__ __forceinline__ unsigned pk2(float a, float b) {
  f32x2 v = {a, b};
  bf16x2_t r = __builtin_convertvector(v, bf16x2_t);
  return __builtin_bit_cast(unsigned, r);
}
__device__ __forceinline__ float rbf(float a) { return __builtin_bit_cast(float, pk2(0.f, a) & 0xffff0000u); }
__device__ __forceinline__ float bflo(unsigned u) { return __builtin_bit_cast(float, u << 16); }
__device__ __forceinline__ float bfhi(unsigned u) { return __builtin_bit_cast(float, u & 0xffff0000u); }
__device__ __forceinline__ u32x2 fin4(u32x2 xv, f32x4 sv, f32x4 gt) {
  return u32x2{pk2(bflo(xv[0]) + rbf(rbf(sv[0]) * gt[0]), bfhi(xv[0]) + rbf(rbf(sv[1]) * gt[1])),
               pk2(bflo(xv[1]) + rbf(rbf(sv[2]) * gt[2]), bfhi(xv[1]) + rbf(rbf(sv[3]) * gt[3]))};
}
__device__ __forceinline__ s16x4 trread(unsigned a) {
  return __builtin_amdgcn_ds_read_tr16_b64_v4i16((LAS s16x4*)(size_t)a);
}
__device__ __forceinline__ bf16x8 cat44(s16x4 a, s16x4 b) {
  return __builtin_shufflevector(a, b, 0, 1, 2, 3, 4, 5, 6, 7);
}
#define WAIT_VM(n) asm volatile("s_waitcnt vmcnt(" #n ")" ::: "memory")
#define WAIT_LGKM0() __builtin_amdgcn_s_waitcnt(0xC07F)
#define BAR() __builtin_amdgcn_s_barrier()
#define SB() __builtin_amdgcn_sched_barrier(0)
#define SGB(m, n) __builtin_amdgcn_sched_group_barrier(m, n, 0)
#define MAX3(a, b, c) __builtin_fmaxf(__builtin_fmaxf(a, b), c)
#define RLX_AGENT __ATOMIC_RELAXED, __HIP_MEMORY_SCOPE_AGENT
constexpr int SC1 = 16;
constexpr int CODE_KB = 39;
template <int N> struct IC { static constexpr int value = N; };

#define STAMP(i)
#define CLK(v)

__device__ __forceinline__ void dma1(const char* sbase, unsigned voff, unsigned lds) {
  unsigned keep;
  asm volatile("s_mov_b32 %0, m0\n\ts_mov_b32 m0, %3\n\ts_nop 0\n\tglobal_load_lds_dwordx4 %1, %2\n\ts_mov_b32 m0, %0"
               : "=&s"(keep) : "v"(voff), "s"(sbase), "s"(lds) : "memory");
}
__device__ __forceinline__ void dma_stage(const char* sa, unsigned va0, unsigned va1, unsigned va2, unsigned va3, unsigned la,
                                          const char* sb, unsigned vb0, unsigned vb1, unsigned lb) {
  unsigned keep;
  asm volatile("s_mov_b32 %0, m0\n\ts_mov_b32 m0, %10\n\ts_nop 0\n\tglobal_load_lds_dwordx4 %8, %7\n\t"
               "s_add_u32 m0, m0, 1024\n\ts_nop 0\n\tglobal_load_lds_dwordx4 %9, %7\n\t"
               "s_add_u32 m0, m0, 7040\n\ts_nop 0\n\tglobal_load_lds_dwordx4 %8, %7 offset:128\n\t"
               "s_add_u32 m0, m0, 1024\n\ts_nop 0\n\tglobal_load_lds_dwordx4 %9, %7 offset:128\n\t"
               "s_mov_b32 m0, %6\n\ts_nop 0\n\tglobal_load_lds_dwordx4 %2, %1\n\t"
               "s_add_u32 m0, m0, 1024\n\ts_nop 0\n\tglobal_load_lds_dwordx4 %3, %1\n\t"
               "s_add_u32 m0, m0, 1024\n\ts_nop 0\n\tglobal_load_lds_dwordx4 %4, %1\n\t"
               "s_add_u32 m0, m0, 1024\n\ts_nop 0\n\tglobal_load_lds_dwordx4 %5, %1\n\ts_mov_b32 m0, %0"
               : "=&s"(keep) : "s"(sa), "v"(va0), "v"(va1), "v"(va2), "v"(va3), "s"(la), "s"(sb), "v"(vb0), "v"(vb1), "s"(lb)
               : "memory", "scc");
}
__device__ __forceinline__ void dma_kv(const char* sb, unsigned vk, unsigned vv, unsigned l) {
  unsigned keep;
  asm volatile("s_mov_b32 %0, m0\n\ts_mov_b32 m0, %4\n\ts_nop 0\n\tglobal_load_lds_dwordx4 %2, %1 sc1\n\t"
               "s_add_u32 m0, m0, 7168\n\ts_nop 0\n\tglobal_load_lds_dwordx4 %2, %1 offset:1024 sc1\n\t"
               "s_add_u32 m0, m0, 7168\n\ts_nop 0\n\tglobal_load_lds_dwordx4 %2, %1 offset:2048 sc1\n\t"
               "s_add_u32 m0, m0, 18432\n\ts_nop 0\n\tglobal_load_lds_dwordx4 %3, %1 sc1\n\t"
               "s_add_u32 m0, m0, 7232\n\ts_nop 0\n\tglobal_load_lds_dwordx4 %3, %1 offset:1024 sc1\n\t"
               "s_add_u32 m0, m0, 7232\n\ts_nop 0\n\tglobal_load_lds_dwordx4 %3, %1 offset:2048 sc1\n\t"
               "s_mov_b32 m0, %0"
               : "=&s"(keep) : "s"(sb), "v"(vk), "v"(vv), "s"(l) : "memory", "scc");
}
__device__ __forceinline__ void dma5_sc1(const char* sb, unsigned v, unsigned l) {
  unsigned keep;
  asm volatile("s_mov_b32 %0, m0\n\ts_mov_b32 m0, %3\n\ts_nop 0\n\tglobal_load_lds_dwordx4 %2, %1 sc1\n\t"
               "global_load_lds_dwordx4 %2, %1 offset:1024 sc1\n\tglobal_load_lds_dwordx4 %2, %1 offset:2048 sc1\n\t"
               "global_load_lds_dwordx4 %2, %1 offset:3072 sc1\n\ts_add_u32 m0, m0, 1024\n\ts_nop 0\n\t"
               "global_load_lds_dwordx4 %4, %1 offset:3072 sc1\n\ts_mov_b32 m0, %0"
               : "=&s"(keep) : "s"(sb), "v"(v), "s"(l), "v"(v + 1024) : "memory", "scc");
}
__device__ __forceinline__ u32x2 aload8(const char* sbase, unsigned voff) {
  u32x2 r;
  asm volatile("global_load_dwordx2 %0, %1, %2 sc1" : "=v"(r) : "v"(voff), "s"(sbase) : "memory");
  return r;
}
__device__ __forceinline__ u32x4 aload16(const char* sbase, unsigned voff) {
  u32x4 r;
  asm volatile("global_load_dwordx4 %0, %1, %2 sc1" : "=v"(r) : "v"(voff), "s"(sbase) : "memory");
  return r;
}
#define PIN(x) asm volatile("" : "+v"(x))
template <int N> __device__ __forceinline__ void wait_vm() { asm volatile("s_waitcnt vmcnt(%0)" :: "i"(N) : "memory"); }
__device__ __forceinline__ void poll_flags(const char* flag, bool need, u64 tag) {
#pragma unroll 1
  for (int spins = 0; spins < (1 << 20); ++spins) {
    const u64 v = need ? __hip_atomic_load((const GAS u64*)flag, RLX_AGENT) : tag;
    if (__all(v == tag)) break;
    __builtin_amdgcn_s_sleep(1);
  }
  __builtin_amdgcn_fence(__ATOMIC_ACQUIRE, "wavefront");
}
__device__ __forceinline__ unsigned lds_arrive(LAS unsigned* c) {
  unsigned r = 0;
  if ((threadIdx.x & 63) == 0) r = __hip_atomic_fetch_add(c, 1u, __ATOMIC_RELAXED, __HIP_MEMORY_SCOPE_WORKGROUP);
  return __builtin_amdgcn_readfirstlane(r);
}
__device__ __forceinline__ void lds_wait_sleep(LAS unsigned* c, unsigned n) {
#pragma unroll 1
  for (int spins = 0; spins < (1 << 22); ++spins) {
    if (__builtin_amdgcn_readfirstlane(__hip_atomic_load(c, __ATOMIC_RELAXED, __HIP_MEMORY_SCOPE_WORKGROUP)) >= n) break;
    __builtin_amdgcn_s_sleep(2);
  }
  __builtin_amdgcn_fence(__ATOMIC_ACQUIRE, "wavefront");
}
__device__ __forceinline__ void lds_wait(LAS unsigned* c, unsigned n) {
#pragma unroll 1
  for (int spins = 0; spins < (1 << 22); ++spins) {
    if (__builtin_amdgcn_readfirstlane(__hip_atomic_load(c, __ATOMIC_RELAXED, __HIP_MEMORY_SCOPE_WORKGROUP)) >= n) break;
  }
  __builtin_amdgcn_fence(__ATOMIC_ACQUIRE, "wavefront");
}

#define WOPRE 4
#define W2HOLD 8
constexpr int STG = 32768, RT_OFF = 4 * STG, LDS_ALL = 163840;
constexpr int B1_OFF = 159744;
constexpr int ST_OFF = 160768;
constexpr int CS_OFF = ST_OFF + 1024, SYNC_OFF = ST_OFF + 2048;
constexpr int KP = 8192, VP = 8192 + 64;
constexpr int K_OFF = 0, V_OFF = K_OFF + 4 * KP;
__device__ __forceinline__ constexpr int slab_of(int w) { return 65536 + w * 20480; }
constexpr int REDG_OFF = 0, GX_OFF = 61440, REDS1_OFF = GX_OFF + 8192, REDS2_OFF = REDS1_OFF + 49152;

__device__ __forceinline__ void st16(u32x4 v, __amdgpu_buffer_rsrc_t rs, unsigned voff, unsigned soff) {
  __builtin_amdgcn_raw_buffer_store_b128(v, rs, voff + soff, 0, SC1);
}
__device__ __forceinline__ bf16x8 ld16(__amdgpu_buffer_rsrc_t rs, unsigned voff, unsigned soff) {
  return __builtin_bit_cast(bf16x8, __builtin_amdgcn_raw_buffer_load_b128(rs, voff, soff, SC1));
}
__device__ __forceinline__ void red_write(LAS char* red, int kw, int lane, const f32x4 (&acc)[4][5]) {
#pragma unroll
  for (int i = 0; i < 4; ++i) {
    if (i != kw) {
      const int slot = kw - (kw > i ? 1 : 0);
#pragma unroll
      for (int j = 0; j < 5; ++j) *(LAS f32x4*)(red + ((i * 3 + slot) * 5 + j) * 1024 + lane * 16) = acc[i][j];
    }
  }
}
__device__ __forceinline__ void red_sum(LAS char* red, int kw, int lane, const f32x4 (&acc)[4][5], f32x4 (&o)[5]) {
#pragma unroll
  for (int j = 0; j < 5; ++j) {
    f32x4 a = kw == 0 ? acc[0][j] : (kw == 1 ? acc[1][j] : (kw == 2 ? acc[2][j] : acc[3][j]));
#pragma unroll
    for (int s = 0; s < 3; ++s) a += *(LAS f32x4*)(red + ((kw * 3 + s) * 5 + j) * 1024 + lane * 16);
    o[j] = a;
  }
}
__device__ __forceinline__ int wg_of(int rt, int cg) { return (((cg & 3) * 8 + (rt & 7)) << 3) | ((cg >> 2) << 1) | (rt >> 3); }
template <int N>
__device__ __forceinline__ bool sum_pairs(const u32x4 (&v)[N], unsigned last_on, unsigned tg, float& a, float& b) {
  bool ok = true;
  a = 0.f; b = 0.f;
#pragma unroll
  for (int k = 0; k < N; ++k) {
    ok = ok && v[k][1] == tg && v[k][3] == tg;
    const unsigned m = k == N - 1 ? last_on : 0xffffffffu;
    a += __uint_as_float(v[k][0] & m);
    b += __uint_as_float(v[k][2] & m);
  }
  return __all(ok);
}
template <int N>
__device__ __forceinline__ void regather(__amdgpu_buffer_rsrc_t rs, const unsigned (&vo)[N], unsigned last_on, unsigned tg, float& a, float& b) {
#pragma unroll 1
  for (int spins = 0; spins < (1 << 20); ++spins) {
    u32x4 v[N];
#pragma unroll
    for (int k = 0; k < N; ++k) v[k] = __builtin_amdgcn_raw_buffer_load_b128(rs, vo[k], 0, SC1);
    if (sum_pairs<N>(v, last_on, tg, a, b)) break;
    __builtin_amdgcn_s_sleep(2);
  }
}

#define STG_STAMP(s_)

#define LDX(set, i, ks) xf[set][i] = *(LAS bf16x8*)(size_t)(xa[ks] + (i) * 2048)
#define LDW(set, j, ks) wf[set][j] = cat44(trread(wa[j] + (ks) * 4096), trread(wa[j] + (ks) * 4096 + 512))
#define HALF(set, ks) do { \
    _Pragma("unroll") for (int j = 0; j < NT; ++j) { \
      if (j == 0) { _Pragma("unroll") for (int i = 0; i < 4; ++i) LDX((set) ^ 1, i, ks); } \
      else if (2 * j - 2 < NT) { LDW((set) ^ 1, 2 * j - 2, ks); if (2 * j - 1 < NT) LDW((set) ^ 1, 2 * j - 1, ks); } \
      _Pragma("unroll") for (int i = 0; i < 4; ++i) acc[i][j] = mfma16(wf[set][j], xf[set][i], acc[i][j]); \
      SGB(0x8, 1); SGB(0x100, 1); SGB(0x8, 1); SGB(0x100, 1); SGB(0x8, 1); SGB(0x100, 1); SGB(0x8, 1); SGB(0x100, 1); \
      asm volatile("" :: "v"(acc[0][j]), "v"(acc[1][j]), "v"(acc[2][j]), "v"(acc[3][j])); \
      SB(); \
    } } while (0)
template <int NT, int S0>
__device__ __forceinline__ void mfma_part(unsigned (&xa)[2], unsigned wl, int tl0, int sigl, unsigned lds0, float (&sh)[4], float (&sc)[4],
                                          int row0, float qs, __amdgpu_buffer_rsrc_t rs, unsigned vo, unsigned so, int cs0, int g, char* ws) {
  constexpr bool ISH = S0 == 10;
  unsigned wa[NT];
  f32x4 acc[4][NT];
#pragma unroll
  for (int j = 0; j < NT; ++j) {
    const int tl = tl0 + j;
    wa[j] = wl + (S0 & 3) * STG + (tl >> 2) * 8192 + (((tl & 3) ^ sigl) << 5);
#pragma unroll
    for (int i = 0; i < 4; ++i) acc[i][j] = f32x4{0.f, 0.f, 0.f, 0.f};
  }
  bf16x8 xf[2][4], wf[2][NT];
#pragma unroll
  for (int i = 0; i < 4; ++i) LDX(0, i, 0);
#pragma unroll
  for (int j = 0; j < NT; ++j) LDW(0, j, 0);
  WAIT_LGKM0();
  int buf = S0 & 3;
#pragma unroll 1
  for (int t = 0; t < 10; ++t) {
    HALF(0, 1);
    {
      const int d = buf == 3 ? -3 * STG : STG;
      buf = (buf + 1) & 3;
      xa[0] += d; xa[1] += d;
#pragma unroll
      for (int j = 0; j < NT; ++j) wa[j] += d;
    }
    WAIT_LGKM0();
    if (ISH) {
      asm volatile("s_cmp_lg_u32 %0, 1\n\ts_cbranch_scc1 .Lk%=\n\ts_waitcnt vmcnt(0)\n.Lk%=:\n\t"
                   "s_cmp_ge_u32 %0, 8\n\ts_cbranch_scc1 .Lc%=\n\ts_barrier\n.Lc%=:" :: "s"(t) : "memory", "scc");
    } else BAR();
    STG_STAMP(S0 + t);
    SB();
    HALF(1, 0);
    WAIT_LGKM0();
  }
  STAMP(ISH ? 2 : 13);
  lds_wait((LAS unsigned*)(size_t)(lds0 + SYNC_OFF + (ISH ? 32 : 28)), 4);
  if (!ISH) {
#pragma unroll
    for (int i = 0; i < 4; ++i) {
      const f32x2 st = *(LAS f32x2*)(size_t)(lds0 + ST_OFF + (row0 + 16 * i) * 8);
      sc[i] = st[1]; sh[i] = -st[0] * st[1];
    }
  }
#pragma unroll
  for (int j = 0; j < NT; ++j) {
    const f32x4 c4 = *(LAS f32x4*)(size_t)(lds0 + CS_OFF + (ISH ? 512 : 0) + ((cs0 + j) * 16 + 4 * g) * 4);
    f32x4 bv = {0.f, 0.f, 0.f, 0.f};
    if (ISH) {
      const u32x2 bb = *(LAS u32x2*)(size_t)(lds0 + B1_OFF + ((cs0 + j) * 16 + 4 * g) * 2);
      bv = f32x4{bflo(bb[0]), bfhi(bb[0]), bflo(bb[1]), bfhi(bb[1])};
    }
#pragma unroll
    for (int pp = 0; pp < 2; ++pp) {
      float v[2][4];
#pragma unroll
      for (int i = 0; i < 2; ++i)
#pragma unroll
        for (int r = 0; r < 4; ++r) {
          float t = __builtin_fmaf(sc[2 * pp + i], acc[2 * pp + i][j][r], __builtin_fmaf(sh[2 * pp + i], c4[r], bv[r]));
          if (ISH) { t = t > 0.f ? t : 0.f; t = t * t; } else t *= qs;
          v[i][r] = t;
        }
      const unsigned x0 = pk2(v[0][0], v[0][1]), x1 = pk2(v[0][2], v[0][3]);
      const unsigned y0 = pk2(v[1][0], v[1][1]), y1 = pk2(v[1][2], v[1][3]);
      const auto s0 = __builtin_amdgcn_permlane16_swap(x0, y0, false, false);
      const auto s1 = __builtin_amdgcn_permlane16_swap(x1, y1, false, false);
      const u32x4 o = {s0[0], s1[0], s0[1], s1[1]};
      st16(o, rs, vo + pp * 512, so + 2 * j * 1024);
    }
  }
}

__global__ __launch_bounds__(512, 2) void k_all(const char* __restrict__ x, const char* __restrict__ wqkv, const char* __restrict__ w1,
                                                const char* __restrict__ b1, const char* __restrict__ w2, const char* __restrict__ wo,
                                                const char* __restrict__ b2, char* ws, char* __restrict__ out) {
  extern __shared__ __attribute__((aligned(16))) char smem_[];
  LAS char* smem = (LAS char*)smem_;
  const int tid = threadIdx.x, lane = tid & 63, w = __builtin_amdgcn_readfirstlane(tid >> 6);
  const int b = blockIdx.x, xcd = b & 7, idx = b >> 3;
  const unsigned lds0 = (unsigned)(size_t)smem;
  asm volatile("" :: "s"(x), "s"(wqkv), "s"(w1), "s"(b1), "s"(w2), "s"(wo), "s"(ws));
  u64 pc_;
  asm volatile("s_getpc_b64 %0" : "=s"(pc_));
  const int rt = (xcd & 1) * 8 + (idx & 7), cg = (xcd >> 1) * 4 + (idx >> 3);
  const int rot = (rt + 3 * cg) % 10;
  const int lw = w - 4, lr = lane >> 3, ls = lane & 7;
  const int bk = 16 * lw + lr, sw0 = (ls ^ (bk & 2)) << 4, sw1 = sw0 ^ 64;
  const unsigned vq0 = bk * 3840 + (cg < 15 ? cg : 14) * 256 + sw0, vq1 = (bk + 8) * 3840 + (cg < 15 ? cg : 14) * 256 + sw1;
  const unsigned vA = (rt * 128 + 32 * lw + lr) * 1280 + ((ls ^ (lr >> 1)) << 4);
  const unsigned vA1 = (vA + 10240) ^ 64u, vA2 = vA + 20480, vA3 = (vA + 30720) ^ 64u;
  const int rkb = b / 5, rnb = b - 5 * rkb;
  if (w >= 4) {
    dma_stage(x + rot * 128, vA, vA1, vA2, vA3, lds0 + lw * 4096, wqkv + (size_t)rot * (64 * 3840), vq0, vq1, lds0 + 16384 + lw * 2048);
    if (b < 170) {
      const char* Wr = rkb < 24 ? w2 + (size_t)rkb * 64 * 1280 : wo + (size_t)(rkb - 24) * 64 * 1280;
      const unsigned r0 = bk * 1280 + rnb * 256 + sw0, r1 = (bk + 8) * 1280 + rnb * 256 + sw1;
      dma1(Wr, r0, lds0 + RT_OFF + lw * 2048);
      dma1(Wr, r1, lds0 + RT_OFF + lw * 2048 + 1024);
      dma1(Wr + 128, r0, lds0 + RT_OFF + 8192 + lw * 2048);
      dma1(Wr + 128, r1, lds0 + RT_OFF + 8192 + lw * 2048 + 1024);
    }
#pragma unroll
    for (int s = 1; s < 4; ++s) {
      const int kt = rot + s >= 10 ? rot + s - 10 : rot + s;
      dma_stage(x + kt * 128, vA, vA1, vA2, vA3, lds0 + s * STG + lw * 4096, wqkv + (size_t)kt * (64 * 3840), vq0, vq1,
                lds0 + s * STG + 16384 + lw * 2048);
    }
  }
  SB();
  const int g = lane >> 4, l15 = lane & 15, q4 = l15 >> 2, p4 = lane & 3;
  const char* pc0 = (const char*)(pc_ & ~(u64)63);
  const __amdgpu_buffer_rsrc_t rs = __builtin_amdgcn_make_buffer_rsrc(ws, 0, WS_USED, 0x00020000);
  LAS unsigned* sync = (LAS unsigned*)(smem + SYNC_OFF);
  STAMP(0);
  u64 tag;
  {
    const char* p = ws;
    asm volatile("" : "+s"(p));
    tag = *(const CAS u64*)(p + OFF_FL) + 1;
  }
  const int lu1 = (6 * cg) & 3;
  const int sigl = ((q4 >> 1) & 1) | ((g & 1) << 1), xsw = g ^ (l15 >> 1);
  const int mt = (xcd >> 1) * 8 + (idx & 7), ct = (xcd & 1) * 4 + (idx >> 3);
  const int head = 4 * xcd + (idx & 3), qb = idx >> 2;
  const int ch = (5 * head) >> 1, odd = head & 1;
  const size_t orow = (size_t)(64 * mt + 16 * (w & 3) + l15) * 1280 + (80 * ct + 4 * g) * 2;
  u32x2 xv[3];
  f32x4 acc[4][5];
  const unsigned va = g * 1024 + l15 * 16;
  bf16x8 wpre[5][5];
  const unsigned wb = w < 4 ? OFF_W2F + (ct * 48 + 12 * w) * 5120 : OFF_WOF + (ct * 20 + 5 * (w - 4)) * 5120;

  if (w >= 4) {
    const int lt = tid - 256;
    const unsigned tg = (unsigned)tag;
    const unsigned vh0 = bk * 3072 + ((6 * cg) >> 2) * 128 + sw0, vh1 = (bk + 8) * 3072 + ((6 * cg) >> 2) * 128 + sw1;
    int ktn = rot + 4 >= 10 ? rot - 6 : rot + 4;
    auto issue = [&](auto S) {
      constexpr int s = decltype(S)::value;
      const unsigned dst = lds0 + (s & 3) * STG;
      const char* sw = s >= 10 ? w1 + (size_t)ktn * (64 * 3072) : wqkv + (size_t)ktn * (64 * 3840);
      dma_stage(x + ktn * 128, vA, vA1, vA2, vA3, dst + lw * 4096, sw, s >= 10 ? vh0 : vq0, s >= 10 ? vh1 : vq1, dst + 16384 + lw * 2048);
      ktn = ktn == 9 ? 0 : ktn + 1;
    };
    if (lt < 16) sync[lt] = 0u;
    WAIT_VM(24);
    BAR();
    STAMP(1);
    const bf16x8 ONE8 = {0x3f80, 0x3f80, 0x3f80, 0x3f80, 0x3f80, 0x3f80, 0x3f80, 0x3f80};
    const f32x4 z4 = {0.f, 0.f, 0.f, 0.f};
    bf16x8 sw_[2][2], sx_[2][2];
    u32x4 gr[5], gc[3];
    u32x2 fl;
    const int fkb = lane < 12 ? 6 * lw + (lane >> 1) : 24 + ((5 * lw) >> 1) + (lane < 18 ? (lane - 12) >> 1 : 0);
    const unsigned fW = FL_W + (fkb * 5 + ((5 * ct + (lane & 1) * 4) >> 3)) * 128;
    const int ty = lane < 24 ? lane >> 3 : 0, rr0 = 2 * qb - 2 + ((lane >> 1) & 3);
    const int rr = rr0 < 0 ? 0 : (ty == 0 && rr0 < 2 * qb ? 2 * qb : rr0);
    const unsigned fQ = FL_Q + wg_of(rr, 5 * ty + ((ch + (lane & 1) * 2) >> 4)) * 128;
    unsigned ro[5], co[3];
#pragma unroll
    for (int k = 0; k < 5; ++k) ro[k] = OFF_RS + (rt * 10 + 2 * k + (lane >> 5)) * 2048 + (32 * lw + (lane & 31)) * 16;
#pragma unroll
    for (int k = 0; k < 3; ++k) co[k] = OFF_CS + (cg * 10 + (k == 2 && g >= 2 ? g : g + 4 * k)) * 1024 + (16 * lw + l15) * 16;
    const unsigned c_on = g < 2 ? 0xffffffffu : 0u;
    auto col_sums = [&](int part) {
#pragma unroll
      for (int k = 0; k < 3; ++k) PIN(gc[k]);
      float c0, c1;
      if (!sum_pairs<3>(gc, c_on, tg, c0, c1)) regather<3>(rs, co, c_on, tg, c0, c1);
      c0 += __shfl_xor(c0, 16); c1 += __shfl_xor(c1, 16);
      c0 += __shfl_xor(c0, 32); c1 += __shfl_xor(c1, 32);
      if (lane < 16) *(LAS f32x2*)(smem + CS_OFF + part * 512 + (16 * lw + lane) * 8) = f32x2{c0, c1};
      WAIT_LGKM0();
      lds_arrive(sync + 7 + part);
    };
    auto stage = [&](auto S) {
      constexpr int s = decltype(S)::value;
      if constexpr (s == 0 || s == 10) {
        const unsigned sb = lds0 + (s & 3) * STG;
#pragma unroll
        for (int i = 0; i < 2; ++i) {
          const int lux = ((s == 0 ? 0 : lu1) + 2 * lw + i) & 7;
          const unsigned wq = sb + 16384 + (lux >> 2) * 8192 + (8 * g + q4) * 128 + (((lux & 3) ^ sigl) << 5) + p4 * 8;
          sw_[i][0] = cat44(trread(wq), trread(wq + 512));
          sw_[i][1] = cat44(trread(wq + 4096), trread(wq + 4608));
          if (s == 0) {
            const unsigned xq = sb + (32 * lw + 16 * i + l15) * 128;
            sx_[i][0] = *(LAS bf16x8*)(size_t)(xq + (xsw << 4));
            sx_[i][1] = *(LAS bf16x8*)(size_t)(xq + ((xsw ^ 4) << 4));
          }
        }
      }
      wait_vm<(s == 6 || s == 7) ? 24 : (s >= 10 && s <= 12 ? 16 + 5 * WOPRE : ((s == 15 || s == 16) ? 19 : (s == 17 ? 0 : 16)))>();
      WAIT_LGKM0();
      BAR();
      if constexpr (s < 16) issue(IC<s + 4>{});
      if constexpr (s == 2 || s == 3) {
        if (b < 170) {
          const unsigned dstb = rkb < 24 ? OFF_W2F : OFF_WOF;
          const int nks = rkb < 24 ? 48 : 20, kb = rkb < 24 ? rkb : rkb - 24;
          const int nl = 2 * lw + s - 2, nt = 8 * rnb + nl, c5 = nt / 5, jj = nt - 5 * c5;
          const unsigned rp = lds0 + RT_OFF + (nl >> 2) * 8192 + (8 * g + q4) * 128 + (((nl & 3) ^ sigl) << 5) + p4 * 8;
#pragma unroll
          for (int ks = 0; ks < 2; ++ks) {
            const bf16x8 f = cat44(trread(rp + ks * 4096), trread(rp + ks * 4096 + 512));
            st16(__builtin_bit_cast(u32x4, f), rs, lane * 16, dstb + ((c5 * nks + 2 * kb + ks) * 5 + jj) * 1024);
          }
        }
      }
      if constexpr (s == 0 || s == 10) {
        __builtin_amdgcn_s_setprio(3);
        const u32x4 c0 = __builtin_bit_cast(u32x4, mfma16(sw_[0][1], ONE8, mfma16(sw_[0][0], ONE8, z4)));
        const u32x4 c1 = __builtin_bit_cast(u32x4, mfma16(sw_[1][1], ONE8, mfma16(sw_[1][0], ONE8, z4)));
        const u32x4 cu = l15 == 0 ? c0 : c1;
        if (l15 < 2) {
          const unsigned o = OFF_CS + (((s == 10 ? 16 : 0) + cg) * 10 + rot) * 1024 + 2 * lw * 128;
          st16(u32x4{cu[0], tg, cu[1], tg}, rs, l15 * 128 + g * 32, o);
          st16(u32x4{cu[2], tg, cu[3], tg}, rs, l15 * 128 + g * 32 + 16, o);
        }
        __builtin_amdgcn_s_setprio(0);
      }
      if constexpr (s == 1) {
        __builtin_amdgcn_s_setprio(3);
#pragma unroll
        for (int i = 0; i < 2; ++i) {
          const f32x4 sx = mfma16(ONE8, sx_[i][1], mfma16(ONE8, sx_[i][0], z4));
          const f32x4 sq = mfma16(sx_[i][1], sx_[i][1], mfma16(sx_[i][0], sx_[i][0], z4));
          const int r = l15 & 3;
          const float sxx = r == 0 ? sq[0] : (r == 1 ? sq[1] : (r == 2 ? sq[2] : sq[3]));
          if (q4 == g)
            st16(u32x4{__float_as_uint(sx[0]), tg, __float_as_uint(sxx), tg}, rs, (32 * lw + 16 * i + l15) * 16,
                 OFF_RS + (rt * 10 + rot) * 2048);
        }
        __builtin_amdgcn_s_setprio(0);
      }
      if constexpr (s == 4) {
        if (lt < 12) dma1(b1 + cg * 192, lane * 16, lds0 + B1_OFF);
        if (lw == 1 && lane < 10) dma1(b2 + ct * 160, lane * 16, lds0 + B1_OFF + 256);
      }
      if constexpr (s == 6 || s == 11) {
        if (lt == 0 && (s == 6 || cg < 15)) __hip_atomic_store((GAS u64*)(ws + (s == 6 ? FL_W : FL_Q) + b * 128), tag, RLX_AGENT);
      }
      if constexpr (s == 5) {
#pragma unroll
        for (int k = 0; k < 5; ++k) gr[k] = aload16(ws, ro[k]);
      }
      if constexpr (s == 5 || s == 14) {
#pragma unroll
        for (int k = 0; k < 3; ++k) { if (s == 14) co[k] += 16 * 10240; gc[k] = aload16(ws, co[k]); }
      }
      if constexpr (s == 8) {
        __builtin_amdgcn_s_setprio(3);
#pragma unroll
        for (int k = 0; k < 5; ++k) PIN(gr[k]);
        float sx, sxx;
        if (!sum_pairs<5>(gr, 0xffffffffu, tg, sx, sxx)) regather<5>(rs, ro, 0xffffffffu, tg, sx, sxx);
        sx += __shfl_xor(sx, 32); sxx += __shfl_xor(sxx, 32);
        const float mean = sx * (1.f / 640.f);
        float var = sxx * (1.f / 640.f) - mean * mean;
        var = var < 0.f ? 0.f : var;
        if (lane < 32) *(LAS f32x2*)(smem + ST_OFF + (32 * lw + lane) * 8) = f32x2{mean, rsqrtf(var + 1e-5f)};
        col_sums(0);
        __builtin_amdgcn_s_setprio(0);
      }
      if constexpr (s == 9) {
        poll_flags(ws + fW, true, tag);
#pragma unroll
        for (int k = 0; k < WOPRE; ++k)
#pragma unroll
          for (int j = 0; j < 5; ++j) wpre[k][j] = ld16(rs, lane * 16, wb + k * 5120 + j * 1024);
      }
      if constexpr (s == 16) fl = aload8(ws, fQ);
    };
    stage(IC<0>{}); stage(IC<1>{}); stage(IC<2>{}); stage(IC<3>{}); stage(IC<4>{}); stage(IC<5>{});
    stage(IC<6>{}); stage(IC<7>{}); stage(IC<8>{}); stage(IC<9>{}); stage(IC<10>{}); stage(IC<11>{});
    stage(IC<12>{}); stage(IC<13>{}); stage(IC<14>{}); stage(IC<15>{}); stage(IC<16>{}); stage(IC<17>{});
    STAMP(2);
    PIN(fl);
    if (!__all(fl[0] == tg && fl[1] == (unsigned)(tag >> 32))) poll_flags(ws + fQ, true, tag);
    STAMP(11);
#pragma unroll
    for (int i = 0; i < 2; ++i) {
      const int seg = 2 * lw + i;
      int tb = 4 * qb - 4 + seg; tb = tb < 0 ? 0 : tb;
      dma_kv(ws + OFF_QKV + (size_t)(tb * 240 + 80 + ch) * 1024, lane * 16, lane * 16 + 81920, lds0 + K_OFF + seg * 1024);
    }
    const int l31 = lane & 31, hh = lane >> 5;
    u32x4 qraw[2][2];
#pragma unroll
    for (int a = 0; a < 2; ++a)
#pragma unroll
      for (int ks = 0; ks < 2; ++ks)
        qraw[a][ks] = __builtin_amdgcn_raw_buffer_load_b128(rs, (ks ? 2 : hh) * 1024 + (32 * a + l31) * 16,
                                                            OFF_QKV + ((4 * qb + lw) * 240 + ch) * 1024, SC1);
    col_sums(1);
    {
      const u32x4 one4 = {0x3f803f80u, 0x3f803f80u, 0x3f803f80u, 0x3f803f80u};
      *(LAS u32x4*)(smem + V_OFF + 3 * VP + lt * 16) = one4;
      if (lt < 40) *(LAS u32x4*)(smem + V_OFF + 3 * VP + 4096 + lt * 16) = one4;
      *(LAS u32x4*)(smem + K_OFF + 3 * KP + lt * 16) = u32x4{qb == 0 ? 0xff80u : 0u, 0u, 0u, 0u};
      *(LAS u32x4*)(smem + K_OFF + 3 * KP + 4096 + lt * 16) = u32x4{0u, 0u, 0u, 0u};
    }
    bf16x8 qf[2][2];
#pragma unroll
    for (int a = 0; a < 2; ++a) {
      u32x4 v0 = qraw[a][0], v1 = qraw[a][1];
      if (odd && hh == 0) { v0[0] = 0u; v0[1] = 0u; }
      if (hh) v1 = u32x4{0x3f80u, 0u, 0u, 0u};
      else if (!odd) { v1[2] = 0u; v1[3] = 0u; }
      qf[a][0] = __builtin_bit_cast(bf16x8, v0);
      qf[a][1] = __builtin_bit_cast(bf16x8, v1);
    }
    f32x16 mF, z16;
#pragma unroll
    for (int e = 0; e < 16; ++e) {
      mF[e] = (e & 3) + 8 * (e >> 2) + 4 * hh > l31 ? 0.f : -INFINITY;
      z16[e] = 0.f;
    }
    asm volatile("" : "+v"(mF), "+v"(qf[0][0]), "+v"(qf[0][1]), "+v"(qf[1][0]), "+v"(qf[1][1]));
    WAIT_VM(0);
    WAIT_LGKM0();
    lds_arrive(sync);
    lds_wait(sync, 4);
    __builtin_amdgcn_s_setprio(2);
    STAMP(5);
    unsigned kp = lds0 + K_OFF + hh * KP + (64 * lw + l31) * 16;
    const bool ones = (g & 1) && (p4 >> 1);
    unsigned vp = lds0 + V_OFF + (2 * (g & 1) + (p4 >> 1)) * VP + ((ones ? 0 : 64 * lw) + 4 * hh + q4) * 16 + (p4 & 1) * 8;
    unsigned ov = l31 * 16 + hh * 8;
    bf16x8 q0 = qf[0][0], q1 = qf[0][1];
#define LDK(T, ks) (*(LAS bf16x8*)(size_t)(kp + (T) * 512 + (ks) * 2 * KP))
#define MF32(a, b, c) __builtin_amdgcn_mfma_f32_32x32x16_bf16(a, b, c, 0, 0, 0)
#define EX2(s_, e) pk2(__builtin_amdgcn_exp2f((s_)[e]), __builtin_amdgcn_exp2f((s_)[(e) + 1]))
    auto tile_max = [&](int T) {
      f32x16 sp = MF32(LDK(T, 1), q1, MF32(LDK(T, 0), q0, z16));
      float m0 = -INFINITY, m1 = -INFINITY;
#pragma unroll
      for (int e = 0; e < 16; ++e) {
        const bool off = T == 0 ? mF[e] != 0.f : (T == 8 ? mF[e] == 0.f : false);
        sp[e] = off ? -INFINITY : sp[e];
      }
#pragma unroll
      for (int e = 0; e < 16; e += 4) { m0 = MAX3(m0, sp[e], sp[e + 1]); m1 = MAX3(m1, sp[e + 2], sp[e + 3]); }
      m0 = fmaxf(m0, m1);
      return fmaxf(m0, __shfl_xor(m0, 32));
    };
#pragma unroll 1
    for (int un = 0; un < 2; ++un) {
      float mx = tile_max(8);
      f32x16 oa;
#pragma unroll 1
      for (int pass = 0; pass < 2; ++pass) {
      f32x16 ca;
#pragma unroll
      for (int e = 0; e < 16; ++e) ca[e] = -mx;
      oa = z16;
      {
        bf16x8 kc[2][2], vf[2][2];
        f32x16 sc[2];
        u32x4 pa[2] = {{0u, 0u, 0u, 0u}, {0u, 0u, 0u, 0u}};
        kc[0][0] = LDK(0, 0); kc[0][1] = LDK(0, 1); kc[1][0] = LDK(1, 0); kc[1][1] = LDK(1, 1);
        sc[0] = MF32(kc[0][1], q1, MF32(kc[0][0], q0, ca + mF));
        sc[1] = z16;
        asm volatile("" :: "v"(sc[0]));
        SB();
#pragma unroll
        for (int T = 0; T < 10; ++T) {
          const int c = T & 1, n = c ^ 1;
          if (T < 9) {
#pragma unroll
            for (int ks = 0; ks < 2; ++ks) vf[c][ks] = cat44(trread(vp + T * 512 + ks * 256), trread(vp + T * 512 + ks * 256 + 128));
          }
          if (T < 8) sc[n] = MF32(kc[n][1], q1, MF32(kc[n][0], q0, ca));
          if (T < 7) { kc[c][0] = LDK(T + 2, 0); kc[c][1] = LDK(T + 2, 1); }
          if (T > 0) oa = MF32(vf[n][1], __builtin_bit_cast(bf16x8, pa[1]), MF32(vf[n][0], __builtin_bit_cast(bf16x8, pa[0]), oa));
          if (T == 8) {
#pragma unroll
            for (int e = 0; e < 16; ++e) sc[c][e] = mF[e] == 0.f ? -INFINITY : sc[c][e];
          }
          if (T < 9) {
#pragma unroll
            for (int e = 0; e < 4; ++e) { pa[0][e] = EX2(sc[c], 2 * e); pa[1][e] = EX2(sc[c], 8 + 2 * e); }
          }
          SGB(0x100, 6);
          if (T == 8) SGB(0x2, 16);
          SGB(0x8, 1); SGB(0x400, 4); SGB(0x2, 1);
#pragma unroll
          for (int k = 0; k < 3; ++k) { SGB(0x8, 1); SGB(0x400, 2); SGB(0x2, 1); SGB(0x400, 2); SGB(0x2, 1); }
          SGB(0x2, 1);
          asm volatile("" :: "v"(sc[n]), "v"(oa), "v"(pa[0]), "v"(pa[1]));
          SB();
        }
      }
      if (pass || __all(oa[12] < 1.8446744e19f)) break;
#pragma unroll 1
      for (int T = 0; T < 8; ++T) mx = fmaxf(mx, tile_max(T));
      }
      const float il = __builtin_amdgcn_rcpf(oa[12]);
#pragma unroll
      for (int pl = 0; pl < 3; ++pl) {
        const bool ok = pl == 1 || (pl == 0 ? !(odd && hh == 0) : (odd || hh == 0));
        if (ok) {
          const u32x2 pkd = {pk2(oa[4 * pl] * il, oa[4 * pl + 1] * il), pk2(oa[4 * pl + 2] * il, oa[4 * pl + 3] * il)};
          __builtin_amdgcn_raw_buffer_store_b64(pkd, rs, pl * 1024 + ov, OFF_O + ((4 * qb + lw) * 80 + ch) * 1024, SC1);
        }
      }
      kp += 512; vp += ones ? 0 : 512; ov += 512;
      q0 = qf[1][0]; q1 = qf[1][1];
    }
    __builtin_amdgcn_s_setprio(0);
    lds_arrive(sync + 4);
    STAMP(6);
    WAIT_VM(0);
    if (lane == 0) __hip_atomic_store((GAS u64*)(ws + FL_O + (b * 4 + lw) * 128), tag, RLX_AGENT);
#pragma unroll
    for (int k = WOPRE; k < 5; ++k)
#pragma unroll
      for (int j = 0; j < 5; ++j) wpre[k][j] = ld16(rs, lane * 16, wb + k * 5120 + j * 1024);
#pragma unroll
    for (int j = 0; j < 2; ++j) xv[j] = *(const u32x2*)(x + orow + (3 + j) * 32);
    const unsigned ab = OFF_O + (mt * 80 + 20 * lw) * 1024;
    {
      const int hd = 8 * lw + (lane & 7);
      poll_flags(ws + FL_O + ((((((mt >> 2) << 2) | (hd & 3)) << 3) | (hd >> 2)) * 4 + (mt & 3)) * 128, lane < 8, tag);
    }
    STAMP(7);
    bf16x8 af[4][5];
#pragma unroll
    for (int i = 0; i < 4; ++i)
#pragma unroll
      for (int k = 0; k < 5; ++k) af[i][k] = ld16(rs, va + ((lw + 1 + i) & 3) * 256, ab + k * 4096);
    SB();
    lds_wait(sync + 4, 4);
    lds_wait(sync + 6, 4);
    f32x4 own[2];
#pragma unroll
    for (int i = 0; i < 4; ++i) {
      const int mi = (lw + 1 + i) & 3;
      f32x4 a5[5] = {z4, z4, z4, z4, z4};
#pragma unroll
      for (int k = 0; k < 5; ++k)
#pragma unroll
        for (int j = 0; j < 5; ++j) a5[j] = mfma16(wpre[k][j], af[i][k], a5[j]);
#pragma unroll
      for (int j = 0; j < 3; ++j) *(LAS f32x4*)(smem + REDS1_OFF + ((mi * 4 + lw) * 3 + j) * 1024 + lane * 16) = a5[j];
      if (i < 3) {
#pragma unroll
        for (int j = 0; j < 2; ++j) *(LAS f32x4*)(smem + REDS2_OFF + ((mi * 3 + 2 - i) * 2 + j) * 1024 + lane * 16) = a5[3 + j];
      } else { own[0] = a5[3]; own[1] = a5[4]; }
    }
    STAMP(8);
    __syncthreads();
    STAMP(9);
#pragma unroll
    for (int j = 0; j < 2; ++j) {
      f32x4 sv = own[j];
#pragma unroll
      for (int k = 0; k < 3; ++k) sv += *(LAS f32x4*)(smem + REDS2_OFF + ((lw * 3 + k) * 2 + j) * 1024 + lane * 16);
      const f32x4 gt = *(LAS f32x4*)(smem + GX_OFF + (lw * 2 + j) * 1024 + lane * 16);
      *(u32x2*)(out + orow + (3 + j) * 32) = fin4(xv[j], sv, gt);
    }

  } else {
    if (idx * 4 + w < CODE_KB) {
      const u32x4 t0 = *(const u32x4*)(pc0 + (idx * 4 + w) * 1024 + lane * 16);
      asm volatile("" :: "v"(t0));
    }
    {
      const int mw = w & 1, nw = w >> 1;
      unsigned xa[2];
      xa[0] = lds0 + (64 * mw + l15) * 128 + (xsw << 4);
      xa[1] = lds0 + (64 * mw + l15) * 128 + ((xsw ^ 4) << 4);
      const unsigned wl = lds0 + 16384 + (8 * g + q4) * 128 + p4 * 8;
      const unsigned vo = (g >> 1) * 1024 + (16 * (g & 1) + l15) * 16;
      float sh[4], sc[4];
      BAR();
      STAMP(1);
      mfma_part<4, 0>(xa, wl, 4 * nw, sigl, lds0, sh, sc, 64 * mw + l15, cg < 5 ? 0.22360679774997896f * 1.4426950408889634f : 1.f, rs, vo,
                      cg < 15 ? OFF_QKV + ((2 * rt + mw) * 240 + 16 * cg + 8 * nw) * 1024 : 0x80000000u, 4 * nw, g, ws);
      STAMP(14);
      mfma_part<3, 10>(xa, wl, lu1 + 3 * nw, sigl, lds0, sh, sc, 0, 1.f, rs, vo,
                       OFF_H + ((2 * rt + mw) * 192 + 12 * cg + 6 * nw) * 1024, 3 * nw, g, ws);
    }
    STAMP(3);
    WAIT_VM(0);
    if (lds_arrive(sync + 2) == 3 && lane == 0) __hip_atomic_store((GAS u64*)(ws + FL_H + b * 128), tag, RLX_AGENT);
    if (b == 0 && w == 0) {
#pragma unroll 1
      for (int spins = 0; spins < (1 << 20); ++spins) {
        bool ok = true;
#pragma unroll
        for (int i = 0; i < 4; ++i) ok = ok && __hip_atomic_load((const GAS u64*)(ws + FL_W + (lane + 64 * i) * 128), RLX_AGENT) == tag;
        if (__all(ok)) break;
        __builtin_amdgcn_s_sleep(1);
      }
      if (lane == 0) __hip_atomic_store((GAS u64*)(ws + OFF_FL), tag, RLX_AGENT);
    }
    STAMP(4);
#pragma unroll
    for (int k = 0; k < 4; ++k)
#pragma unroll
      for (int j = 0; j < 5; ++j) wpre[k][j] = ld16(rs, lane * 16, wb + k * 5120 + j * 1024);
    const unsigned wl_ = lds0 + slab_of(w);
    lds_wait(sync + 2, 4);
    dma5_sc1(ws + wb + 4 * 5120, lane * 16, wl_);
    dma5_sc1(ws + wb + 5 * 5120, lane * 16, wl_ + 5120);
    const unsigned ab = OFF_H + (mt * 192 + 48 * w) * 1024;
    poll_flags(ws + FL_H + wg_of(mt >> 1, 4 * w + (lane & 3)) * 128, lane < 4, tag);
    STAMP(7);
    bf16x8 af[4][4];
#pragma unroll
    for (int k = 0; k < 4; ++k)
#pragma unroll
      for (int i = 0; i < 4; ++i) af[k][i] = ld16(rs, va + i * 256, ab + k * 4096);
    dma5_sc1(ws + ab + 4 * 4096, lane * 16, wl_ + 10240);
    dma5_sc1(ws + ab + 4 * 4096 + 3072, lane * 16, wl_ + 10240 + 3072);
    {
      SB();
      WAIT_VM(0);
    }
    STAMP(12);
#pragma unroll
    for (int i = 0; i < 4; ++i)
#pragma unroll
      for (int j = 0; j < 5; ++j) acc[i][j] = f32x4{0.f, 0.f, 0.f, 0.f};
#pragma unroll
    for (int k = 0; k < 12; ++k) {
      const int cs = k & 3;
      if (k == W2HOLD) {
        unsigned tv, ts;
        asm volatile(".Lp%=:\n\tds_read_b32 %0, %2\n\ts_waitcnt lgkmcnt(0)\n\tv_readfirstlane_b32 %1, %0\n\t"
                     "s_cmp_ge_u32 %1, 4\n\ts_cbranch_scc1 .Le%=\n\ts_sleep 2\n\ts_branch .Lp%=\n.Le%=:"
                     : "=&v"(tv), "=&s"(ts) : "v"(lds0 + SYNC_OFF + 16) : "memory", "scc");
      }
#pragma unroll
      for (int i = 0; i < 4; ++i)
#pragma unroll
        for (int j = 0; j < 5; ++j) acc[i][j] = mfma16(wpre[cs][j], af[cs][i], acc[i][j]);
      SB();
      if (k < 2) {
#pragma unroll
        for (int j = 0; j < 5; ++j) wpre[cs][j] = *(LAS bf16x8*)(size_t)(wl_ + k * 5120 + j * 1024 + lane * 16);
#pragma unroll
        for (int i = 0; i < 4; ++i) af[cs][i] = *(LAS bf16x8*)(size_t)(wl_ + 10240 + k * 4096 + va + i * 256);
      } else if (k < 8) {
#pragma unroll
        for (int i = 0; i < 4; ++i) af[cs][i] = ld16(rs, va + i * 256, ab + (k + 4) * 4096);
#pragma unroll
        for (int j = 0; j < 5; ++j) wpre[cs][j] = ld16(rs, lane * 16, wb + (k + 4) * 5120 + j * 1024);
      }
      SB();
    }
    lds_arrive(sync + 6);
    lds_wait(sync + 6, 4);
    lds_wait(sync + 4, 4);
    STAMP(8);
    red_write(smem + REDG_OFF, w, lane, acc);
    u32x2 bb[5];
#pragma unroll
    for (int j = 0; j < 5; ++j) {
      if (j < 3) xv[j] = *(const u32x2*)(x + orow + j * 32);
      bb[j] = *(LAS u32x2*)(smem + B1_OFF + 256 + (16 * j + 4 * g) * 2);
    }
    WAIT_LGKM0();
    lds_arrive(sync + 3);
    lds_wait(sync + 3, 4);
    f32x4 gt[5];
    red_sum(smem + REDG_OFF, w, lane, acc, gt);
#pragma unroll
    for (int j = 0; j < 5; ++j) {
      const float bv[4] = {bflo(bb[j][0]), bfhi(bb[j][0]), bflo(bb[j][1]), bfhi(bb[j][1])};
#pragma unroll
      for (int e = 0; e < 4; ++e) {
        const float t = rbf(rbf(gt[j][e]) + bv[e]);
        gt[j][e] = rbf(t * __builtin_amdgcn_rcpf(1.f + __expf(-t)));
      }
      if (j >= 3) *(LAS f32x4*)(smem + GX_OFF + (w * 2 + j - 3) * 1024 + lane * 16) = gt[j];
    }
    __syncthreads();
    STAMP(9);
#pragma unroll
    for (int j = 0; j < 3; ++j) {
      f32x4 sv = *(LAS f32x4*)(smem + REDS1_OFF + ((w * 4) * 3 + j) * 1024 + lane * 16);
#pragma unroll
      for (int k = 1; k < 4; ++k) sv += *(LAS f32x4*)(smem + REDS1_OFF + ((w * 4 + k) * 3 + j) * 1024 + lane * 16);
      *(u32x2*)(out + orow + j * 32) = fin4(xv[j], sv, gt[j]);
    }
  STAMP(10);
    __builtin_amdgcn_endpgm();
  }
  STAMP(10);
}

extern "C" void kernel_launch(void* const* d_in, const int* in_sizes, int n_in, void* d_out, int out_size, void* d_ws,
                              size_t ws_size, hipStream_t stream) {
  (void)hipFuncSetAttribute((const void*)k_all, hipFuncAttributeMaxDynamicSharedMemorySize, LDS_ALL);
  k_all<<<256, 512, LDS_ALL, stream>>>((const char*)d_in[0], (const char*)d_in[1], (const char*)d_in[4], (const char*)d_in[5],
                                       (const char*)d_in[6], (const char*)d_in[2], (const char*)d_in[7], (char*)d_ws, (char*)d_out);
}
```

```cpp
#include <hip/hip_runtime.h>
#include <stdint.h>

#define LAS __attribute__((address_space(3)))
#define GAS __attribute__((address_space(1)))
#define CAS __attribute__((address_space(4)))
typedef short bf16x8 __attribute__((ext_vector_type(8)));
typedef short s16x4 __attribute__((ext_vector_type(4)));
typedef float f32x4 __attribute__((ext_vector_type(4)));
typedef unsigned u32x4 __attribute__((ext_vector_type(4)));
typedef unsigned u32x2 __attribute__((ext_vector_type(2)));
typedef __bf16 bf16x2_t __attribute__((ext_vector_type(2)));
typedef float f32x2 __attribute__((ext_vector_type(2)));
typedef float f32x16 __attribute__((ext_vector_type(16)));
typedef unsigned long long u64;

constexpr unsigned OFF_FL = 0;
constexpr unsigned FL_W = 1024, FL_A = FL_W + 32768, FL_O = FL_A + 32768;
constexpr unsigned OFF_RS = 131072;
constexpr unsigned OFF_CS = OFF_RS + 16 * 10 * 2048;
constexpr unsigned OFF_QKV = OFF_CS + 16 * 10 * 2048;
constexpr unsigned OFF_H = OFF_QKV + 32 * 240 * 1024;
constexpr unsigned OFF_O = OFF_H + 32 * 192 * 1024;
constexpr unsigned OFF_W2F = OFF_O + 32 * 80 * 1024;
constexpr unsigned OFF_WOF = OFF_W2F + 8 * 48 * 5120;
constexpr unsigned WS_USED = OFF_WOF + 8 * 20 * 5120;

__device__ __forceinline__ f32x4 mfma16(bf16x8 a, bf16x8 b, f32x4 c) {
  return __builtin_amdgcn_mfma_f32_16x16x32_bf16(a, b, c, 0, 0, 0);
}
__device__ __forceinline__ unsigned pk2(float a, float b) {
  f32x2 v = {a, b};
  bf16x2_t r = __builtin_convertvector(v, bf16x2_t);
  return __builtin_bit_cast(unsigned, r);
}
__device__ __forceinline__ float rbf(float a) { return __builtin_bit_cast(float, pk2(0.f, a) & 0xffff0000u); }
__device__ __forceinline__ float bflo(unsigned u) { return __builtin_bit_cast(float, u << 16); }
__device__ __forceinline__ float bfhi(unsigned u) { return __builtin_bit_cast(float, u & 0xffff0000u); }
__device__ __forceinline__ u32x2 fin4(u32x2 xv, f32x4 sv, f32x4 gt) {
  return u32x2{pk2(bflo(xv[0]) + rbf(rbf(sv[0]) * gt[0]), bfhi(xv[0]) + rbf(rbf(sv[1]) * gt[1])),
               pk2(bflo(xv[1]) + rbf(rbf(sv[2]) * gt[2]), bfhi(xv[1]) + rbf(rbf(sv[3]) * gt[3]))};
}
__device__ __forceinline__ s16x4 trread(unsigned a) {
  return __builtin_amdgcn_ds_read_tr16_b64_v4i16((LAS s16x4*)(size_t)a);
}
__device__ __forceinline__ bf16x8 cat44(s16x4 a, s16x4 b) {
  return __builtin_shufflevector(a, b, 0, 1, 2, 3, 4, 5, 6, 7);
}
#define WAIT_VM(n) asm volatile("s_waitcnt vmcnt(" #n ")" ::: "memory")
#define WAIT_LGKM0() __builtin_amdgcn_s_waitcnt(0xC07F)
#define BAR() __builtin_amdgcn_s_barrier()
#define SB() __builtin_amdgcn_sched_barrier(0)
#define SGB(m, n) __builtin_amdgcn_sched_group_barrier(m, n, 0)
#define MAX3(a, b, c) __builtin_fmaxf(__builtin_fmaxf(a, b), c)
#define RLX_AGENT __ATOMIC_RELAXED, __HIP_MEMORY_SCOPE_AGENT
constexpr int SC1 = 16;
constexpr int CODE_KB = 36;
template <int N> struct IC { static constexpr int value = N; };

#define STAMP(i)
#define CLK(v)

__device__ __forceinline__ void dma1(const char* sbase, unsigned voff, unsigned lds) {
  unsigned keep;
  asm volatile("s_mov_b32 %0, m0\n\ts_mov_b32 m0, %3\n\ts_nop 0\n\tglobal_load_lds_dwordx4 %1, %2\n\ts_mov_b32 m0, %0"
               : "=&s"(keep) : "v"(voff), "s"(sbase), "s"(lds) : "memory");
}
#define DMA_W2(ofs) "s_add_u32 m0, m0, 7040\n\ts_nop 0\n\tglobal_load_lds_dwordx4 %8, %7 offset:" #ofs "\n\t" \
                    "s_add_u32 m0, m0, 1024\n\ts_nop 0\n\tglobal_load_lds_dwordx4 %9, %7 offset:" #ofs "\n\t"
#define DMA_HEAD "s_mov_b32 %0, m0\n\ts_mov_b32 m0, %10\n\ts_nop 0\n\tglobal_load_lds_dwordx4 %8, %7\n\t" \
                 "s_add_u32 m0, m0, 1024\n\ts_nop 0\n\tglobal_load_lds_dwordx4 %9, %7\n\t" DMA_W2(128) DMA_W2(256)
#define DMA_X "s_mov_b32 m0, %6\n\ts_nop 0\n\tglobal_load_lds_dwordx4 %2, %1\n\t" \
              "s_add_u32 m0, m0, 1024\n\ts_nop 0\n\tglobal_load_lds_dwordx4 %3, %1\n\t" \
              "s_add_u32 m0, m0, 1024\n\ts_nop 0\n\tglobal_load_lds_dwordx4 %4, %1\n\t" \
              "s_add_u32 m0, m0, 1024\n\ts_nop 0\n\tglobal_load_lds_dwordx4 %5, %1\n\ts_mov_b32 m0, %0"
#define DMA_OPS : "=&s"(keep) : "s"(sa), "v"(va0), "v"(va1), "v"(va2), "v"(va3), "s"(la), "s"(sb), "v"(vb0), "v"(vb1), "s"(lb) : "memory", "scc"
template <int NP>
__device__ __forceinline__ void dma_stage(const char* sa, unsigned va0, unsigned va1, unsigned va2, unsigned va3, unsigned la,
                                          const char* sb, unsigned vb0, unsigned vb1, unsigned lb) {
  unsigned keep;
  if constexpr (NP == 3) asm volatile(DMA_HEAD DMA_X DMA_OPS);
  else asm volatile(DMA_HEAD DMA_W2(384) DMA_X DMA_OPS);
}
__device__ __forceinline__ void dma_kv(const char* sb, unsigned vk, unsigned vv, unsigned l) {
  unsigned keep;
  asm volatile("s_mov_b32 %0, m0\n\ts_mov_b32 m0, %4\n\ts_nop 0\n\tglobal_load_lds_dwordx4 %2, %1 sc1\n\t"
               "s_add_u32 m0, m0, 7168\n\ts_nop 0\n\tglobal_load_lds_dwordx4 %2, %1 offset:1024 sc1\n\t"
               "s_add_u32 m0, m0, 7168\n\ts_nop 0\n\tglobal_load_lds_dwordx4 %2, %1 offset:2048 sc1\n\t"
               "s_add_u32 m0, m0, 18432\n\ts_nop 0\n\tglobal_load_lds_dwordx4 %3, %1 sc1\n\t"
               "s_add_u32 m0, m0, 7232\n\ts_nop 0\n\tglobal_load_lds_dwordx4 %3, %1 offset:1024 sc1\n\t"
               "s_add_u32 m0, m0, 7232\n\ts_nop 0\n\tglobal_load_lds_dwordx4 %3, %1 offset:2048 sc1\n\t"
               "s_mov_b32 m0, %0"
               : "=&s"(keep) : "s"(sb), "v"(vk), "v"(vv), "s"(l) : "memory", "scc");
}
__device__ __forceinline__ void dma5_sc1(const char* sb, unsigned v, unsigned l) {
  unsigned keep;
  asm volatile("s_mov_b32 %0, m0\n\ts_mov_b32 m0, %3\n\ts_nop 0\n\tglobal_load_lds_dwordx4 %2, %1 sc1\n\t"
               "global_load_lds_dwordx4 %2, %1 offset:1024 sc1\n\tglobal_load_lds_dwordx4 %2, %1 offset:2048 sc1\n\t"
               "global_load_lds_dwordx4 %2, %1 offset:3072 sc1\n\ts_add_u32 m0, m0, 1024\n\ts_nop 0\n\t"
               "global_load_lds_dwordx4 %4, %1 offset:3072 sc1\n\ts_mov_b32 m0, %0"
               : "=&s"(keep) : "s"(sb), "v"(v), "s"(l), "v"(v + 1024) : "memory", "scc");
}
__device__ __forceinline__ u32x2 aload8(const char* sbase, unsigned voff) {
  u32x2 r;
  asm volatile("global_load_dwordx2 %0, %1, %2 sc1" : "=v"(r) : "v"(voff), "s"(sbase) : "memory");
  return r;
}
__device__ __forceinline__ u32x4 aload16(const char* sbase, unsigned voff) {
  u32x4 r;
  asm volatile("global_load_dwordx4 %0, %1, %2 sc1" : "=v"(r) : "v"(voff), "s"(sbase) : "memory");
  return r;
}
#define PIN(x) asm volatile("" : "+v"(x))
template <int N> __device__ __forceinline__ void wait_vm() { asm volatile("s_waitcnt vmcnt(%0)" :: "i"(N) : "memory"); }
__device__ __forceinline__ void poll_flags(const char* flag, bool need, u64 tag) {
#pragma unroll 1
  for (int spins = 0; spins < (1 << 20); ++spins) {
    const u64 v = need ? __hip_atomic_load((const GAS u64*)flag, RLX_AGENT) : tag;
    if (__all(v == tag)) break;
    __builtin_amdgcn_s_sleep(1);
  }
  __builtin_amdgcn_fence(__ATOMIC_ACQUIRE, "wavefront");
}
__device__ __forceinline__ unsigned lds_arrive(LAS unsigned* c) {
  unsigned r = 0;
  if ((threadIdx.x & 63) == 0) r = __hip_atomic_fetch_add(c, 1u, __ATOMIC_RELAXED, __HIP_MEMORY_SCOPE_WORKGROUP);
  return __builtin_amdgcn_readfirstlane(r);
}
__device__ __forceinline__ void lds_wait_sleep(LAS unsigned* c, unsigned n) {
#pragma unroll 1
  for (int spins = 0; spins < (1 << 22); ++spins) {
    if (__builtin_amdgcn_readfirstlane(__hip_atomic_load(c, __ATOMIC_RELAXED, __HIP_MEMORY_SCOPE_WORKGROUP)) >= n) break;
    __builtin_amdgcn_s_sleep(2);
  }
  __builtin_amdgcn_fence(__ATOMIC_ACQUIRE, "wavefront");
}
__device__ __forceinline__ void lds_wait(LAS unsigned* c, unsigned n) {
#pragma unroll 1
  for (int spins = 0; spins < (1 << 22); ++spins) {
    if (__builtin_amdgcn_readfirstlane(__hip_atomic_load(c, __ATOMIC_RELAXED, __HIP_MEMORY_SCOPE_WORKGROUP)) >= n) break;
  }
  __builtin_amdgcn_fence(__ATOMIC_ACQUIRE, "wavefront");
}

#define WOPRE 4
#define W2HOLD 8
constexpr int STG = 49152, RT_OFF = 3 * STG, LDS_ALL = 163840;
constexpr int ST_OFF = 160768;
constexpr int CS_OFF = ST_OFF + 1024, SYNC_OFF = ST_OFF + 2048;
constexpr int B1_OFF = 157696;
constexpr int KP = 8192, VP = 8192 + 64;
constexpr int K_OFF = STG, V_OFF = K_OFF + 4 * KP;
__device__ __forceinline__ constexpr int slab_of(int w) { return w < 2 ? w * 20480 : 111616 + (w - 2) * 20480; }
constexpr int REDG_OFF = 0, GX_OFF = 61440, REDS1_OFF = GX_OFF + 8192, REDS2_OFF = REDS1_OFF + 49152;

__device__ __forceinline__ void st16(u32x4 v, __amdgpu_buffer_rsrc_t rs, unsigned voff, unsigned soff) {
  __builtin_amdgcn_raw_buffer_store_b128(v, rs, voff + soff, 0, SC1);
}
__device__ __forceinline__ bf16x8 ld16(__amdgpu_buffer_rsrc_t rs, unsigned voff, unsigned soff) {
  return __builtin_bit_cast(bf16x8, __builtin_amdgcn_raw_buffer_load_b128(rs, voff, soff, SC1));
}
__device__ __forceinline__ void red_write(LAS char* red, int kw, int lane, const f32x4 (&acc)[4][5]) {
#pragma unroll
  for (int i = 0; i < 4; ++i) {
    if (i != kw) {
      const int slot = kw - (kw > i ? 1 : 0);
#pragma unroll
      for (int j = 0; j < 5; ++j) *(LAS f32x4*)(red + ((i * 3 + slot) * 5 + j) * 1024 + lane * 16) = acc[i][j];
    }
  }
}
__device__ __forceinline__ void red_sum(LAS char* red, int kw, int lane, const f32x4 (&acc)[4][5], f32x4 (&o)[5]) {
#pragma unroll
  for (int j = 0; j < 5; ++j) {
    f32x4 a = kw == 0 ? acc[0][j] : (kw == 1 ? acc[1][j] : (kw == 2 ? acc[2][j] : acc[3][j]));
#pragma unroll
    for (int s = 0; s < 3; ++s) a += *(LAS f32x4*)(red + ((kw * 3 + s) * 5 + j) * 1024 + lane * 16);
    o[j] = a;
  }
}
__device__ __forceinline__ int grp_qkv(int u) { return u < 14 ? 0 : (u < 27 ? 1 : 2); }
__device__ __forceinline__ int grp_h(int u) { return u < 70 ? u / 14 : (u < 83 ? 5 : 6); }
__device__ __forceinline__ int wg_of(int rt, int cg) { return (((cg & 3) * 8 + (rt & 7)) << 3) | ((cg >> 2) << 1) | (rt >> 3); }
template <int N>
__device__ __forceinline__ bool sum_pairs(const u32x4 (&v)[N], unsigned last_on, unsigned tg, float& a, float& b) {
  bool ok = true;
  a = 0.f; b = 0.f;
#pragma unroll
  for (int k = 0; k < N; ++k) {
    ok = ok && v[k][1] == tg && v[k][3] == tg;
    const unsigned m = k == N - 1 ? last_on : 0xffffffffu;
    a += __uint_as_float(v[k][0] & m);
    b += __uint_as_float(v[k][2] & m);
  }
  return __all(ok);
}
template <int N>
__device__ __forceinline__ void regather(__amdgpu_buffer_rsrc_t rs, const unsigned (&vo)[N], unsigned last_on, unsigned tg, float& a, float& b) {
#pragma unroll 1
  for (int spins = 0; spins < (1 << 20); ++spins) {
    u32x4 v[N];
#pragma unroll
    for (int k = 0; k < N; ++k) v[k] = __builtin_amdgcn_raw_buffer_load_b128(rs, vo[k], 0, SC1);
    if (sum_pairs<N>(v, last_on, tg, a, b)) break;
    __builtin_amdgcn_s_sleep(2);
  }
}

#define STG_STAMP(s_)

struct Ctx {
  const char *x, *W, *b1, *w2, *wo;
  char* ws;
  unsigned lds0, vA, vb0, vb1;
  int b, rt, cg, rot, ct, qb, ch, w, lane, u0, nu, ldw;
};

__device__ __forceinline__ void mfma_phase(const Ctx& c, __amdgpu_buffer_rsrc_t rs) {
  constexpr int NT = 7;
  const bool ISH = c.cg >= 9;
  char* ws = c.ws;
  const int lane = c.lane, g = lane >> 4, l15 = lane & 15, q4 = l15 >> 2, p4 = lane & 3;
  const int mw = c.w & 1, nw = c.w >> 1;
  const int sigl = ((q4 >> 1) & 1) | ((g & 1) << 1), xsw = g ^ (l15 >> 1);
  unsigned xa[2], wa[NT];
  xa[0] = c.lds0 + (64 * mw + l15) * 128 + (xsw << 4);
  xa[1] = c.lds0 + (64 * mw + l15) * 128 + ((xsw ^ 4) << 4);
  f32x4 acc[4][NT];
#pragma unroll
  for (int j = 0; j < NT; ++j) {
    int tl = (c.u0 & 3) + NT * nw + j; tl = tl > 15 ? 15 : tl;
    wa[j] = c.lds0 + 16384 + (tl >> 2) * 8192 + (8 * g + q4) * 128 + (((tl & 3) ^ sigl) << 5) + p4 * 8;
#pragma unroll
    for (int i = 0; i < 4; ++i) acc[i][j] = f32x4{0.f, 0.f, 0.f, 0.f};
  }
  bf16x8 xf[2][4], wf[2][NT];
#define LDX(set, i, ks) xf[set][i] = *(LAS bf16x8*)(size_t)(xa[ks] + (i) * 2048)
#define LDW(set, j, ks) wf[set][j] = cat44(trread(wa[j] + (ks) * 4096), trread(wa[j] + (ks) * 4096 + 512))
#define HALF(set, ks) do { \
    _Pragma("unroll") for (int j = 0; j < NT; ++j) { \
      if (j == 0) { _Pragma("unroll") for (int i = 0; i < 4; ++i) LDX((set) ^ 1, i, ks); } \
      else if (2 * j - 2 < NT) { LDW((set) ^ 1, 2 * j - 2, ks); if (2 * j - 1 < NT) LDW((set) ^ 1, 2 * j - 1, ks); } \
      _Pragma("unroll") for (int i = 0; i < 4; ++i) acc[i][j] = mfma16(wf[set][j], xf[set][i], acc[i][j]); \
      SGB(0x8, 1); SGB(0x100, 1); SGB(0x8, 1); SGB(0x100, 1); SGB(0x8, 1); SGB(0x100, 1); SGB(0x8, 1); SGB(0x100, 1); \
      asm volatile("" :: "v"(acc[0][j]), "v"(acc[1][j]), "v"(acc[2][j]), "v"(acc[3][j])); \
      SB(); \
    } } while (0)
  BAR();
  STAMP(1);
#pragma unroll
  for (int i = 0; i < 4; ++i) LDX(0, i, 0);
#pragma unroll
  for (int j = 0; j < NT; ++j) LDW(0, j, 0);
  WAIT_LGKM0();
  int buf = 0;
#pragma unroll 1
  for (int t = 0; t < 10; ++t) {
    HALF(0, 1);
    {
      const int d = buf == 2 ? -2 * STG : STG;
      buf = buf == 2 ? 0 : buf + 1;
      xa[0] += d; xa[1] += d;
#pragma unroll
      for (int j = 0; j < NT; ++j) wa[j] += d;
    }
    WAIT_LGKM0();
    asm volatile("s_cmp_ge_u32 %0, 9\n\ts_cbranch_scc1 .Lc%=\n\ts_barrier\n.Lc%=:" :: "s"(t) : "memory", "scc");
    STG_STAMP(t);
    SB();
    HALF(1, 0);
    WAIT_LGKM0();
  }
  STAMP(2);
  lds_wait((LAS unsigned*)(size_t)(c.lds0 + SYNC_OFF + 28), 4);
  float mean[4], rstd[4];
#pragma unroll
  for (int i = 0; i < 4; ++i) {
    const f32x2 st = *(LAS f32x2*)(size_t)(c.lds0 + ST_OFF + (64 * mw + 16 * i + l15) * 8);
    rstd[i] = st[1]; mean[i] = -st[0] * st[1];
  }
  const int u0 = c.u0 + NT * nw;
  const unsigned so = (ISH ? OFF_H + (2 * c.rt + mw) * 192 * 1024 : OFF_QKV + (2 * c.rt + mw) * 240 * 1024) + 2 * u0 * 1024;
  const unsigned vo = (g >> 1) * 1024 + (16 * (g & 1) + l15) * 16;
  const float qs = c.cg < 3 ? 0.22360679774997896f * 1.4426950408889634f : 1.f;
  auto emit = [&](auto H) {
    constexpr bool ish = decltype(H)::value;
#pragma unroll
    for (int j = 0; j < NT; ++j) {
      const f32x4 c4 = *(LAS f32x4*)(size_t)(c.lds0 + CS_OFF + ((NT * nw + j) * 16 + 4 * g) * 4);
      f32x4 bv = {0.f, 0.f, 0.f, 0.f};
      if (ish) {
        const u32x2 bb = *(LAS u32x2*)(size_t)(c.lds0 + B1_OFF + ((NT * nw + j) * 16 + 4 * g) * 2);
        bv = f32x4{bflo(bb[0]), bfhi(bb[0]), bflo(bb[1]), bfhi(bb[1])};
      }
      const unsigned oob = j == NT - 1 && NT * nw + j >= c.nu ? 0x80000000u : 0u;
#pragma unroll
      for (int pp = 0; pp < 2; ++pp) {
        float v[2][4];
#pragma unroll
        for (int i = 0; i < 2; ++i)
#pragma unroll
          for (int r = 0; r < 4; ++r) {
            float t = __builtin_fmaf(rstd[2 * pp + i], acc[2 * pp + i][j][r], __builtin_fmaf(mean[2 * pp + i], c4[r], bv[r]));
            if (ish) { t = t > 0.f ? t : 0.f; t = t * t; }
            v[i][r] = t;
          }
        const unsigned x0 = pk2(v[0][0], v[0][1]), x1 = pk2(v[0][2], v[0][3]);
        const unsigned y0 = pk2(v[1][0], v[1][1]), y1 = pk2(v[1][2], v[1][3]);
        const auto s0 = __builtin_amdgcn_permlane16_swap(x0, y0, false, false);
        const auto s1 = __builtin_amdgcn_permlane16_swap(x1, y1, false, false);
        const u32x4 o = {s0[0], s1[0], s0[1], s1[1]};
        st16(o, rs, vo + pp * 512, (so + 2 * j * 1024) | oob);
      }
    }
  };
#pragma unroll
  for (int i = 0; i < 4; ++i) { rstd[i] *= qs; mean[i] *= qs; }
  if (ISH) emit(IC<1>{}); else emit(IC<0>{});
}

__device__ __forceinline__ void load_phase(const Ctx& c, __amdgpu_buffer_rsrc_t rs, u64& tag, bf16x8 (&wpre)[5][5], unsigned wb) {
  constexpr int NP = 4, D = 12;
  char* ws = c.ws;
  const unsigned lds0 = c.lds0;
  const int lane = c.lane, g = lane >> 4, l15 = lane & 15, q4 = l15 >> 2, p4 = lane & 3;
  const int lw = c.w - 4, lt = lw * 64 + lane;
  const unsigned vb0 = c.vb0, vb1 = c.vb1, vA = c.vA;
  int ktn = c.rot + 3 >= 10 ? c.rot - 7 : c.rot + 3;
  auto issue = [&](auto S) {
    constexpr int s = decltype(S)::value;
    const unsigned dst = lds0 + (s % 3) * STG;
    dma_stage<NP>(c.x + ktn * 128, vA, (vA + 10240) ^ 64u, vA + 20480, (vA + 30720) ^ 64u, dst + lw * 4096,
                  c.W + (size_t)ktn * 64 * c.ldw, vb0, vb1, dst + 16384 + lw * 2048);
    ktn = ktn == 9 ? 0 : ktn + 1;
  };
  const bool do_rt = c.b < 170;
  const int rkb = c.b / 5, rnb = c.b - 5 * rkb;
  {
    const char* p = ws;
    asm volatile("" : "+s"(p));
    tag = *(const CAS u64*)(p + OFF_FL) + 1;
  }
  const unsigned tg = (unsigned)tag;
  wait_vm<2 * D>();
  BAR();
  STAMP(1);
  const int sigl = ((q4 >> 1) & 1) | ((g & 1) << 1), xsw = g ^ (l15 >> 1);
  const bf16x8 ONE8 = {0x3f80, 0x3f80, 0x3f80, 0x3f80, 0x3f80, 0x3f80, 0x3f80, 0x3f80};
  const f32x4 z4 = {0.f, 0.f, 0.f, 0.f};
  bf16x8 sw_[NP][2], sx_[2][2];
  u32x4 gr[5], gc[5];
  u32x2 fl;
  const int fkb = lane < 12 ? 6 * lw + (lane >> 1) : 24 + ((5 * lw) >> 1) + (lane < 18 ? (lane - 12) >> 1 : 0);
  const unsigned fW = FL_W + (fkb * 5 + ((5 * c.ct + (lane & 1) * 4) >> 3)) * 128;
  unsigned ro[5], co[5];
#pragma unroll
  for (int k = 0; k < 5; ++k) {
    ro[k] = OFF_RS + (c.rt * 10 + 2 * k + (lane >> 5)) * 2048 + (32 * lw + (lane & 31)) * 16;
    co[k] = OFF_CS + (c.cg * 10 + 2 * k + (lane >> 5)) * 2048 + (lw == 3 && (lane & 31) >= 16 ? 0 : 32 * lw + (lane & 31)) * 16;
  }
  auto stage = [&](auto S) {
    constexpr int s = decltype(S)::value;
    if constexpr (s == 0) {
#pragma unroll
      for (int i = 0; i < NP; ++i) {
        int tl = (c.u0 & 3) + NP * lw + i; tl = tl > 15 ? 15 : tl;
        const unsigned wq = lds0 + 16384 + (tl >> 2) * 8192 + (8 * g + q4) * 128 + (((tl & 3) ^ sigl) << 5) + p4 * 8;
        sw_[i][0] = cat44(trread(wq), trread(wq + 512));
        sw_[i][1] = cat44(trread(wq + 4096), trread(wq + 4608));
      }
#pragma unroll
      for (int i = 0; i < 2; ++i) {
        const unsigned xq = lds0 + (32 * lw + 16 * i + l15) * 128;
        sx_[i][0] = *(LAS bf16x8*)(size_t)(xq + (xsw << 4));
        sx_[i][1] = *(LAS bf16x8*)(size_t)(xq + ((xsw ^ 4) << 4));
      }
    }
    if constexpr (s < 9) wait_vm<s == 6 ? D + 1 : (s == 7 ? D + 10 : (s == 8 ? 10 + 10 : D))>();
    WAIT_LGKM0();
    BAR();
    if constexpr (s < 7) issue(IC<s + 3>{});
    if constexpr (s == 2) {
      __builtin_amdgcn_s_setprio(3);
      u32x4 cu = {0u, 0u, 0u, 0u};
#pragma unroll
      for (int i = 0; i < NP; ++i) {
        const u32x4 ci = __builtin_bit_cast(u32x4, mfma16(sw_[i][1], ONE8, mfma16(sw_[i][0], ONE8, z4)));
        if (l15 == i) cu = ci;
      }
      if (l15 < NP) {
        const unsigned o = OFF_CS + (c.cg * 10 + c.rot) * 2048 + NP * lw * 128;
        st16(u32x4{cu[0], tg, cu[1], tg}, rs, l15 * 128 + g * 32, o);
        st16(u32x4{cu[2], tg, cu[3], tg}, rs, l15 * 128 + g * 32 + 16, o);
      }
      __builtin_amdgcn_s_setprio(0);
    }
    if constexpr (s == 3) {
      __builtin_amdgcn_s_setprio(3);
#pragma unroll
      for (int i = 0; i < 2; ++i) {
        const f32x4 sx = mfma16(ONE8, sx_[i][1], mfma16(ONE8, sx_[i][0], z4));
        const f32x4 sq = mfma16(sx_[i][1], sx_[i][1], mfma16(sx_[i][0], sx_[i][0], z4));
        const int r = l15 & 3;
        const float sxx = r == 0 ? sq[0] : (r == 1 ? sq[1] : (r == 2 ? sq[2] : sq[3]));
        if (q4 == g)
          st16(u32x4{__float_as_uint(sx[0]), tg, __float_as_uint(sxx), tg}, rs,
                                                 (32 * lw + 16 * i + l15) * 16, OFF_RS + (c.rt * 10 + c.rot) * 2048);
      }
      __builtin_amdgcn_s_setprio(0);
    }
    if constexpr (s < 2) {
      if (do_rt) {
        const unsigned dstb = rkb < 24 ? OFF_W2F : OFF_WOF;
        const int nks = rkb < 24 ? 48 : 20, kb = rkb < 24 ? rkb : rkb - 24;
        {
          const int nl = 2 * lw + s, nt = 8 * rnb + nl, c5 = nt / 5, jj = nt - 5 * c5;
          const unsigned rp = lds0 + RT_OFF + (nl >> 2) * 8192 + (8 * g + q4) * 128 + (((nl & 3) ^ sigl) << 5) + p4 * 8;
#pragma unroll
          for (int ks = 0; ks < 2; ++ks) {
            const bf16x8 f = cat44(trread(rp + ks * 4096), trread(rp + ks * 4096 + 512));
            st16(__builtin_bit_cast(u32x4, f), rs, lane * 16,
                                                   dstb + ((c5 * nks + 2 * kb + ks) * 5 + jj) * 1024);
          }
        }
      }
    }
    if constexpr (s == 2) {
      if (lt < 16) *(LAS unsigned*)(size_t)(lds0 + SYNC_OFF + lt * 4) = 0u;
      if (c.cg >= 9 && lt < 2 * c.nu) dma1(c.b1 + c.u0 * 32, lane * 16, lds0 + B1_OFF);
    }
    if constexpr (s == 3) {
      if (lt == 0) __hip_atomic_store((GAS u64*)(ws + FL_W + c.b * 128), tag, RLX_AGENT);
    }
    if constexpr (s == 6) {
#pragma unroll
      for (int k = 0; k < 5; ++k) { gr[k] = aload16(ws, ro[k]); gc[k] = aload16(ws, co[k]); }
    }
    if constexpr (s == 5) fl = aload8(ws, fW);
    if constexpr (s == 7) {
      PIN(fl);
      if (!__all(fl[0] == tg && fl[1] == (unsigned)(tag >> 32))) poll_flags(ws + fW, true, tag);
#pragma unroll
      for (int k = 0; k < 2; ++k)
#pragma unroll
        for (int j = 0; j < 5; ++j) wpre[k][j] = ld16(rs, lane * 16, wb + k * 5120 + j * 1024);
    }
  };
  stage(IC<0>{}); stage(IC<1>{}); stage(IC<2>{}); stage(IC<3>{}); stage(IC<4>{});
  stage(IC<5>{}); stage(IC<6>{}); stage(IC<7>{}); stage(IC<8>{});
  wait_vm<10>();
  {
#pragma unroll
      for (int k = 0; k < 5; ++k) { PIN(gr[k]); PIN(gc[k]); }
      float sx, sxx, c0, c1;
      if (!sum_pairs<5>(gr, 0xffffffffu, tg, sx, sxx)) regather<5>(rs, ro, 0xffffffffu, tg, sx, sxx);
      if (!sum_pairs<5>(gc, 0xffffffffu, tg, c0, c1)) regather<5>(rs, co, 0xffffffffu, tg, c0, c1);
      sx += __shfl_xor(sx, 32); sxx += __shfl_xor(sxx, 32);
      c0 += __shfl_xor(c0, 32); c1 += __shfl_xor(c1, 32);
      const float mean = sx * (1.f / 640.f);
      float var = sxx * (1.f / 640.f) - mean * mean;
      var = var < 0.f ? 0.f : var;
      if (lane < 32) {
        *(LAS f32x2*)(size_t)(lds0 + ST_OFF + (32 * lw + lane) * 8) = f32x2{mean, rsqrtf(var + 1e-5f)};
        *(LAS f32x2*)(size_t)(lds0 + CS_OFF + (32 * lw + lane) * 8) = f32x2{c0, c1};
      }
    }
  WAIT_LGKM0();
  lds_arrive((LAS unsigned*)(size_t)(lds0 + SYNC_OFF + 28));
#pragma unroll
  for (int k = 2; k < WOPRE; ++k)
#pragma unroll
    for (int j = 0; j < 5; ++j) wpre[k][j] = ld16(rs, lane * 16, wb + k * 5120 + j * 1024);
}

__global__ __launch_bounds__(512, 2) void k_all(const char* __restrict__ x, const char* __restrict__ wqkv, const char* __restrict__ w1,
                                                const char* __restrict__ b1, const char* __restrict__ w2, const char* __restrict__ wo,
                                                const char* __restrict__ b2, char* ws, char* __restrict__ out) {
  extern __shared__ __attribute__((aligned(16))) char smem_[];
  LAS char* smem = (LAS char*)smem_;
  const int tid = threadIdx.x, lane = tid & 63, w = __builtin_amdgcn_readfirstlane(tid >> 6);
  const int b = blockIdx.x, xcd = b & 7, idx = b >> 3;
  const unsigned lds0 = (unsigned)(size_t)smem;
  asm volatile("" :: "s"(x), "s"(wqkv), "s"(w1), "s"(b1), "s"(w2), "s"(wo), "s"(ws));
  u64 pc_;
  asm volatile("s_getpc_b64 %0" : "=s"(pc_));
  const int rt = (xcd & 1) * 8 + (idx & 7), cg = (xcd >> 1) * 4 + (idx >> 3);
  const int rot = (rt + 3 * cg) % 10;
  const char* W = cg < 9 ? wqkv : w1;
  const int r3 = cg % 3;
  const int u0 = cg < 9 ? 40 * (cg / 3) + (r3 == 0 ? 0 : (r3 == 1 ? 14 : 27)) : (cg < 15 ? 14 * (cg - 9) : 83);
  const int nu = cg < 9 ? (r3 == 0 ? 14 : 13) : (cg < 14 ? 14 : 13);
  const int ldw = cg < 9 ? 3840 : 3072;
  unsigned vA = 0, vb0 = 0, vb1 = 0;
  if (w >= 4) {
    const int lw = w - 4, lr = lane >> 3, ls = lane & 7;
    const int bk = 16 * lw + lr, sw0 = (ls ^ (bk & 2)) << 4, sw1 = sw0 ^ 64;
    const int col0 = (u0 >> 2) * 128;
    vb0 = bk * ldw + col0 + sw0;
    vb1 = (bk + 8) * ldw + col0 + sw1;
    vA = (rt * 128 + 32 * lw + lr) * 1280 + ((ls ^ (lr >> 1)) << 4);
    const unsigned a1 = (vA + 10240) ^ 64u, a2 = vA + 20480, a3 = (vA + 30720) ^ 64u;
    dma_stage<4>(x + rot * 128, vA, a1, a2, a3, lds0 + lw * 4096, W + (size_t)rot * 64 * ldw, vb0, vb1, lds0 + 16384 + lw * 2048);
    if (b < 170) {
      const int rkb = b / 5, rnb = b - 5 * rkb;
      const char* Wr = rkb < 24 ? w2 + (size_t)rkb * 64 * 1280 : wo + (size_t)(rkb - 24) * 64 * 1280;
      const unsigned r0 = bk * 1280 + rnb * 256 + sw0, r1 = (bk + 8) * 1280 + rnb * 256 + sw1;
      dma1(Wr, r0, lds0 + RT_OFF + lw * 2048);
      dma1(Wr, r1, lds0 + RT_OFF + lw * 2048 + 1024);
      dma1(Wr + 128, r0, lds0 + RT_OFF + 8192 + lw * 2048);
      dma1(Wr + 128, r1, lds0 + RT_OFF + 8192 + lw * 2048 + 1024);
    }
#pragma unroll
    for (int s = 1; s < 3; ++s) {
      const int kt = rot + s >= 10 ? rot + s - 10 : rot + s;
      dma_stage<4>(x + kt * 128, vA, a1, a2, a3, lds0 + s * STG + lw * 4096, W + (size_t)kt * 64 * ldw, vb0, vb1,
                   lds0 + s * STG + 16384 + lw * 2048);
    }
  }
  SB();
  const int g = lane >> 4, l15 = lane & 15, q4 = l15 >> 2, p4 = lane & 3;
  const char* pc0 = (const char*)(pc_ & ~(u64)63);
  u64 tag;
  const __amdgpu_buffer_rsrc_t rs = __builtin_amdgcn_make_buffer_rsrc(ws, 0, WS_USED, 0x00020000);
  LAS unsigned* sync = (LAS unsigned*)(smem + SYNC_OFF);
  STAMP(0);
  const int mt = (xcd >> 1) * 8 + (idx & 7), ct = (xcd & 1) * 4 + (idx >> 3);
  const int head = 4 * xcd + (idx & 3), qb = idx >> 2;
  const int ch = (5 * head) >> 1, odd = head & 1;
  const Ctx c = {x, W, b1, w2, wo, ws, lds0, vA, vb0, vb1, b, rt, cg, rot, ct, qb, ch, w, lane, u0, nu, ldw};
  const size_t orow = (size_t)(64 * mt + 16 * (w & 3) + l15) * 1280 + (80 * ct + 4 * g) * 2;
  u32x2 xv[3];
  f32x4 acc[4][5];
  const unsigned va = g * 1024 + l15 * 16;
  bf16x8 wpre[5][5];
  const unsigned wb = w < 4 ? OFF_W2F + (ct * 48 + 12 * w) * 5120 : OFF_WOF + (ct * 20 + 5 * (w - 4)) * 5120;

  if (__builtin_expect(w >= 4, 1)) {
    load_phase(c, rs, tag, wpre, wb);
    STAMP(2);
    const int lw = w - 4, lt = tid - 256;
    const f32x4 z4 = {0.f, 0.f, 0.f, 0.f};
    {
      const u32x4 one4 = {0x3f803f80u, 0x3f803f80u, 0x3f803f80u, 0x3f803f80u};
      *(LAS u32x4*)(smem + V_OFF + 3 * VP + lt * 16) = one4;
      if (lt < 40) *(LAS u32x4*)(smem + V_OFF + 3 * VP + 4096 + lt * 16) = one4;
      *(LAS u32x4*)(smem + K_OFF + 3 * KP + lt * 16) = u32x4{qb == 0 ? 0xff80u : 0u, 0u, 0u, 0u};
      *(LAS u32x4*)(smem + K_OFF + 3 * KP + 4096 + lt * 16) = u32x4{0u, 0u, 0u, 0u};
      const int ty = lane < 24 ? lane >> 3 : 0, rr0 = 2 * qb - 2 + ((lane >> 1) & 3);
      const int rr = rr0 < 0 ? 0 : (ty == 0 && rr0 < 2 * qb ? 2 * qb : rr0);
      poll_flags(ws + FL_A + wg_of(rr, 3 * ty + grp_qkv((ch + (lane & 1) * 2) >> 1)) * 128, true, tag);
    }
    STAMP(11);
#pragma unroll
    for (int i = 0; i < 2; ++i) {
      const int seg = 2 * lw + i;
      int tb = 4 * qb - 4 + seg; tb = tb < 0 ? 0 : tb;
      dma_kv(ws + OFF_QKV + (size_t)(tb * 240 + 80 + ch) * 1024, lane * 16, lane * 16 + 81920, lds0 + K_OFF + seg * 1024);
    }
    const int l31 = lane & 31, hh = lane >> 5;
    u32x4 qraw[2][2];
#pragma unroll
    for (int a = 0; a < 2; ++a)
#pragma unroll
      for (int ks = 0; ks < 2; ++ks)
        qraw[a][ks] = __builtin_amdgcn_raw_buffer_load_b128(rs, (ks ? 2 : hh) * 1024 + (32 * a + l31) * 16,
                                                            OFF_QKV + ((4 * qb + lw) * 240 + ch) * 1024, SC1);
    lds_arrive(sync + 5);
    bf16x8 qf[2][2];
#pragma unroll
    for (int a = 0; a < 2; ++a) {
      u32x4 v0 = qraw[a][0], v1 = qraw[a][1];
      if (odd && hh == 0) { v0[0] = 0u; v0[1] = 0u; }
      if (hh) v1 = u32x4{0x3f80u, 0u, 0u, 0u};
      else if (!odd) { v1[2] = 0u; v1[3] = 0u; }
      qf[a][0] = __builtin_bit_cast(bf16x8, v0);
      qf[a][1] = __builtin_bit_cast(bf16x8, v1);
    }
    f32x16 mF, z16;
#pragma unroll
    for (int e = 0; e < 16; ++e) {
      mF[e] = (e & 3) + 8 * (e >> 2) + 4 * hh > l31 ? 0.f : -INFINITY;
      z16[e] = 0.f;
    }
    asm volatile("" : "+v"(mF), "+v"(qf[0][0]), "+v"(qf[0][1]), "+v"(qf[1][0]), "+v"(qf[1][1]));
    WAIT_VM(0);
    WAIT_LGKM0();
    lds_arrive(sync);
    lds_wait(sync, 4);
    __builtin_amdgcn_s_setprio(2);
    STAMP(5);
    unsigned kp = lds0 + K_OFF + hh * KP + (64 * lw + l31) * 16;
    const bool ones = (g & 1) && (p4 >> 1);
    unsigned vp = lds0 + V_OFF + (2 * (g & 1) + (p4 >> 1)) * VP + ((ones ? 0 : 64 * lw) + 4 * hh + q4) * 16 + (p4 & 1) * 8;
    unsigned ov = l31 * 16 + hh * 8;
    bf16x8 q0 = qf[0][0], q1 = qf[0][1];
#define LDK(T, ks) (*(LAS bf16x8*)(size_t)(kp + (T) * 512 + (ks) * 2 * KP))
#define MF32(a, b, c) __builtin_amdgcn_mfma_f32_32x32x16_bf16(a, b, c, 0, 0, 0)
#define EX2(s_, e) pk2(__builtin_amdgcn_exp2f((s_)[e]), __builtin_amdgcn_exp2f((s_)[(e) + 1]))
    auto tile_max = [&](int T) {
      f32x16 sp = MF32(LDK(T, 1), q1, MF32(LDK(T, 0), q0, z16));
      float m0 = -INFINITY, m1 = -INFINITY;
#pragma unroll
      for (int e = 0; e < 16; ++e) {
        const bool off = T == 0 ? mF[e] != 0.f : (T == 8 ? mF[e] == 0.f : false);
        sp[e] = off ? -INFINITY : sp[e];
      }
#pragma unroll
      for (int e = 0; e < 16; e += 4) { m0 = MAX3(m0, sp[e], sp[e + 1]); m1 = MAX3(m1, sp[e + 2], sp[e + 3]); }
      m0 = fmaxf(m0, m1);
      return fmaxf(m0, __shfl_xor(m0, 32));
    };
#pragma unroll 1
    for (int un = 0; un < 2; ++un) {
      float mx = tile_max(8);
      f32x16 oa;
#pragma unroll 1
      for (int pass = 0; pass < 2; ++pass) {
      f32x16 ca;
#pragma unroll
      for (int e = 0; e < 16; ++e) ca[e] = -mx;
      oa = z16;
      {
        bf16x8 kc[2][2], vf[2][2];
        f32x16 sc[2];
        u32x4 pa[2] = {{0u, 0u, 0u, 0u}, {0u, 0u, 0u, 0u}};
        kc[0][0] = LDK(0, 0); kc[0][1] = LDK(0, 1); kc[1][0] = LDK(1, 0); kc[1][1] = LDK(1, 1);
        sc[0] = MF32(kc[0][1], q1, MF32(kc[0][0], q0, ca + mF));
        sc[1] = z16;
        asm volatile("" :: "v"(sc[0]));
        SB();
#pragma unroll
        for (int T = 0; T < 10; ++T) {
          const int c = T & 1, n = c ^ 1;
          if (T < 9) {
#pragma unroll
            for (int ks = 0; ks < 2; ++ks) vf[c][ks] = cat44(trread(vp + T * 512 + ks * 256), trread(vp + T * 512 + ks * 256 + 128));
          }
          if (T < 8) sc[n] = MF32(kc[n][1], q1, MF32(kc[n][0], q0, ca));
          if (T < 7) { kc[c][0] = LDK(T + 2, 0); kc[c][1] = LDK(T + 2, 1); }
          if (T > 0) oa = MF32(vf[n][1], __builtin_bit_cast(bf16x8, pa[1]), MF32(vf[n][0], __builtin_bit_cast(bf16x8, pa[0]), oa));
          if (T == 8) {
#pragma unroll
            for (int e = 0; e < 16; ++e) sc[c][e] = mF[e] == 0.f ? -INFINITY : sc[c][e];
          }
          if (T < 9) {
#pragma unroll
            for (int e = 0; e < 4; ++e) { pa[0][e] = EX2(sc[c], 2 * e); pa[1][e] = EX2(sc[c], 8 + 2 * e); }
          }
          SGB(0x100, 6);
          if (T == 8) SGB(0x2, 16);
          SGB(0x8, 1); SGB(0x400, 4); SGB(0x2, 1);
#pragma unroll
          for (int k = 0; k < 3; ++k) { SGB(0x8, 1); SGB(0x400, 2); SGB(0x2, 1); SGB(0x400, 2); SGB(0x2, 1); }
          SGB(0x2, 1);
          asm volatile("" :: "v"(sc[n]), "v"(oa), "v"(pa[0]), "v"(pa[1]));
          SB();
        }
      }
      if (pass || __all(oa[12] < 1.8446744e19f)) break;
#pragma unroll 1
      for (int T = 0; T < 8; ++T) mx = fmaxf(mx, tile_max(T));
      }
      const float il = __builtin_amdgcn_rcpf(oa[12]);
#pragma unroll
      for (int pl = 0; pl < 3; ++pl) {
        const bool ok = pl == 1 || (pl == 0 ? !(odd && hh == 0) : (odd || hh == 0));
        if (ok) {
          const u32x2 pkd = {pk2(oa[4 * pl] * il, oa[4 * pl + 1] * il), pk2(oa[4 * pl + 2] * il, oa[4 * pl + 3] * il)};
          __builtin_amdgcn_raw_buffer_store_b64(pkd, rs, pl * 1024 + ov, OFF_O + ((4 * qb + lw) * 80 + ch) * 1024, SC1);
        }
      }
      kp += 512; vp += ones ? 0 : 512; ov += 512;
      q0 = qf[1][0]; q1 = qf[1][1];
    }
    __builtin_amdgcn_s_setprio(0);
    lds_arrive(sync + 4);
    STAMP(6);
#pragma unroll
    for (int k = WOPRE; k < 5; ++k)
#pragma unroll
      for (int j = 0; j < 5; ++j) wpre[k][j] = ld16(rs, lane * 16, wb + k * 5120 + j * 1024);
#pragma unroll
    for (int j = 0; j < 2; ++j) xv[j] = *(const u32x2*)(x + orow + (3 + j) * 32);
    wait_vm<5 * (5 - WOPRE) + 2>();
    const bool last_o = lds_arrive(sync + 1) == 3;
    if (last_o && lane == 0) __hip_atomic_store((GAS u64*)(ws + FL_O + b * 128), tag, RLX_AGENT);
    const unsigned ab = OFF_O + (mt * 80 + 20 * lw) * 1024;
    {
      const int hd = 8 * lw + (lane & 7);
      poll_flags(ws + FL_O + (((((mt >> 2) << 2) | (hd & 3)) << 3) | (hd >> 2)) * 128, lane < 8, tag);
    }
    STAMP(7);
    bf16x8 af[4][5];
#pragma unroll
    for (int i = 0; i < 4; ++i)
#pragma unroll
      for (int k = 0; k < 5; ++k) af[i][k] = ld16(rs, va + ((lw + 1 + i) & 3) * 256, ab + k * 4096);
    SB();
    lds_wait(sync + 1, 4);
    lds_wait(sync + 6, 4);
    f32x4 own[2];
#pragma unroll
    for (int i = 0; i < 4; ++i) {
      const int mi = (lw + 1 + i) & 3;
      f32x4 a5[5] = {z4, z4, z4, z4, z4};
#pragma unroll
      for (int k = 0; k < 5; ++k)
#pragma unroll
        for (int j = 0; j < 5; ++j) a5[j] = mfma16(wpre[k][j], af[i][k], a5[j]);
#pragma unroll
      for (int j = 0; j < 3; ++j) *(LAS f32x4*)(smem + REDS1_OFF + ((mi * 4 + lw) * 3 + j) * 1024 + lane * 16) = a5[j];
      if (i < 3) {
#pragma unroll
        for (int j = 0; j < 2; ++j) *(LAS f32x4*)(smem + REDS2_OFF + ((mi * 3 + 2 - i) * 2 + j) * 1024 + lane * 16) = a5[3 + j];
      } else { own[0] = a5[3]; own[1] = a5[4]; }
    }
    STAMP(8);
    __syncthreads();
    STAMP(9);
#pragma unroll
    for (int j = 0; j < 2; ++j) {
      f32x4 sv = own[j];
#pragma unroll
      for (int k = 0; k < 3; ++k) sv += *(LAS f32x4*)(smem + REDS2_OFF + ((lw * 3 + k) * 2 + j) * 1024 + lane * 16);
      const f32x4 gt = *(LAS f32x4*)(smem + GX_OFF + (lw * 2 + j) * 1024 + lane * 16);
      *(u32x2*)(out + orow + (3 + j) * 32) = fin4(xv[j], sv, gt);
    }

  } else {
    if (idx * 4 + w < CODE_KB) {
      const u32x4 t0 = *(const u32x4*)(pc0 + (idx * 4 + w) * 1024 + lane * 16);
      asm volatile("" :: "v"(t0));
    }
    {
      const char* p = ws;
      asm volatile("" : "+s"(p));
      tag = *(const CAS u64*)(p + OFF_FL) + 1;
    }
    mfma_phase(c, rs);
    STAMP(3);
    WAIT_VM(0);
    if (lds_arrive(sync + 2) == 3 && lane == 0) __hip_atomic_store((GAS u64*)(ws + FL_A + b * 128), tag, RLX_AGENT);
    if (b == 0 && w == 0) {
#pragma unroll 1
      for (int spins = 0; spins < (1 << 20); ++spins) {
        bool ok = true;
#pragma unroll
        for (int i = 0; i < 4; ++i) ok = ok && __hip_atomic_load((const GAS u64*)(ws + FL_W + (lane + 64 * i) * 128), RLX_AGENT) == tag;
        if (__all(ok)) break;
        __builtin_amdgcn_s_sleep(1);
      }
      if (lane == 0) __hip_atomic_store((GAS u64*)(ws + OFF_FL), tag, RLX_AGENT);
    }
    STAMP(4);
#pragma unroll
    for (int k = 0; k < 4; ++k)
#pragma unroll
      for (int j = 0; j < 5; ++j) wpre[k][j] = ld16(rs, lane * 16, wb + k * 5120 + j * 1024);
    const unsigned wl_ = lds0 + slab_of(w);
    lds_wait(sync + 2, 4);
    dma5_sc1(ws + wb + 4 * 5120, lane * 16, wl_);
    dma5_sc1(ws + wb + 5 * 5120, lane * 16, wl_ + 5120);
    const unsigned ab = OFF_H + (mt * 192 + 48 * w) * 1024;
    poll_flags(ws + FL_A + wg_of(mt >> 1, 9 + grp_h(24 * w + (lane == 0 ? 0 : (lane == 1 ? 12 : 23)))) * 128, lane < 3, tag);
    lds_wait(sync + 5, 4);
    STAMP(7);
    bf16x8 af[4][4];
#pragma unroll
    for (int k = 0; k < 4; ++k)
#pragma unroll
      for (int i = 0; i < 4; ++i) af[k][i] = ld16(rs, va + i * 256, ab + k * 4096);
    dma5_sc1(ws + ab + 4 * 4096, lane * 16, wl_ + 10240);
    dma5_sc1(ws + ab + 4 * 4096 + 3072, lane * 16, wl_ + 10240 + 3072);
    {
      SB();
      WAIT_VM(0);
    }
    STAMP(12);
#pragma unroll
    for (int i = 0; i < 4; ++i)
#pragma unroll
      for (int j = 0; j < 5; ++j) acc[i][j] = f32x4{0.f, 0.f, 0.f, 0.f};
#pragma unroll
    for (int k = 0; k < 12; ++k) {
      const int cs = k & 3;
      if (k == W2HOLD) {
        unsigned tv, ts;
        asm volatile(".Lp%=:\n\tds_read_b32 %0, %2\n\ts_waitcnt lgkmcnt(0)\n\tv_readfirstlane_b32 %1, %0\n\t"
                     "s_cmp_ge_u32 %1, 4\n\ts_cbranch_scc1 .Le%=\n\ts_sleep 2\n\ts_branch .Lp%=\n.Le%=:"
                     : "=&v"(tv), "=&s"(ts) : "v"(lds0 + SYNC_OFF + 16) : "memory", "scc");
      }
#pragma unroll
      for (int i = 0; i < 4; ++i)
#pragma unroll
        for (int j = 0; j < 5; ++j) acc[i][j] = mfma16(wpre[cs][j], af[cs][i], acc[i][j]);
      SB();
      if (k < 2) {
#pragma unroll
        for (int j = 0; j < 5; ++j) wpre[cs][j] = *(LAS bf16x8*)(size_t)(wl_ + k * 5120 + j * 1024 + lane * 16);
#pragma unroll
        for (int i = 0; i < 4; ++i) af[cs][i] = *(LAS bf16x8*)(size_t)(wl_ + 10240 + k * 4096 + va + i * 256);
      } else if (k < 8) {
#pragma unroll
        for (int i = 0; i < 4; ++i) af[cs][i] = ld16(rs, va + i * 256, ab + (k + 4) * 4096);
#pragma unroll
        for (int j = 0; j < 5; ++j) wpre[cs][j] = ld16(rs, lane * 16, wb + (k + 4) * 5120 + j * 1024);
      }
      SB();
    }
    lds_arrive(sync + 6);
    lds_wait(sync + 6, 4);
    STAMP(8);
    red_write(smem + REDG_OFF, w, lane, acc);
    u32x2 bb[5];
#pragma unroll
    for (int j = 0; j < 5; ++j) {
      if (j < 3) xv[j] = *(const u32x2*)(x + orow + j * 32);
      bb[j] = *(const u32x2*)(b2 + (80 * ct + 16 * j + 4 * g) * 2);
    }
    WAIT_LGKM0();
    lds_arrive(sync + 3);
    lds_wait(sync + 3, 4);
    f32x4 gt[5];
    red_sum(smem + REDG_OFF, w, lane, acc, gt);
#pragma unroll
    for (int j = 0; j < 5; ++j) {
      const float bv[4] = {bflo(bb[j][0]), bfhi(bb[j][0]), bflo(bb[j][1]), bfhi(bb[j][1])};
#pragma unroll
      for (int e = 0; e < 4; ++e) {
        const float t = rbf(rbf(gt[j][e]) + bv[e]);
        gt[j][e] = rbf(t * __builtin_amdgcn_rcpf(1.f + __expf(-t)));
      }
      if (j >= 3) *(LAS f32x4*)(smem + GX_OFF + (w * 2 + j - 3) * 1024 + lane * 16) = gt[j];
    }
    __syncthreads();
    STAMP(9);
#pragma unroll
    for (int j = 0; j < 3; ++j) {
      f32x4 sv = *(LAS f32x4*)(smem + REDS1_OFF + ((w * 4) * 3 + j) * 1024 + lane * 16);
#pragma unroll
      for (int k = 1; k < 4; ++k) sv += *(LAS f32x4*)(smem + REDS1_OFF + ((w * 4 + k) * 3 + j) * 1024 + lane * 16);
      *(u32x2*)(out + orow + j * 32) = fin4(xv[j], sv, gt[j]);
    }
  }
  STAMP(10);
}

extern "C" void kernel_launch(void* const* d_in, const int* in_sizes, int n_in, void* d_out, int out_size, void* d_ws,
                              size_t ws_size, hipStream_t stream) {
  (void)hipFuncSetAttribute((const void*)k_all, hipFuncAttributeMaxDynamicSharedMemorySize, LDS_ALL);
  k_all<<<256, 512, LDS_ALL, stream>>>((const char*)d_in[0], (const char*)d_in[1], (const char*)d_in[4], (const char*)d_in[5],
                                       (const char*)d_in[6], (const char*)d_in[2], (const char*)d_in[7], (char*)d_ws, (char*)d_out);
}
```

```cpp
#include <hip/hip_runtime.h>
#include <stdint.h>

#define LAS __attribute__((address_space(3)))
#define GAS __attribute__((address_space(1)))
#define CAS __attribute__((address_space(4)))
typedef short bf16x8 __attribute__((ext_vector_type(8)));
typedef short s16x4 __attribute__((ext_vector_type(4)));
typedef float f32x4 __attribute__((ext_vector_type(4)));
typedef unsigned u32x4 __attribute__((ext_vector_type(4)));
typedef unsigned u32x2 __attribute__((ext_vector_type(2)));
typedef __bf16 bf16x2_t __attribute__((ext_vector_type(2)));
typedef float f32x2 __attribute__((ext_vector_type(2)));
typedef float f32x16 __attribute__((ext_vector_type(16)));
typedef unsigned long long u64;

constexpr unsigned OFF_FL = 0;
constexpr unsigned FL_W = 1024, FL_Q = FL_W + 32768, FL_H = FL_Q + 32768, FL_O = FL_H + 32768;
constexpr unsigned OFF_RS = 262144;
constexpr unsigned OFF_CS = OFF_RS + 16 * 10 * 2048;
constexpr unsigned OFF_QKV = OFF_CS + 2 * 16 * 10 * 1024;
constexpr unsigned OFF_H = OFF_QKV + 32 * 240 * 1024;
constexpr unsigned OFF_O = OFF_H + 32 * 192 * 1024;
constexpr unsigned OFF_W2F = OFF_O + 32 * 80 * 1024;
constexpr unsigned OFF_WOF = OFF_W2F + 8 * 48 * 5120;
constexpr unsigned WS_USED = OFF_WOF + 8 * 20 * 5120;

__device__ __forceinline__ f32x4 mfma16(bf16x8 a, bf16x8 b, f32x4 c) {
  return __builtin_amdgcn_mfma_f32_16x16x32_bf16(a, b, c, 0, 0, 0);
}
__device__ __forceinline__ unsigned pk2(float a, float b) {
  f32x2 v = {a, b};
  bf16x2_t r = __builtin_convertvector(v, bf16x2_t);
  return __builtin_bit_cast(unsigned, r);
}
__device__ __forceinline__ float rbf(float a) { return __builtin_bit_cast(float, pk2(0.f, a) & 0xffff0000u); }
__device__ __forceinline__ float bflo(unsigned u) { return __builtin_bit_cast(float, u << 16); }
__device__ __forceinline__ float bfhi(unsigned u) { return __builtin_bit_cast(float, u & 0xffff0000u); }
__device__ __forceinline__ u32x2 fin4(u32x2 xv, f32x4 sv, f32x4 gt) {
  return u32x2{pk2(bflo(xv[0]) + rbf(rbf(sv[0]) * gt[0]), bfhi(xv[0]) + rbf(rbf(sv[1]) * gt[1])),
               pk2(bflo(xv[1]) + rbf(rbf(sv[2]) * gt[2]), bfhi(xv[1]) + rbf(rbf(sv[3]) * gt[3]))};
}
__device__ __forceinline__ s16x4 trread(unsigned a) {
  return __builtin_amdgcn_ds_read_tr16_b64_v4i16((LAS s16x4*)(size_t)a);
}
__device__ __forceinline__ bf16x8 cat44(s16x4 a, s16x4 b) {
  return __builtin_shufflevector(a, b, 0, 1, 2, 3, 4, 5, 6, 7);
}
#define WAIT_VM(n) asm volatile("s_waitcnt vmcnt(" #n ")" ::: "memory")
#define WAIT_LGKM0() __builtin_amdgcn_s_waitcnt(0xC07F)
#define BAR() __builtin_amdgcn_s_barrier()
#define SB() __builtin_amdgcn_sched_barrier(0)
#define SGB(m, n) __builtin_amdgcn_sched_group_barrier(m, n, 0)
#define MAX3(a, b, c) __builtin_fmaxf(__builtin_fmaxf(a, b), c)
#define RLX_AGENT __ATOMIC_RELAXED, __HIP_MEMORY_SCOPE_AGENT
constexpr int SC1 = 16;
constexpr int CODE_KB = 39;
template <int N> struct IC { static constexpr int value = N; };

#define STAMP(i)
#define CLK(v)

__device__ __forceinline__ void dma1(const char* sbase, unsigned voff, unsigned lds) {
  unsigned keep;
  asm volatile("s_mov_b32 %0, m0\n\ts_mov_b32 m0, %3\n\ts_nop 0\n\tglobal_load_lds_dwordx4 %1, %2\n\ts_mov_b32 m0, %0"
               : "=&s"(keep) : "v"(voff), "s"(sbase), "s"(lds) : "memory");
}
__device__ __forceinline__ void dma_stage(const char* sa, unsigned va0, unsigned va1, unsigned va2, unsigned va3, unsigned la,
                                          const char* sb, unsigned vb0, unsigned vb1, unsigned lb) {
  unsigned keep;
  asm volatile("s_mov_b32 %0, m0\n\ts_mov_b32 m0, %10\n\ts_nop 0\n\tglobal_load_lds_dwordx4 %8, %7\n\t"
               "s_add_u32 m0, m0, 1024\n\ts_nop 0\n\tglobal_load_lds_dwordx4 %9, %7\n\t"
               "s_add_u32 m0, m0, 7040\n\ts_nop 0\n\tglobal_load_lds_dwordx4 %8, %7 offset:128\n\t"
               "s_add_u32 m0, m0, 1024\n\ts_nop 0\n\tglobal_load_lds_dwordx4 %9, %7 offset:128\n\t"
               "s_mov_b32 m0, %6\n\ts_nop 0\n\tglobal_load_lds_dwordx4 %2, %1\n\t"
               "s_add_u32 m0, m0, 1024\n\ts_nop 0\n\tglobal_load_lds_dwordx4 %3, %1\n\t"
               "s_add_u32 m0, m0, 1024\n\ts_nop 0\n\tglobal_load_lds_dwordx4 %4, %1\n\t"
               "s_add_u32 m0, m0, 1024\n\ts_nop 0\n\tglobal_load_lds_dwordx4 %5, %1\n\ts_mov_b32 m0, %0"
               : "=&s"(keep) : "s"(sa), "v"(va0), "v"(va1), "v"(va2), "v"(va3), "s"(la), "s"(sb), "v"(vb0), "v"(vb1), "s"(lb)
               : "memory", "scc");
}
__device__ __forceinline__ void dma_kv(const char* sb, unsigned vk, unsigned vv, unsigned l) {
  unsigned keep;
  asm volatile("s_mov_b32 %0, m0\n\ts_mov_b32 m0, %4\n\ts_nop 0\n\tglobal_load_lds_dwordx4 %2, %1 sc1\n\t"
               "s_add_u32 m0, m0, 7168\n\ts_nop 0\n\tglobal_load_lds_dwordx4 %2, %1 offset:1024 sc1\n\t"
               "s_add_u32 m0, m0, 7168\n\ts_nop 0\n\tglobal_load_lds_dwordx4 %2, %1 offset:2048 sc1\n\t"
               "s_add_u32 m0, m0, 18432\n\ts_nop 0\n\tglobal_load_lds_dwordx4 %3, %1 sc1\n\t"
               "s_add_u32 m0, m0, 7232\n\ts_nop 0\n\tglobal_load_lds_dwordx4 %3, %1 offset:1024 sc1\n\t"
               "s_add_u32 m0, m0, 7232\n\ts_nop 0\n\tglobal_load_lds_dwordx4 %3, %1 offset:2048 sc1\n\t"
               "s_mov_b32 m0, %0"
               : "=&s"(keep) : "s"(sb), "v"(vk), "v"(vv), "s"(l) : "memory", "scc");
}
__device__ __forceinline__ void dma5_sc1(const char* sb, unsigned v, unsigned l) {
  unsigned keep;
  asm volatile("s_mov_b32 %0, m0\n\ts_mov_b32 m0, %3\n\ts_nop 0\n\tglobal_load_lds_dwordx4 %2, %1 sc1\n\t"
               "global_load_lds_dwordx4 %2, %1 offset:1024 sc1\n\tglobal_load_lds_dwordx4 %2, %1 offset:2048 sc1\n\t"
               "global_load_lds_dwordx4 %2, %1 offset:3072 sc1\n\ts_add_u32 m0, m0, 1024\n\ts_nop 0\n\t"
               "global_load_lds_dwordx4 %4, %1 offset:3072 sc1\n\ts_mov_b32 m0, %0"
               : "=&s"(keep) : "s"(sb), "v"(v), "s"(l), "v"(v + 1024) : "memory", "scc");
}
__device__ __forceinline__ u32x2 aload8(const char* sbase, unsigned voff) {
  u32x2 r;
  asm volatile("global_load_dwordx2 %0, %1, %2 sc1" : "=v"(r) : "v"(voff), "s"(sbase) : "memory");
  return r;
}
__device__ __forceinline__ u32x4 aload16(const char* sbase, unsigned voff) {
  u32x4 r;
  asm volatile("global_load_dwordx4 %0, %1, %2 sc1" : "=v"(r) : "v"(voff), "s"(sbase) : "memory");
  return r;
}
#define PIN(x) asm volatile("" : "+v"(x))
template <int N> __device__ __forceinline__ void wait_vm() { asm volatile("s_waitcnt vmcnt(%0)" :: "i"(N) : "memory"); }
__device__ __forceinline__ void poll_flags(const char* flag, bool need, u64 tag) {
#pragma unroll 1
  for (int spins = 0; spins < (1 << 20); ++spins) {
    const u64 v = need ? __hip_atomic_load((const GAS u64*)flag, RLX_AGENT) : tag;
    if (__all(v == tag)) break;
    __builtin_amdgcn_s_sleep(1);
  }
  __builtin_amdgcn_fence(__ATOMIC_ACQUIRE, "wavefront");
}
__device__ __forceinline__ unsigned lds_arrive(LAS unsigned* c) {
  unsigned r = 0;
  if ((threadIdx.x & 63) == 0) r = __hip_atomic_fetch_add(c, 1u, __ATOMIC_RELAXED, __HIP_MEMORY_SCOPE_WORKGROUP);
  return __builtin_amdgcn_readfirstlane(r);
}
__device__ __forceinline__ void lds_wait_sleep(LAS unsigned* c, unsigned n) {
#pragma unroll 1
  for (int spins = 0; spins < (1 << 22); ++spins) {
    if (__builtin_amdgcn_readfirstlane(__hip_atomic_load(c, __ATOMIC_RELAXED, __HIP_MEMORY_SCOPE_WORKGROUP)) >= n) break;
    __builtin_amdgcn_s_sleep(2);
  }
  __builtin_amdgcn_fence(__ATOMIC_ACQUIRE, "wavefront");
}
__device__ __forceinline__ void lds_wait(LAS unsigned* c, unsigned n) {
#pragma unroll 1
  for (int spins = 0; spins < (1 << 22); ++spins) {
    if (__builtin_amdgcn_readfirstlane(__hip_atomic_load(c, __ATOMIC_RELAXED, __HIP_MEMORY_SCOPE_WORKGROUP)) >= n) break;
  }
  __builtin_amdgcn_fence(__ATOMIC_ACQUIRE, "wavefront");
}

#define WOPRE 4
#define W2HOLD 8
constexpr int STG = 32768, RT_OFF = 4 * STG, LDS_ALL = 163840;
constexpr int B1_OFF = 159744;
constexpr int ST_OFF = 160768;
constexpr int CS_OFF = ST_OFF + 1024, SYNC_OFF = ST_OFF + 2048;
constexpr int KP = 8192, VP = 8192 + 64;
constexpr int K_OFF = 0, V_OFF = K_OFF + 4 * KP;
__device__ __forceinline__ constexpr int slab_of(int w) { return 65536 + w * 20480; }
constexpr int REDG_OFF = 0, GX_OFF = 61440, REDS1_OFF = GX_OFF + 8192, REDS2_OFF = REDS1_OFF + 49152;

__device__ __forceinline__ void st16(u32x4 v, __amdgpu_buffer_rsrc_t rs, unsigned voff, unsigned soff) {
  __builtin_amdgcn_raw_buffer_store_b128(v, rs, voff + soff, 0, SC1);
}
__device__ __forceinline__ bf16x8 ld16(__amdgpu_buffer_rsrc_t rs, unsigned voff, unsigned soff) {
  return __builtin_bit_cast(bf16x8, __builtin_amdgcn_raw_buffer_load_b128(rs, voff, soff, SC1));
}
__device__ __forceinline__ void red_write(LAS char* red, int kw, int lane, const f32x4 (&acc)[4][5]) {
#pragma unroll
  for (int i = 0; i < 4; ++i) {
    if (i != kw) {
      const int slot = kw - (kw > i ? 1 : 0);
#pragma unroll
      for (int j = 0; j < 5; ++j) *(LAS f32x4*)(red + ((i * 3 + slot) * 5 + j) * 1024 + lane * 16) = acc[i][j];
    }
  }
}
__device__ __forceinline__ void red_sum(LAS char* red, int kw, int lane, const f32x4 (&acc)[4][5], f32x4 (&o)[5]) {
#pragma unroll
  for (int j = 0; j < 5; ++j) {
    f32x4 a = kw == 0 ? acc[0][j] : (kw == 1 ? acc[1][j] : (kw == 2 ? acc[2][j] : acc[3][j]));
#pragma unroll
    for (int s = 0; s < 3; ++s) a += *(LAS f32x4*)(red + ((kw * 3 + s) * 5 + j) * 1024 + lane * 16);
    o[j] = a;
  }
}
__device__ __forceinline__ int wg_of(int rt, int cg) { return (((cg & 3) * 8 + (rt & 7)) << 3) | ((cg >> 2) << 1) | (rt >> 3); }
template <int N>
__device__ __forceinline__ bool sum_pairs(const u32x4 (&v)[N], unsigned last_on, unsigned tg, float& a, float& b) {
  bool ok = true;
  a = 0.f; b = 0.f;
#pragma unroll
  for (int k = 0; k < N; ++k) {
    ok = ok && v[k][1] == tg && v[k][3] == tg;
    const unsigned m = k == N - 1 ? last_on : 0xffffffffu;
    a += __uint_as_float(v[k][0] & m);
    b += __uint_as_float(v[k][2] & m);
  }
  return __all(ok);
}
template <int N>
__device__ __forceinline__ void regather(__amdgpu_buffer_rsrc_t rs, const unsigned (&vo)[N], unsigned last_on, unsigned tg, float& a, float& b) {
#pragma unroll 1
  for (int spins = 0; spins < (1 << 20); ++spins) {
    u32x4 v[N];
#pragma unroll
    for (int k = 0; k < N; ++k) v[k] = __builtin_amdgcn_raw_buffer_load_b128(rs, vo[k], 0, SC1);
    if (sum_pairs<N>(v, last_on, tg, a, b)) break;
    __builtin_amdgcn_s_sleep(2);
  }
}

#define STG_STAMP(s_)

#define LDX(set, i, ks) xf[set][i] = *(LAS bf16x8*)(size_t)(xa[ks] + (i) * 2048)
#define LDW(set, j, ks) wf[set][j] = cat44(trread(wa[j] + (ks) * 4096), trread(wa[j] + (ks) * 4096 + 512))
#define HALF(set, ks) do { \
    _Pragma("unroll") for (int j = 0; j < NT; ++j) { \
      if (j == 0) { _Pragma("unroll") for (int i = 0; i < 4; ++i) LDX((set) ^ 1, i, ks); } \
      else if (2 * j - 2 < NT) { LDW((set) ^ 1, 2 * j - 2, ks); if (2 * j - 1 < NT) LDW((set) ^ 1, 2 * j - 1, ks); } \
      _Pragma("unroll") for (int i = 0; i < 4; ++i) acc[i][j] = mfma16(wf[set][j], xf[set][i], acc[i][j]); \
      SGB(0x8, 1); SGB(0x100, 1); SGB(0x8, 1); SGB(0x100, 1); SGB(0x8, 1); SGB(0x100, 1); SGB(0x8, 1); SGB(0x100, 1); \
      asm volatile("" :: "v"(acc[0][j]), "v"(acc[1][j]), "v"(acc[2][j]), "v"(acc[3][j])); \
      SB(); \
    } } while (0)
template <int NT, int S0>
__device__ __forceinline__ void mfma_part(unsigned (&xa)[2], unsigned wl, int tl0, int sigl, unsigned lds0, float (&sh)[4], float (&sc)[4],
                                          int row0, float qs, __amdgpu_buffer_rsrc_t rs, unsigned vo, unsigned so, int cs0, int g, char* ws) {
  constexpr bool ISH = S0 == 10;
  unsigned wa[NT];
  f32x4 acc[4][NT];
#pragma unroll
  for (int j = 0; j < NT; ++j) {
    const int tl = tl0 + j;
    wa[j] = wl + (S0 & 3) * STG + (tl >> 2) * 8192 + (((tl & 3) ^ sigl) << 5);
#pragma unroll
    for (int i = 0; i < 4; ++i) acc[i][j] = f32x4{0.f, 0.f, 0.f, 0.f};
  }
  bf16x8 xf[2][4], wf[2][NT];
#pragma unroll
  for (int i = 0; i < 4; ++i) LDX(0, i, 0);
#pragma unroll
  for (int j = 0; j < NT; ++j) LDW(0, j, 0);
  WAIT_LGKM0();
  int buf = S0 & 3;
#pragma unroll 1
  for (int t = 0; t < 10; ++t) {
    HALF(0, 1);
    {
      const int d = buf == 3 ? -3 * STG : STG;
      buf = (buf + 1) & 3;
      xa[0] += d; xa[1] += d;
#pragma unroll
      for (int j = 0; j < NT; ++j) wa[j] += d;
    }
    WAIT_LGKM0();
    if (ISH) {
      asm volatile("s_cmp_lg_u32 %0, 1\n\ts_cbranch_scc1 .Lk%=\n\ts_waitcnt vmcnt(0)\n.Lk%=:\n\t"
                   "s_cmp_ge_u32 %0, 8\n\ts_cbranch_scc1 .Lc%=\n\ts_barrier\n.Lc%=:" :: "s"(t) : "memory", "scc");
    } else BAR();
    STG_STAMP(S0 + t);
    SB();
    HALF(1, 0);
    WAIT_LGKM0();
  }
  STAMP(ISH ? 2 : 13);
  lds_wait((LAS unsigned*)(size_t)(lds0 + SYNC_OFF + (ISH ? 32 : 28)), 4);
  if (!ISH) {
#pragma unroll
    for (int i = 0; i < 4; ++i) {
      const f32x2 st = *(LAS f32x2*)(size_t)(lds0 + ST_OFF + (row0 + 16 * i) * 8);
      sc[i] = st[1]; sh[i] = -st[0] * st[1];
    }
  }
#pragma unroll
  for (int j = 0; j < NT; ++j) {
    const f32x4 c4 = *(LAS f32x4*)(size_t)(lds0 + CS_OFF + (ISH ? 512 : 0) + ((cs0 + j) * 16 + 4 * g) * 4);
    f32x4 bv = {0.f, 0.f, 0.f, 0.f};
    if (ISH) {
      const u32x2 bb = *(LAS u32x2*)(size_t)(lds0 + B1_OFF + ((cs0 + j) * 16 + 4 * g) * 2);
      bv = f32x4{bflo(bb[0]), bfhi(bb[0]), bflo(bb[1]), bfhi(bb[1])};
    }
#pragma unroll
    for (int pp = 0; pp < 2; ++pp) {
      float v[2][4];
#pragma unroll
      for (int i = 0; i < 2; ++i)
#pragma unroll
        for (int r = 0; r < 4; ++r) {
          float t = __builtin_fmaf(sc[2 * pp + i], acc[2 * pp + i][j][r], __builtin_fmaf(sh[2 * pp + i], c4[r], bv[r]));
          if (ISH) { t = t > 0.f ? t : 0.f; t = t * t; } else t *= qs;
          v[i][r] = t;
        }
      const unsigned x0 = pk2(v[0][0], v[0][1]), x1 = pk2(v[0][2], v[0][3]);
      const unsigned y0 = pk2(v[1][0], v[1][1]), y1 = pk2(v[1][2], v[1][3]);
      const auto s0 = __builtin_amdgcn_permlane16_swap(x0, y0, false, false);
      const auto s1 = __builtin_amdgcn_permlane16_swap(x1, y1, false, false);
      const u32x4 o = {s0[0], s1[0], s0[1], s1[1]};
      st16(o, rs, vo + pp * 512, so + 2 * j * 1024);
    }
  }
}

__global__ __launch_bounds__(512, 2) void k_all(const char* __restrict__ x, const char* __restrict__ wqkv, const char* __restrict__ w1,
                                                const char* __restrict__ b1, const char* __restrict__ w2, const char* __restrict__ wo,
                                                const char* __restrict__ b2, char* ws, char* __restrict__ out) {
  extern __shared__ __attribute__((aligned(16))) char smem_[];
  LAS char* smem = (LAS char*)smem_;
  const int tid = threadIdx.x, lane = tid & 63, w = __builtin_amdgcn_readfirstlane(tid >> 6);
  const int b = blockIdx.x, xcd = b & 7, idx = b >> 3;
  const unsigned lds0 = (unsigned)(size_t)smem;
  asm volatile("" :: "s"(x), "s"(wqkv), "s"(w1), "s"(b1), "s"(w2), "s"(wo), "s"(ws));
  const u64 rt0_ = __builtin_amdgcn_s_memrealtime();
  u64 pc_;
  asm volatile("s_getpc_b64 %0" : "=s"(pc_));
  const int rt = (xcd & 1) * 8 + (idx & 7), cg = (xcd >> 1) * 4 + (idx >> 3);
  const int rot = (rt + 3 * cg) % 10;
  const int lw = w - 4, lr = lane >> 3, ls = lane & 7;
  const int bk = 16 * lw + lr, sw0 = (ls ^ (bk & 2)) << 4, sw1 = sw0 ^ 64;
  const unsigned vq0 = bk * 3840 + (cg < 15 ? cg : 14) * 256 + sw0, vq1 = (bk + 8) * 3840 + (cg < 15 ? cg : 14) * 256 + sw1;
  const unsigned vA = (rt * 128 + 32 * lw + lr) * 1280 + ((ls ^ (lr >> 1)) << 4);
  const unsigned vA1 = (vA + 10240) ^ 64u, vA2 = vA + 20480, vA3 = (vA + 30720) ^ 64u;
  const int rkb = b / 5, rnb = b - 5 * rkb;
  if (w >= 4) {
    dma_stage(x + rot * 128, vA, vA1, vA2, vA3, lds0 + lw * 4096, wqkv + (size_t)rot * (64 * 3840), vq0, vq1, lds0 + 16384 + lw * 2048);
    if (b < 170) {
      const char* Wr = rkb < 24 ? w2 + (size_t)rkb * 64 * 1280 : wo + (size_t)(rkb - 24) * 64 * 1280;
      const unsigned r0 = bk * 1280 + rnb * 256 + sw0, r1 = (bk + 8) * 1280 + rnb * 256 + sw1;
      dma1(Wr, r0, lds0 + RT_OFF + lw * 2048);
      dma1(Wr, r1, lds0 + RT_OFF + lw * 2048 + 1024);
      dma1(Wr + 128, r0, lds0 + RT_OFF + 8192 + lw * 2048);
      dma1(Wr + 128, r1, lds0 + RT_OFF + 8192 + lw * 2048 + 1024);
    }
#pragma unroll
    for (int s = 1; s < 4; ++s) {
      const int kt = rot + s >= 10 ? rot + s - 10 : rot + s;
      dma_stage(x + kt * 128, vA, vA1, vA2, vA3, lds0 + s * STG + lw * 4096, wqkv + (size_t)kt * (64 * 3840), vq0, vq1,
                lds0 + s * STG + 16384 + lw * 2048);
    }
  }
  SB();
  const int g = lane >> 4, l15 = lane & 15, q4 = l15 >> 2, p4 = lane & 3;
  const char* pc0 = (const char*)(pc_ & ~(u64)63);
  const __amdgpu_buffer_rsrc_t rs = __builtin_amdgcn_make_buffer_rsrc(ws, 0, WS_USED, 0x00020000);
  LAS unsigned* sync = (LAS unsigned*)(smem + SYNC_OFF);
  STAMP(0);
  u64 tag;
  {
    const char* p = ws;
    asm volatile("" : "+s"(p));
    tag = *(const CAS u64*)(p + OFF_FL) + 1;
  }
  const int lu1 = (6 * cg) & 3;
  const int sigl = ((q4 >> 1) & 1) | ((g & 1) << 1), xsw = g ^ (l15 >> 1);
  const int mt = (xcd >> 1) * 8 + (idx & 7), ct = (xcd & 1) * 4 + (idx >> 3);
  const int head = 4 * xcd + (idx & 3), qb = idx >> 2;
  const int ch = (5 * head) >> 1, odd = head & 1;
  const size_t orow = (size_t)(64 * mt + 16 * (w & 3) + l15) * 1280 + (80 * ct + 4 * g) * 2;
  u32x2 xv[3];
  f32x4 acc[4][5];
  const unsigned va = g * 1024 + l15 * 16;
  bf16x8 wpre[5][5];
  const unsigned wb = w < 4 ? OFF_W2F + (ct * 48 + 12 * w) * 5120 : OFF_WOF + (ct * 20 + 5 * (w - 4)) * 5120;

  if (w >= 4) {
    const int lt = tid - 256;
    const unsigned tg = (unsigned)tag;
    const unsigned vh0 = bk * 3072 + ((6 * cg) >> 2) * 128 + sw0, vh1 = (bk + 8) * 3072 + ((6 * cg) >> 2) * 128 + sw1;
    int ktn = rot + 4 >= 10 ? rot - 6 : rot + 4;
    auto issue = [&](auto S) {
      constexpr int s = decltype(S)::value;
      const unsigned dst = lds0 + (s & 3) * STG;
      const char* sw = s >= 10 ? w1 + (size_t)ktn * (64 * 3072) : wqkv + (size_t)ktn * (64 * 3840);
      dma_stage(x + ktn * 128, vA, vA1, vA2, vA3, dst + lw * 4096, sw, s >= 10 ? vh0 : vq0, s >= 10 ? vh1 : vq1, dst + 16384 + lw * 2048);
      ktn = ktn == 9 ? 0 : ktn + 1;
    };
    if (lt < 16) sync[lt] = 0u;
    WAIT_VM(24);
    BAR();
    STAMP(1);
    const bf16x8 ONE8 = {0x3f80, 0x3f80, 0x3f80, 0x3f80, 0x3f80, 0x3f80, 0x3f80, 0x3f80};
    const f32x4 z4 = {0.f, 0.f, 0.f, 0.f};
    bf16x8 sw_[2][2], sx_[2][2];
    u32x4 gr[5], gc[3];
    u32x2 fl;
    const int fkb = lane < 12 ? 6 * lw + (lane >> 1) : 24 + ((5 * lw) >> 1) + (lane < 18 ? (lane - 12) >> 1 : 0);
    const unsigned fW = FL_W + (fkb * 5 + ((5 * ct + (lane & 1) * 4) >> 3)) * 128;
    const int ty = lane < 24 ? lane >> 3 : 0, rr0 = 2 * qb - 2 + ((lane >> 1) & 3);
    const int rr = rr0 < 0 ? 0 : (ty == 0 && rr0 < 2 * qb ? 2 * qb : rr0);
    const unsigned fQ = FL_Q + wg_of(rr, 5 * ty + ((ch + (lane & 1) * 2) >> 4)) * 128;
    unsigned ro[5], co[3];
#pragma unroll
    for (int k = 0; k < 5; ++k) ro[k] = OFF_RS + (rt * 10 + 2 * k + (lane >> 5)) * 2048 + (32 * lw + (lane & 31)) * 16;
#pragma unroll
    for (int k = 0; k < 3; ++k) co[k] = OFF_CS + (cg * 10 + (k == 2 && g >= 2 ? g : g + 4 * k)) * 1024 + (16 * lw + l15) * 16;
    const unsigned c_on = g < 2 ? 0xffffffffu : 0u;
    auto col_sums = [&](int part) {
#pragma unroll
      for (int k = 0; k < 3; ++k) PIN(gc[k]);
      float c0, c1;
      if (!sum_pairs<3>(gc, c_on, tg, c0, c1)) regather<3>(rs, co, c_on, tg, c0, c1);
      c0 += __shfl_xor(c0, 16); c1 += __shfl_xor(c1, 16);
      c0 += __shfl_xor(c0, 32); c1 += __shfl_xor(c1, 32);
      if (lane < 16) *(LAS f32x2*)(smem + CS_OFF + part * 512 + (16 * lw + lane) * 8) = f32x2{c0, c1};
      WAIT_LGKM0();
      lds_arrive(sync + 7 + part);
    };
    auto stage = [&](auto S) {
      constexpr int s = decltype(S)::value;
      if constexpr (s == 0 || s == 10) {
        const unsigned sb = lds0 + (s & 3) * STG;
#pragma unroll
        for (int i = 0; i < 2; ++i) {
          const int lux = ((s == 0 ? 0 : lu1) + 2 * lw + i) & 7;
          const unsigned wq = sb + 16384 + (lux >> 2) * 8192 + (8 * g + q4) * 128 + (((lux & 3) ^ sigl) << 5) + p4 * 8;
          sw_[i][0] = cat44(trread(wq), trread(wq + 512));
          sw_[i][1] = cat44(trread(wq + 4096), trread(wq + 4608));
          if (s == 0) {
            const unsigned xq = sb + (32 * lw + 16 * i + l15) * 128;
            sx_[i][0] = *(LAS bf16x8*)(size_t)(xq + (xsw << 4));
            sx_[i][1] = *(LAS bf16x8*)(size_t)(xq + ((xsw ^ 4) << 4));
          }
        }
      }
      wait_vm<(s == 6 || s == 7) ? 24 : (s >= 10 && s <= 12 ? 16 + 5 * WOPRE : ((s == 15 || s == 16) ? 19 : (s == 17 ? 0 : 16)))>();
      WAIT_LGKM0();
      BAR();
      if constexpr (s < 16) issue(IC<s + 4>{});
      if constexpr (s == 2 || s == 3) {
        if (b < 170) {
          const unsigned dstb = rkb < 24 ? OFF_W2F : OFF_WOF;
          const int nks = rkb < 24 ? 48 : 20, kb = rkb < 24 ? rkb : rkb - 24;
          const int nl = 2 * lw + s - 2, nt = 8 * rnb + nl, c5 = nt / 5, jj = nt - 5 * c5;
          const unsigned rp = lds0 + RT_OFF + (nl >> 2) * 8192 + (8 * g + q4) * 128 + (((nl & 3) ^ sigl) << 5) + p4 * 8;
#pragma unroll
          for (int ks = 0; ks < 2; ++ks) {
            const bf16x8 f = cat44(trread(rp + ks * 4096), trread(rp + ks * 4096 + 512));
            st16(__builtin_bit_cast(u32x4, f), rs, lane * 16, dstb + ((c5 * nks + 2 * kb + ks) * 5 + jj) * 1024);
          }
        }
      }
      if constexpr (s == 0 || s == 10) {
        __builtin_amdgcn_s_setprio(3);
        const u32x4 c0 = __builtin_bit_cast(u32x4, mfma16(sw_[0][1], ONE8, mfma16(sw_[0][0], ONE8, z4)));
        const u32x4 c1 = __builtin_bit_cast(u32x4, mfma16(sw_[1][1], ONE8, mfma16(sw_[1][0], ONE8, z4)));
        const u32x4 cu = l15 == 0 ? c0 : c1;
        if (l15 < 2) {
          const unsigned o = OFF_CS + (((s == 10 ? 16 : 0) + cg) * 10 + rot) * 1024 + 2 * lw * 128;
          st16(u32x4{cu[0], tg, cu[1], tg}, rs, l15 * 128 + g * 32, o);
          st16(u32x4{cu[2], tg, cu[3], tg}, rs, l15 * 128 + g * 32 + 16, o);
        }
        __builtin_amdgcn_s_setprio(0);
      }
      if constexpr (s == 1) {
        __builtin_amdgcn_s_setprio(3);
#pragma unroll
        for (int i = 0; i < 2; ++i) {
          const f32x4 sx = mfma16(ONE8, sx_[i][1], mfma16(ONE8, sx_[i][0], z4));
          const f32x4 sq = mfma16(sx_[i][1], sx_[i][1], mfma16(sx_[i][0], sx_[i][0], z4));
          const int r = l15 & 3;
          const float sxx = r == 0 ? sq[0] : (r == 1 ? sq[1] : (r == 2 ? sq[2] : sq[3]));
          if (q4 == g)
            st16(u32x4{__float_as_uint(sx[0]), tg, __float_as_uint(sxx), tg}, rs, (32 * lw + 16 * i + l15) * 16,
                 OFF_RS + (rt * 10 + rot) * 2048);
        }
        __builtin_amdgcn_s_setprio(0);
      }
      if constexpr (s == 4) {
        if (lt < 12) dma1(b1 + cg * 192, lane * 16, lds0 + B1_OFF);
        if (lw == 1 && lane < 10) dma1(b2 + ct * 160, lane * 16, lds0 + B1_OFF + 256);
      }
      if constexpr (s == 6 || s == 11) {
        if (lt == 0 && (s == 6 || cg < 15)) __hip_atomic_store((GAS u64*)(ws + (s == 6 ? FL_W : FL_Q) + b * 128), tag, RLX_AGENT);
      }
      if constexpr (s == 5) {
#pragma unroll
        for (int k = 0; k < 5; ++k) gr[k] = aload16(ws, ro[k]);
      }
      if constexpr (s == 5 || s == 14) {
#pragma unroll
        for (int k = 0; k < 3; ++k) { if (s == 14) co[k] += 16 * 10240; gc[k] = aload16(ws, co[k]); }
      }
      if constexpr (s == 8) {
        __builtin_amdgcn_s_setprio(3);
#pragma unroll
        for (int k = 0; k < 5; ++k) PIN(gr[k]);
        float sx, sxx;
        if (!sum_pairs<5>(gr, 0xffffffffu, tg, sx, sxx)) regather<5>(rs, ro, 0xffffffffu, tg, sx, sxx);
        sx += __shfl_xor(sx, 32); sxx += __shfl_xor(sxx, 32);
        const float mean = sx * (1.f / 640.f);
        float var = sxx * (1.f / 640.f) - mean * mean;
        var = var < 0.f ? 0.f : var;
        if (lane < 32) *(LAS f32x2*)(smem + ST_OFF + (32 * lw + lane) * 8) = f32x2{mean, rsqrtf(var + 1e-5f)};
        col_sums(0);
        __builtin_amdgcn_s_setprio(0);
      }
      if constexpr (s == 9) {
        poll_flags(ws + fW, true, tag);
#pragma unroll
        for (int k = 0; k < WOPRE; ++k)
#pragma unroll
          for (int j = 0; j < 5; ++j) wpre[k][j] = ld16(rs, lane * 16, wb + k * 5120 + j * 1024);
      }
      if constexpr (s == 16) fl = aload8(ws, fQ);
    };
    stage(IC<0>{}); stage(IC<1>{}); stage(IC<2>{}); stage(IC<3>{}); stage(IC<4>{}); stage(IC<5>{});
    stage(IC<6>{}); stage(IC<7>{}); stage(IC<8>{}); stage(IC<9>{}); stage(IC<10>{}); stage(IC<11>{});
    stage(IC<12>{}); stage(IC<13>{}); stage(IC<14>{}); stage(IC<15>{}); stage(IC<16>{}); stage(IC<17>{});
    STAMP(2);
    PIN(fl);
    if (!__all(fl[0] == tg && fl[1] == (unsigned)(tag >> 32))) poll_flags(ws + fQ, true, tag);
    STAMP(11);
#pragma unroll
    for (int i = 0; i < 2; ++i) {
      const int seg = 2 * lw + i;
      int tb = 4 * qb - 4 + seg; tb = tb < 0 ? 0 : tb;
      dma_kv(ws + OFF_QKV + (size_t)(tb * 240 + 80 + ch) * 1024, lane * 16, lane * 16 + 81920, lds0 + K_OFF + seg * 1024);
    }
    const int l31 = lane & 31, hh = lane >> 5;
    u32x4 qraw[2][2];
#pragma unroll
    for (int a = 0; a < 2; ++a)
#pragma unroll
      for (int ks = 0; ks < 2; ++ks)
        qraw[a][ks] = __builtin_amdgcn_raw_buffer_load_b128(rs, (ks ? 2 : hh) * 1024 + (32 * a + l31) * 16,
                                                            OFF_QKV + ((4 * qb + lw) * 240 + ch) * 1024, SC1);
    col_sums(1);
    {
      const u32x4 one4 = {0x3f803f80u, 0x3f803f80u, 0x3f803f80u, 0x3f803f80u};
      *(LAS u32x4*)(smem + V_OFF + 3 * VP + lt * 16) = one4;
      if (lt < 40) *(LAS u32x4*)(smem + V_OFF + 3 * VP + 4096 + lt * 16) = one4;
      *(LAS u32x4*)(smem + K_OFF + 3 * KP + lt * 16) = u32x4{qb == 0 ? 0xff80u : 0u, 0u, 0u, 0u};
      *(LAS u32x4*)(smem + K_OFF + 3 * KP + 4096 + lt * 16) = u32x4{0u, 0u, 0u, 0u};
    }
    bf16x8 qf[2][2];
#pragma unroll
    for (int a = 0; a < 2; ++a) {
      u32x4 v0 = qraw[a][0], v1 = qraw[a][1];
      if (odd && hh == 0) { v0[0] = 0u; v0[1] = 0u; }
      if (hh) v1 = u32x4{0x3f80u, 0u, 0u, 0u};
      else if (!odd) { v1[2] = 0u; v1[3] = 0u; }
      qf[a][0] = __builtin_bit_cast(bf16x8, v0);
      qf[a][1] = __builtin_bit_cast(bf16x8, v1);
    }
    f32x16 mF, z16;
#pragma unroll
    for (int e = 0; e < 16; ++e) {
      mF[e] = (e & 3) + 8 * (e >> 2) + 4 * hh > l31 ? 0.f : -INFINITY;
      z16[e] = 0.f;
    }
    asm volatile("" : "+v"(mF), "+v"(qf[0][0]), "+v"(qf[0][1]), "+v"(qf[1][0]), "+v"(qf[1][1]));
    WAIT_VM(0);
    WAIT_LGKM0();
    lds_arrive(sync);
    lds_wait(sync, 4);
    __builtin_amdgcn_s_setprio(2);
    STAMP(5);
    unsigned kp = lds0 + K_OFF + hh * KP + (64 * lw + l31) * 16;
    const bool ones = (g & 1) && (p4 >> 1);
    unsigned vp = lds0 + V_OFF + (2 * (g & 1) + (p4 >> 1)) * VP + ((ones ? 0 : 64 * lw) + 4 * hh + q4) * 16 + (p4 & 1) * 8;
    unsigned ov = l31 * 16 + hh * 8;
    bf16x8 q0 = qf[0][0], q1 = qf[0][1];
#define LDK(T, ks) (*(LAS bf16x8*)(size_t)(kp + (T) * 512 + (ks) * 2 * KP))
#define MF32(a, b, c) __builtin_amdgcn_mfma_f32_32x32x16_bf16(a, b, c, 0, 0, 0)
#define EX2(s_, e) pk2(__builtin_amdgcn_exp2f((s_)[e]), __builtin_amdgcn_exp2f((s_)[(e) + 1]))
    auto tile_max = [&](int T) {
      f32x16 sp = MF32(LDK(T, 1), q1, MF32(LDK(T, 0), q0, z16));
      float m0 = -INFINITY, m1 = -INFINITY;
#pragma unroll
      for (int e = 0; e < 16; ++e) {
        const bool off = T == 0 ? mF[e] != 0.f : (T == 8 ? mF[e] == 0.f : false);
        sp[e] = off ? -INFINITY : sp[e];
      }
#pragma unroll
      for (int e = 0; e < 16; e += 4) { m0 = MAX3(m0, sp[e], sp[e + 1]); m1 = MAX3(m1, sp[e + 2], sp[e + 3]); }
      m0 = fmaxf(m0, m1);
      return fmaxf(m0, __shfl_xor(m0, 32));
    };
#pragma unroll 1
    for (int un = 0; un < 2; ++un) {
      float mx = tile_max(8);
      f32x16 oa;
#pragma unroll 1
      for (int pass = 0; pass < 2; ++pass) {
      f32x16 ca;
#pragma unroll
      for (int e = 0; e < 16; ++e) ca[e] = -mx;
      oa = z16;
      {
        bf16x8 kc[2][2], vf[2][2];
        f32x16 sc[2];
        u32x4 pa[2] = {{0u, 0u, 0u, 0u}, {0u, 0u, 0u, 0u}};
        kc[0][0] = LDK(0, 0); kc[0][1] = LDK(0, 1); kc[1][0] = LDK(1, 0); kc[1][1] = LDK(1, 1);
        sc[0] = MF32(kc[0][1], q1, MF32(kc[0][0], q0, ca + mF));
        sc[1] = z16;
        asm volatile("" :: "v"(sc[0]));
        SB();
#pragma unroll
        for (int T = 0; T < 10; ++T) {
          const int c = T & 1, n = c ^ 1;
          if (T < 9) {
#pragma unroll
            for (int ks = 0; ks < 2; ++ks) vf[c][ks] = cat44(trread(vp + T * 512 + ks * 256), trread(vp + T * 512 + ks * 256 + 128));
          }
          if (T < 8) sc[n] = MF32(kc[n][1], q1, MF32(kc[n][0], q0, ca));
          if (T < 7) { kc[c][0] = LDK(T + 2, 0); kc[c][1] = LDK(T + 2, 1); }
          if (T > 0) oa = MF32(vf[n][1], __builtin_bit_cast(bf16x8, pa[1]), MF32(vf[n][0], __builtin_bit_cast(bf16x8, pa[0]), oa));
          if (T == 8) {
#pragma unroll
            for (int e = 0; e < 16; ++e) sc[c][e] = mF[e] == 0.f ? -INFINITY : sc[c][e];
          }
          if (T < 9) {
#pragma unroll
            for (int e = 0; e < 4; ++e) { pa[0][e] = EX2(sc[c], 2 * e); pa[1][e] = EX2(sc[c], 8 + 2 * e); }
          }
          SGB(0x100, 6);
          if (T == 8) SGB(0x2, 16);
          SGB(0x8, 1); SGB(0x400, 4); SGB(0x2, 1);
#pragma unroll
          for (int k = 0; k < 3; ++k) { SGB(0x8, 1); SGB(0x400, 2); SGB(0x2, 1); SGB(0x400, 2); SGB(0x2, 1); }
          SGB(0x2, 1);
          asm volatile("" :: "v"(sc[n]), "v"(oa), "v"(pa[0]), "v"(pa[1]));
          SB();
        }
      }
      if (pass || __all(oa[12] < 1.8446744e19f)) break;
#pragma unroll 1
      for (int T = 0; T < 8; ++T) mx = fmaxf(mx, tile_max(T));
      }
      const float il = __builtin_amdgcn_rcpf(oa[12]);
#pragma unroll
      for (int pl = 0; pl < 3; ++pl) {
        const bool ok = pl == 1 || (pl == 0 ? !(odd && hh == 0) : (odd || hh == 0));
        if (ok) {
          const u32x2 pkd = {pk2(oa[4 * pl] * il, oa[4 * pl + 1] * il), pk2(oa[4 * pl + 2] * il, oa[4 * pl + 3] * il)};
          __builtin_amdgcn_raw_buffer_store_b64(pkd, rs, pl * 1024 + ov, OFF_O + ((4 * qb + lw) * 80 + ch) * 1024, SC1);
        }
      }
      kp += 512; vp += ones ? 0 : 512; ov += 512;
      q0 = qf[1][0]; q1 = qf[1][1];
    }
    __builtin_amdgcn_s_setprio(0);
    lds_arrive(sync + 4);
    STAMP(6);
    WAIT_VM(0);
    if (lane == 0) __hip_atomic_store((GAS u64*)(ws + FL_O + (b * 4 + lw) * 128), tag, RLX_AGENT);
#pragma unroll
    for (int k = WOPRE; k < 5; ++k)
#pragma unroll
      for (int j = 0; j < 5; ++j) wpre[k][j] = ld16(rs, lane * 16, wb + k * 5120 + j * 1024);
#pragma unroll
    for (int j = 0; j < 2; ++j) xv[j] = *(const u32x2*)(x + orow + (3 + j) * 32);
    const unsigned ab = OFF_O + (mt * 80 + 20 * lw) * 1024;
    {
      const int hd = 8 * lw + (lane & 7);
      poll_flags(ws + FL_O + ((((((mt >> 2) << 2) | (hd & 3)) << 3) | (hd >> 2)) * 4 + (mt & 3)) * 128, lane < 8, tag);
    }
    STAMP(7);
    bf16x8 af[4][5];
#pragma unroll
    for (int i = 0; i < 4; ++i)
#pragma unroll
      for (int k = 0; k < 5; ++k) af[i][k] = ld16(rs, va + ((lw + 1 + i) & 3) * 256, ab + k * 4096);
    SB();
    lds_wait(sync + 4, 4);
    lds_wait(sync + 6, 4);
    f32x4 own[2];
#pragma unroll
    for (int i = 0; i < 4; ++i) {
      const int mi = (lw + 1 + i) & 3;
      f32x4 a5[5] = {z4, z4, z4, z4, z4};
#pragma unroll
      for (int k = 0; k < 5; ++k)
#pragma unroll
        for (int j = 0; j < 5; ++j) a5[j] = mfma16(wpre[k][j], af[i][k], a5[j]);
#pragma unroll
      for (int j = 0; j < 3; ++j) *(LAS f32x4*)(smem + REDS1_OFF + ((mi * 4 + lw) * 3 + j) * 1024 + lane * 16) = a5[j];
      if (i < 3) {
#pragma unroll
        for (int j = 0; j < 2; ++j) *(LAS f32x4*)(smem + REDS2_OFF + ((mi * 3 + 2 - i) * 2 + j) * 1024 + lane * 16) = a5[3 + j];
      } else { own[0] = a5[3]; own[1] = a5[4]; }
    }
    STAMP(8);
    __syncthreads();
    STAMP(9);
#pragma unroll
    for (int j = 0; j < 2; ++j) {
      f32x4 sv = own[j];
#pragma unroll
      for (int k = 0; k < 3; ++k) sv += *(LAS f32x4*)(smem + REDS2_OFF + ((lw * 3 + k) * 2 + j) * 1024 + lane * 16);
      const f32x4 gt = *(LAS f32x4*)(smem + GX_OFF + (lw * 2 + j) * 1024 + lane * 16);
      *(u32x2*)(out + orow + (3 + j) * 32) = fin4(xv[j], sv, gt);
    }

  } else {
    if (idx * 4 + w < CODE_KB) {
      const u32x4 t0 = *(const u32x4*)(pc0 + (idx * 4 + w) * 1024 + lane * 16);
      asm volatile("" :: "v"(t0));
    }
    {
      const int mw = w & 1, nw = w >> 1;
      unsigned xa[2];
      xa[0] = lds0 + (64 * mw + l15) * 128 + (xsw << 4);
      xa[1] = lds0 + (64 * mw + l15) * 128 + ((xsw ^ 4) << 4);
      const unsigned wl = lds0 + 16384 + (8 * g + q4) * 128 + p4 * 8;
      const unsigned vo = (g >> 1) * 1024 + (16 * (g & 1) + l15) * 16;
      float sh[4], sc[4];
      BAR();
      STAMP(1);
      mfma_part<4, 0>(xa, wl, 4 * nw, sigl, lds0, sh, sc, 64 * mw + l15, cg < 5 ? 0.22360679774997896f * 1.4426950408889634f : 1.f, rs, vo,
                      cg < 15 ? OFF_QKV + ((2 * rt + mw) * 240 + 16 * cg + 8 * nw) * 1024 : 0x80000000u, 4 * nw, g, ws);
      STAMP(14);
      mfma_part<3, 10>(xa, wl, lu1 + 3 * nw, sigl, lds0, sh, sc, 0, 1.f, rs, vo,
                       OFF_H + ((2 * rt + mw) * 192 + 12 * cg + 6 * nw) * 1024, 3 * nw, g, ws);
    }
    STAMP(3);
    WAIT_VM(0);
    if (lds_arrive(sync + 2) == 3 && lane == 0) __hip_atomic_store((GAS u64*)(ws + FL_H + b * 128), tag, RLX_AGENT);
    if (b == 0 && w == 0) {
#pragma unroll 1
      for (int spins = 0; spins < (1 << 20); ++spins) {
        bool ok = true;
#pragma unroll
        for (int i = 0; i < 4; ++i) ok = ok && __hip_atomic_load((const GAS u64*)(ws + FL_W + (lane + 64 * i) * 128), RLX_AGENT) == tag;
        if (__all(ok)) break;
        __builtin_amdgcn_s_sleep(1);
      }
      if (lane == 0) __hip_atomic_store((GAS u64*)(ws + OFF_FL), tag, RLX_AGENT);
    }
    STAMP(4);
#pragma unroll
    for (int k = 0; k < 4; ++k)
#pragma unroll
      for (int j = 0; j < 5; ++j) wpre[k][j] = ld16(rs, lane * 16, wb + k * 5120 + j * 1024);
    const unsigned wl_ = lds0 + slab_of(w);
    lds_wait(sync + 2, 4);
    dma5_sc1(ws + wb + 4 * 5120, lane * 16, wl_);
    dma5_sc1(ws + wb + 5 * 5120, lane * 16, wl_ + 5120);
    const unsigned ab = OFF_H + (mt * 192 + 48 * w) * 1024;
    poll_flags(ws + FL_H + wg_of(mt >> 1, 4 * w + (lane & 3)) * 128, lane < 4, tag);
    STAMP(7);
    bf16x8 af[4][4];
#pragma unroll
    for (int k = 0; k < 4; ++k)
#pragma unroll
      for (int i = 0; i < 4; ++i) af[k][i] = ld16(rs, va + i * 256, ab + k * 4096);
    dma5_sc1(ws + ab + 4 * 4096, lane * 16, wl_ + 10240);
    dma5_sc1(ws + ab + 4 * 4096 + 3072, lane * 16, wl_ + 10240 + 3072);
    {
      SB();
      WAIT_VM(0);
    }
    STAMP(12);
#pragma unroll
    for (int i = 0; i < 4; ++i)
#pragma unroll
      for (int j = 0; j < 5; ++j) acc[i][j] = f32x4{0.f, 0.f, 0.f, 0.f};
#pragma unroll
    for (int k = 0; k < 12; ++k) {
      const int cs = k & 3;
      if (k == W2HOLD) {
        unsigned tv, ts;
        asm volatile(".Lp%=:\n\tds_read_b32 %0, %2\n\ts_waitcnt lgkmcnt(0)\n\tv_readfirstlane_b32 %1, %0\n\t"
                     "s_cmp_ge_u32 %1, 4\n\ts_cbranch_scc1 .Le%=\n\ts_sleep 2\n\ts_branch .Lp%=\n.Le%=:"
                     : "=&v"(tv), "=&s"(ts) : "v"(lds0 + SYNC_OFF + 16) : "memory", "scc");
      }
#pragma unroll
      for (int i = 0; i < 4; ++i)
#pragma unroll
        for (int j = 0; j < 5; ++j) acc[i][j] = mfma16(wpre[cs][j], af[cs][i], acc[i][j]);
      SB();
      if (k < 2) {
#pragma unroll
        for (int j = 0; j < 5; ++j) wpre[cs][j] = *(LAS bf16x8*)(size_t)(wl_ + k * 5120 + j * 1024 + lane * 16);
#pragma unroll
        for (int i = 0; i < 4; ++i) af[cs][i] = *(LAS bf16x8*)(size_t)(wl_ + 10240 + k * 4096 + va + i * 256);
      } else if (k < 8) {
#pragma unroll
        for (int i = 0; i < 4; ++i) af[cs][i] = ld16(rs, va + i * 256, ab + (k + 4) * 4096);
#pragma unroll
        for (int j = 0; j < 5; ++j) wpre[cs][j] = ld16(rs, lane * 16, wb + (k + 4) * 5120 + j * 1024);
      }
      SB();
    }
    lds_arrive(sync + 6);
    lds_wait(sync + 6, 4);
    lds_wait(sync + 4, 4);
    STAMP(8);
    red_write(smem + REDG_OFF, w, lane, acc);
    u32x2 bb[5];
#pragma unroll
    for (int j = 0; j < 5; ++j) {
      if (j < 3) xv[j] = *(const u32x2*)(x + orow + j * 32);
      bb[j] = *(LAS u32x2*)(smem + B1_OFF + 256 + (16 * j + 4 * g) * 2);
    }
    WAIT_LGKM0();
    lds_arrive(sync + 3);
    lds_wait(sync + 3, 4);
    f32x4 gt[5];
    red_sum(smem + REDG_OFF, w, lane, acc, gt);
#pragma unroll
    for (int j = 0; j < 5; ++j) {
      const float bv[4] = {bflo(bb[j][0]), bfhi(bb[j][0]), bflo(bb[j][1]), bfhi(bb[j][1])};
#pragma unroll
      for (int e = 0; e < 4; ++e) {
        const float t = rbf(rbf(gt[j][e]) + bv[e]);
        gt[j][e] = rbf(t * __builtin_amdgcn_rcpf(1.f + __expf(-t)));
      }
      if (j >= 3) *(LAS f32x4*)(smem + GX_OFF + (w * 2 + j - 3) * 1024 + lane * 16) = gt[j];
    }
    __syncthreads();
    STAMP(9);
#pragma unroll
    for (int j = 0; j < 3; ++j) {
      f32x4 sv = *(LAS f32x4*)(smem + REDS1_OFF + ((w * 4) * 3 + j) * 1024 + lane * 16);
#pragma unroll
      for (int k = 1; k < 4; ++k) sv += *(LAS f32x4*)(smem + REDS1_OFF + ((w * 4 + k) * 3 + j) * 1024 + lane * 16);
      *(u32x2*)(out + orow + j * 32) = fin4(xv[j], sv, gt[j]);
    }
  STAMP(10);
    __builtin_amdgcn_endpgm();
  }
  STAMP(10);
}

extern "C" void kernel_launch(void* const* d_in, const int* in_sizes, int n_in, void* d_out, int out_size, void* d_ws,
                              size_t ws_size, hipStream_t stream) {
  (void)hipFuncSetAttribute((const void*)k_all, hipFuncAttributeMaxDynamicSharedMemorySize, LDS_ALL);
  k_all<<<256, 512, LDS_ALL, stream>>>((const char*)d_in[0], (const char*)d_in[1], (const char*)d_in[4], (const char*)d_in[5],
                                       (const char*)d_in[6], (const char*)d_in[2], (const char*)d_in[7], (char*)d_ws, (char*)d_out);
}
```

```cpp
#include <hip/hip_runtime.h>
#include <stdint.h>

#define LAS __attribute__((address_space(3)))
#define GAS __attribute__((address_space(1)))
#define CAS __attribute__((address_space(4)))
typedef short bf16x8 __attribute__((ext_vector_type(8)));
typedef short s16x4 __attribute__((ext_vector_type(4)));
typedef float f32x4 __attribute__((ext_vector_type(4)));
typedef unsigned u32x4 __attribute__((ext_vector_type(4)));
typedef unsigned u32x2 __attribute__((ext_vector_type(2)));
typedef __bf16 bf16x2_t __attribute__((ext_vector_type(2)));
typedef float f32x2 __attribute__((ext_vector_type(2)));
typedef float f32x16 __attribute__((ext_vector_type(16)));
typedef unsigned long long u64;

constexpr unsigned OFF_FL = 0;
constexpr unsigned FL_W = 1024, FL_Q = FL_W + 32768, FL_H = FL_Q + 32768, FL_O = FL_H + 32768;
constexpr unsigned OFF_RS = 262144;
constexpr unsigned OFF_CS = OFF_RS + 16 * 10 * 2048;
constexpr unsigned OFF_QKV = OFF_CS + 2 * 16 * 10 * 1024;
constexpr unsigned OFF_H = OFF_QKV + 32 * 240 * 1024;
constexpr unsigned OFF_O = OFF_H + 32 * 192 * 1024;
constexpr unsigned OFF_W2F = OFF_O + 32 * 80 * 1024;
constexpr unsigned OFF_WOF = OFF_W2F + 8 * 48 * 5120;
constexpr unsigned WS_USED = OFF_WOF + 8 * 20 * 5120;

__device__ __forceinline__ f32x4 mfma16(bf16x8 a, bf16x8 b, f32x4 c) {
  return __builtin_amdgcn_mfma_f32_16x16x32_bf16(a, b, c, 0, 0, 0);
}
__device__ __forceinline__ unsigned pk2(float a, float b) {
  f32x2 v = {a, b};
  bf16x2_t r = __builtin_convertvector(v, bf16x2_t);
  return __builtin_bit_cast(unsigned, r);
}
__device__ __forceinline__ float rbf(float a) { return __builtin_bit_cast(float, pk2(0.f, a) & 0xffff0000u); }
__device__ __forceinline__ float bflo(unsigned u) { return __builtin_bit_cast(float, u << 16); }
__device__ __forceinline__ float bfhi(unsigned u) { return __builtin_bit_cast(float, u & 0xffff0000u); }
__device__ __forceinline__ u32x2 fin4(u32x2 xv, f32x4 sv, f32x4 gt) {
  return u32x2{pk2(bflo(xv[0]) + rbf(rbf(sv[0]) * gt[0]), bfhi(xv[0]) + rbf(rbf(sv[1]) * gt[1])),
               pk2(bflo(xv[1]) + rbf(rbf(sv[2]) * gt[2]), bfhi(xv[1]) + rbf(rbf(sv[3]) * gt[3]))};
}
__device__ __forceinline__ s16x4 trread(unsigned a) {
  return __builtin_amdgcn_ds_read_tr16_b64_v4i16((LAS s16x4*)(size_t)a);
}
__device__ __forceinline__ bf16x8 cat44(s16x4 a, s16x4 b) {
  return __builtin_shufflevector(a, b, 0, 1, 2, 3, 4, 5, 6, 7);
}
#define WAIT_VM(n) asm volatile("s_waitcnt vmcnt(" #n ")" ::: "memory")
#define WAIT_LGKM0() __builtin_amdgcn_s_waitcnt(0xC07F)
#define BAR() __builtin_amdgcn_s_barrier()
#define SB() __builtin_amdgcn_sched_barrier(0)
#define SGB(m, n) __builtin_amdgcn_sched_group_barrier(m, n, 0)
#define MAX3(a, b, c) __builtin_fmaxf(__builtin_fmaxf(a, b), c)
#define RLX_AGENT __ATOMIC_RELAXED, __HIP_MEMORY_SCOPE_AGENT
constexpr int SC1 = 16;
constexpr int CODE_KB = 39;
template <int N> struct IC { static constexpr int value = N; };

#define STAMP(i)
#define CLK(v)

__device__ __forceinline__ void dma1(const char* sbase, unsigned voff, unsigned lds) {
  unsigned keep;
  asm volatile("s_mov_b32 %0, m0\n\ts_mov_b32 m0, %3\n\ts_nop 0\n\tglobal_load_lds_dwordx4 %1, %2\n\ts_mov_b32 m0, %0"
               : "=&s"(keep) : "v"(voff), "s"(sbase), "s"(lds) : "memory");
}
__device__ __forceinline__ void dma_stage(const char* sa, unsigned va0, unsigned va1, unsigned va2, unsigned va3, unsigned la,
                                          const char* sb, unsigned vb0, unsigned vb1, unsigned lb) {
  unsigned keep;
  asm volatile("s_mov_b32 %0, m0\n\ts_mov_b32 m0, %10\n\ts_nop 0\n\tglobal_load_lds_dwordx4 %8, %7\n\t"
               "s_add_u32 m0, m0, 1024\n\ts_nop 0\n\tglobal_load_lds_dwordx4 %9, %7\n\t"
               "s_add_u32 m0, m0, 7040\n\ts_nop 0\n\tglobal_load_lds_dwordx4 %8, %7 offset:128\n\t"
               "s_add_u32 m0, m0, 1024\n\ts_nop 0\n\tglobal_load_lds_dwordx4 %9, %7 offset:128\n\t"
               "s_mov_b32 m0, %6\n\ts_nop 0\n\tglobal_load_lds_dwordx4 %2, %1\n\t"
               "s_add_u32 m0, m0, 1024\n\ts_nop 0\n\tglobal_load_lds_dwordx4 %3, %1\n\t"
               "s_add_u32 m0, m0, 1024\n\ts_nop 0\n\tglobal_load_lds_dwordx4 %4, %1\n\t"
               "s_add_u32 m0, m0, 1024\n\ts_nop 0\n\tglobal_load_lds_dwordx4 %5, %1\n\ts_mov_b32 m0, %0"
               : "=&s"(keep) : "s"(sa), "v"(va0), "v"(va1), "v"(va2), "v"(va3), "s"(la), "s"(sb), "v"(vb0), "v"(vb1), "s"(lb)
               : "memory", "scc");
}
__device__ __forceinline__ void dma_kv(const char* sb, unsigned vk, unsigned vv, unsigned l) {
  unsigned keep;
  asm volatile("s_mov_b32 %0, m0\n\ts_mov_b32 m0, %4\n\ts_nop 0\n\tglobal_load_lds_dwordx4 %2, %1 sc1\n\t"
               "s_add_u32 m0, m0, 7168\n\ts_nop 0\n\tglobal_load_lds_dwordx4 %2, %1 offset:1024 sc1\n\t"
               "s_add_u32 m0, m0, 7168\n\ts_nop 0\n\tglobal_load_lds_dwordx4 %2, %1 offset:2048 sc1\n\t"
               "s_add_u32 m0, m0, 18432\n\ts_nop 0\n\tglobal_load_lds_dwordx4 %3, %1 sc1\n\t"
               "s_add_u32 m0, m0, 7232\n\ts_nop 0\n\tglobal_load_lds_dwordx4 %3, %1 offset:1024 sc1\n\t"
               "s_add_u32 m0, m0, 7232\n\ts_nop 0\n\tglobal_load_lds_dwordx4 %3, %1 offset:2048 sc1\n\t"
               "s_mov_b32 m0, %0"
               : "=&s"(keep) : "s"(sb), "v"(vk), "v"(vv), "s"(l) : "memory", "scc");
}
__device__ __forceinline__ void dma5_sc1(const char* sb, unsigned v, unsigned l) {
  unsigned keep;
  asm volatile("s_mov_b32 %0, m0\n\ts_mov_b32 m0, %3\n\ts_nop 0\n\tglobal_load_lds_dwordx4 %2, %1 sc1\n\t"
               "global_load_lds_dwordx4 %2, %1 offset:1024 sc1\n\tglobal_load_lds_dwordx4 %2, %1 offset:2048 sc1\n\t"
               "global_load_lds_dwordx4 %2, %1 offset:3072 sc1\n\ts_add_u32 m0, m0, 1024\n\ts_nop 0\n\t"
               "global_load_lds_dwordx4 %4, %1 offset:3072 sc1\n\ts_mov_b32 m0, %0"
               : "=&s"(keep) : "s"(sb), "v"(v), "s"(l), "v"(v + 1024) : "memory", "scc");
}
__device__ __forceinline__ u32x2 aload8(const char* sbase, unsigned voff) {
  u32x2 r;
  asm volatile("global_load_dwordx2 %0, %1, %2 sc1" : "=v"(r) : "v"(voff), "s"(sbase) : "memory");
  return r;
}
__device__ __forceinline__ u32x4 aload16(const char* sbase, unsigned voff) {
  u32x4 r;
  asm volatile("global_load_dwordx4 %0, %1, %2 sc1" : "=v"(r) : "v"(voff), "s"(sbase) : "memory");
  return r;
}
#define PIN(x) asm volatile("" : "+v"(x))
template <int N> __device__ __forceinline__ void wait_vm() { asm volatile("s_waitcnt vmcnt(%0)" :: "i"(N) : "memory"); }
__device__ __forceinline__ void poll_flags(const char* flag, bool need, u64 tag) {
#pragma unroll 1
  for (int spins = 0; spins < (1 << 20); ++spins) {
    const u64 v = need ? __hip_atomic_load((const GAS u64*)flag, RLX_AGENT) : tag;
    if (__all(v == tag)) break;
    __builtin_amdgcn_s_sleep(1);
  }
  __builtin_amdgcn_fence(__ATOMIC_ACQUIRE, "wavefront");
}
__device__ __forceinline__ unsigned lds_arrive(LAS unsigned* c) {
  unsigned r = 0;
  if ((threadIdx.x & 63) == 0) r = __hip_atomic_fetch_add(c, 1u, __ATOMIC_RELAXED, __HIP_MEMORY_SCOPE_WORKGROUP);
  return __builtin_amdgcn_readfirstlane(r);
}
__device__ __forceinline__ void lds_wait_sleep(LAS unsigned* c, unsigned n) {
#pragma unroll 1
  for (int spins = 0; spins < (1 << 22); ++spins) {
    if (__builtin_amdgcn_readfirstlane(__hip_atomic_load(c, __ATOMIC_RELAXED, __HIP_MEMORY_SCOPE_WORKGROUP)) >= n) break;
    __builtin_amdgcn_s_sleep(2);
  }
  __builtin_amdgcn_fence(__ATOMIC_ACQUIRE, "wavefront");
}
__device__ __forceinline__ void lds_wait(LAS unsigned* c, unsigned n) {
#pragma unroll 1
  for (int spins = 0; spins < (1 << 22); ++spins) {
    if (__builtin_amdgcn_readfirstlane(__hip_atomic_load(c, __ATOMIC_RELAXED, __HIP_MEMORY_SCOPE_WORKGROUP)) >= n) break;
  }
  __builtin_amdgcn_fence(__ATOMIC_ACQUIRE, "wavefront");
}

#define WOPRE 4
#define W2HOLD 10
constexpr int STG = 32768, RT_OFF = 4 * STG, LDS_ALL = 163840;
constexpr int B1_OFF = 159744;
constexpr int ST_OFF = 160768;
constexpr int CS_OFF = ST_OFF + 1024, SYNC_OFF = ST_OFF + 2048;
constexpr int KP = 8192, VP = 8192 + 64;
constexpr int K_OFF = 0, V_OFF = K_OFF + 4 * KP;
__device__ __forceinline__ constexpr int slab_of(int w) { return 65536 + w * 20480; }
constexpr int REDG_OFF = 0, GX_OFF = 61440, REDS1_OFF = GX_OFF + 8192, REDS2_OFF = REDS1_OFF + 49152;

__device__ __forceinline__ void st16(u32x4 v, __amdgpu_buffer_rsrc_t rs, unsigned voff, unsigned soff) {
  __builtin_amdgcn_raw_buffer_store_b128(v, rs, voff + soff, 0, SC1);
}
__device__ __forceinline__ bf16x8 ld16(__amdgpu_buffer_rsrc_t rs, unsigned voff, unsigned soff) {
  return __builtin_bit_cast(bf16x8, __builtin_amdgcn_raw_buffer_load_b128(rs, voff, soff, SC1));
}
__device__ __forceinline__ int wg_of(int rt, int cg) { return (((cg & 3) * 8 + (rt & 7)) << 3) | ((cg >> 2) << 1) | (rt >> 3); }
template <int N>
__device__ __forceinline__ bool sum_pairs(const u32x4 (&v)[N], unsigned last_on, unsigned tg, float& a, float& b) {
  bool ok = true;
  a = 0.f; b = 0.f;
#pragma unroll
  for (int k = 0; k < N; ++k) {
    ok = ok && v[k][1] == tg && v[k][3] == tg;
    const unsigned m = k == N - 1 ? last_on : 0xffffffffu;
    a += __uint_as_float(v[k][0] & m);
    b += __uint_as_float(v[k][2] & m);
  }
  return __all(ok);
}
template <int N>
__device__ __forceinline__ void regather(__amdgpu_buffer_rsrc_t rs, const unsigned (&vo)[N], unsigned last_on, unsigned tg, float& a, float& b) {
#pragma unroll 1
  for (int spins = 0; spins < (1 << 20); ++spins) {
    u32x4 v[N];
#pragma unroll
    for (int k = 0; k < N; ++k) v[k] = __builtin_amdgcn_raw_buffer_load_b128(rs, vo[k], 0, SC1);
    if (sum_pairs<N>(v, last_on, tg, a, b)) break;
    __builtin_amdgcn_s_sleep(2);
  }
}

#define STG_STAMP(s_)

#define LDX(set, i, ks) xf[set][i] = *(LAS bf16x8*)(size_t)(xa[ks] + (i) * 2048)
#define LDW(set, j, ks) wf[set][j] = cat44(trread(wa[j] + (ks) * 4096), trread(wa[j] + (ks) * 4096 + 512))
#define HALF(set, ks) do { \
    _Pragma("unroll") for (int j = 0; j < NT; ++j) { \
      if (j == 0) { _Pragma("unroll") for (int i = 0; i < 4; ++i) LDX((set) ^ 1, i, ks); } \
      else if (2 * j - 2 < NT) { LDW((set) ^ 1, 2 * j - 2, ks); if (2 * j - 1 < NT) LDW((set) ^ 1, 2 * j - 1, ks); } \
      _Pragma("unroll") for (int i = 0; i < 4; ++i) acc[i][j] = mfma16(wf[set][j], xf[set][i], acc[i][j]); \
      SGB(0x8, 1); SGB(0x100, 1); SGB(0x8, 1); SGB(0x100, 1); SGB(0x8, 1); SGB(0x100, 1); SGB(0x8, 1); SGB(0x100, 1); \
      asm volatile("" :: "v"(acc[0][j]), "v"(acc[1][j]), "v"(acc[2][j]), "v"(acc[3][j])); \
      SB(); \
    } } while (0)
template <int NT, int S0>
__device__ __forceinline__ void mfma_part(unsigned (&xa)[2], unsigned wl, int tl0, int sigl, unsigned lds0, float (&sh)[4], float (&sc)[4],
                                          int row0, float qs, __amdgpu_buffer_rsrc_t rs, unsigned vo, unsigned so, int cs0, int g, char* ws) {
  constexpr bool ISH = S0 == 10;
  unsigned wa[NT];
  f32x4 acc[4][NT];
#pragma unroll
  for (int j = 0; j < NT; ++j) {
    const int tl = tl0 + j;
    wa[j] = wl + (S0 & 3) * STG + (tl >> 2) * 8192 + (((tl & 3) ^ sigl) << 5);
#pragma unroll
    for (int i = 0; i < 4; ++i) acc[i][j] = f32x4{0.f, 0.f, 0.f, 0.f};
  }
  bf16x8 xf[2][4], wf[2][NT];
#pragma unroll
  for (int i = 0; i < 4; ++i) LDX(0, i, 0);
#pragma unroll
  for (int j = 0; j < NT; ++j) LDW(0, j, 0);
  WAIT_LGKM0();
  int buf = S0 & 3;
#pragma unroll 1
  for (int t = 0; t < 10; ++t) {
    HALF(0, 1);
    {
      const int d = buf == 3 ? -3 * STG : STG;
      buf = (buf + 1) & 3;
      xa[0] += d; xa[1] += d;
#pragma unroll
      for (int j = 0; j < NT; ++j) wa[j] += d;
    }
    WAIT_LGKM0();
    if (ISH) {
      asm volatile("s_cmp_lg_u32 %0, 1\n\ts_cbranch_scc1 .Lk%=\n\ts_waitcnt vmcnt(0)\n.Lk%=:\n\t"
                   "s_cmp_ge_u32 %0, 8\n\ts_cbranch_scc1 .Lc%=\n\ts_barrier\n.Lc%=:" :: "s"(t) : "memory", "scc");
    } else BAR();
    STG_STAMP(S0 + t);
    SB();
    HALF(1, 0);
    WAIT_LGKM0();
  }
  STAMP(ISH ? 2 : 13);
  lds_wait((LAS unsigned*)(size_t)(lds0 + SYNC_OFF + (ISH ? 32 : 28)), 4);
  if (!ISH) {
#pragma unroll
    for (int i = 0; i < 4; ++i) {
      const f32x2 st = *(LAS f32x2*)(size_t)(lds0 + ST_OFF + (row0 + 16 * i) * 8);
      sc[i] = st[1]; sh[i] = -st[0] * st[1];
    }
  }
#pragma unroll
  for (int j = 0; j < NT; ++j) {
    const f32x4 c4 = *(LAS f32x4*)(size_t)(lds0 + CS_OFF + (ISH ? 512 : 0) + ((cs0 + j) * 16 + 4 * g) * 4);
    f32x4 bv = {0.f, 0.f, 0.f, 0.f};
    if (ISH) {
      const u32x2 bb = *(LAS u32x2*)(size_t)(lds0 + B1_OFF + ((cs0 + j) * 16 + 4 * g) * 2);
      bv = f32x4{bflo(bb[0]), bfhi(bb[0]), bflo(bb[1]), bfhi(bb[1])};
    }
#pragma unroll
    for (int pp = 0; pp < 2; ++pp) {
      float v[2][4];
#pragma unroll
      for (int i = 0; i < 2; ++i)
#pragma unroll
        for (int r = 0; r < 4; ++r) {
          float t = __builtin_fmaf(sc[2 * pp + i], acc[2 * pp + i][j][r], __builtin_fmaf(sh[2 * pp + i], c4[r], bv[r]));
          if (ISH) { t = t > 0.f ? t : 0.f; t = t * t; } else t *= qs;
          v[i][r] = t;
        }
      const unsigned x0 = pk2(v[0][0], v[0][1]), x1 = pk2(v[0][2], v[0][3]);
      const unsigned y0 = pk2(v[1][0], v[1][1]), y1 = pk2(v[1][2], v[1][3]);
      const auto s0 = __builtin_amdgcn_permlane16_swap(x0, y0, false, false);
      const auto s1 = __builtin_amdgcn_permlane16_swap(x1, y1, false, false);
      const u32x4 o = {s0[0], s1[0], s0[1], s1[1]};
      st16(o, rs, vo + pp * 512, so + 2 * j * 1024);
    }
  }
}

__global__ __launch_bounds__(512, 2) void k_all(const char* __restrict__ x, const char* __restrict__ wqkv, const char* __restrict__ w1,
                                                const char* __restrict__ b1, const char* __restrict__ w2, const char* __restrict__ wo,
                                                const char* __restrict__ b2, char* ws, char* __restrict__ out) {
  extern __shared__ __attribute__((aligned(16))) char smem_[];
  LAS char* smem = (LAS char*)smem_;
  const int tid = threadIdx.x, lane = tid & 63, w = __builtin_amdgcn_readfirstlane(tid >> 6);
  const int b = blockIdx.x, xcd = b & 7, idx = b >> 3;
  const unsigned lds0 = (unsigned)(size_t)smem;
  asm volatile("" :: "s"(x), "s"(wqkv), "s"(w1), "s"(b1), "s"(w2), "s"(wo), "s"(ws));
  u64 pc_;
  asm volatile("s_getpc_b64 %0" : "=s"(pc_));
  const int rt = (xcd & 1) * 8 + (idx & 7), cg = (xcd >> 1) * 4 + (idx >> 3);
  const int rot = (rt + 3 * cg) % 10;
  const int lw = w - 4, lr = lane >> 3, ls = lane & 7;
  const int bk = 16 * lw + lr, sw0 = (ls ^ (bk & 2)) << 4, sw1 = sw0 ^ 64;
  const unsigned vq0 = bk * 3840 + (cg < 15 ? cg : 14) * 256 + sw0, vq1 = (bk + 8) * 3840 + (cg < 15 ? cg : 14) * 256 + sw1;
  const unsigned vA = (rt * 128 + 32 * lw + lr) * 1280 + ((ls ^ (lr >> 1)) << 4);
  const unsigned vA1 = (vA + 10240) ^ 64u, vA2 = vA + 20480, vA3 = (vA + 30720) ^ 64u;
  const int rkb = b / 5, rnb = b - 5 * rkb;
  if (w >= 4) {
    dma_stage(x + rot * 128, vA, vA1, vA2, vA3, lds0 + lw * 4096, wqkv + (size_t)rot * (64 * 3840), vq0, vq1, lds0 + 16384 + lw * 2048);
    if (b < 170) {
      const char* Wr = rkb < 24 ? w2 + (size_t)rkb * 64 * 1280 : wo + (size_t)(rkb - 24) * 64 * 1280;
      const unsigned r0 = bk * 1280 + rnb * 256 + sw0, r1 = (bk + 8) * 1280 + rnb * 256 + sw1;
      dma1(Wr, r0, lds0 + RT_OFF + lw * 2048);
      dma1(Wr, r1, lds0 + RT_OFF + lw * 2048 + 1024);
      dma1(Wr + 128, r0, lds0 + RT_OFF + 8192 + lw * 2048);
      dma1(Wr + 128, r1, lds0 + RT_OFF + 8192 + lw * 2048 + 1024);
    }
#pragma unroll
    for (int s = 1; s < 4; ++s) {
      const int kt = rot + s >= 10 ? rot + s - 10 : rot + s;
      dma_stage(x + kt * 128, vA, vA1, vA2, vA3, lds0 + s * STG + lw * 4096, wqkv + (size_t)kt * (64 * 3840), vq0, vq1,
                lds0 + s * STG + 16384 + lw * 2048);
    }
  }
  SB();
  const int g = lane >> 4, l15 = lane & 15, q4 = l15 >> 2, p4 = lane & 3;
  const char* pc0 = (const char*)(pc_ & ~(u64)63);
  const __amdgpu_buffer_rsrc_t rs = __builtin_amdgcn_make_buffer_rsrc(ws, 0, WS_USED, 0x00020000);
  LAS unsigned* sync = (LAS unsigned*)(smem + SYNC_OFF);
  STAMP(0);
  u64 tag;
  {
    const char* p = ws;
    asm volatile("" : "+s"(p));
    tag = *(const CAS u64*)(p + OFF_FL) + 1;
  }
  const int lu1 = (6 * cg) & 3;
  const int sigl = ((q4 >> 1) & 1) | ((g & 1) << 1), xsw = g ^ (l15 >> 1);
  const int mt = (xcd >> 1) * 8 + (idx & 7), ct = (xcd & 1) * 4 + (idx >> 3);
  const int head = 4 * xcd + (idx & 3), qb = idx >> 2;
  const int ch = (5 * head) >> 1, odd = head & 1;
  auto orow_f = [&]() {
    unsigned ln = lane;
    asm volatile("" : "+v"(ln));
    return (64 * mt + 16 * (w & 3) + (ln & 15)) * 1280 + (80 * ct + 4 * (ln >> 4)) * 2;
  };
  u32x2 xv[3];
  f32x4 acc[4][5];
  const unsigned va = g * 1024 + l15 * 16;
  bf16x8 wpre[5][5];
  const unsigned wb = w < 4 ? OFF_W2F + (ct * 48 + 12 * w) * 5120 : OFF_WOF + (ct * 20 + 5 * (w - 4)) * 5120;

  if (w >= 4) {
    const int lt = tid - 256;
    const unsigned tg = (unsigned)tag;
    const unsigned vh0 = bk * 3072 + ((6 * cg) >> 2) * 128 + sw0, vh1 = (bk + 8) * 3072 + ((6 * cg) >> 2) * 128 + sw1;
    int ktn = rot + 4 >= 10 ? rot - 6 : rot + 4;
    auto issue = [&](auto S) {
      constexpr int s = decltype(S)::value;
      const unsigned dst = lds0 + (s & 3) * STG;
      const char* sw = s >= 10 ? w1 + (size_t)ktn * (64 * 3072) : wqkv + (size_t)ktn * (64 * 3840);
      dma_stage(x + ktn * 128, vA, vA1, vA2, vA3, dst + lw * 4096, sw, s >= 10 ? vh0 : vq0, s >= 10 ? vh1 : vq1, dst + 16384 + lw * 2048);
      ktn = ktn == 9 ? 0 : ktn + 1;
    };
    if (lt < 16) sync[lt] = 0u;
    WAIT_VM(24);
    BAR();
    STAMP(1);
    const bf16x8 ONE8 = {0x3f80, 0x3f80, 0x3f80, 0x3f80, 0x3f80, 0x3f80, 0x3f80, 0x3f80};
    const f32x4 z4 = {0.f, 0.f, 0.f, 0.f};
    bf16x8 sw_[2][2], sx_[2][2];
    u32x4 gr[5], gc[3];
    u32x2 fl;
    const int fkb = lane < 12 ? 6 * lw + (lane >> 1) : 24 + ((5 * lw) >> 1) + (lane < 18 ? (lane - 12) >> 1 : 0);
    const unsigned fW = FL_W + (fkb * 5 + ((5 * ct + (lane & 1) * 4) >> 3)) * 128;
    const int ty = lane < 24 ? lane >> 3 : 0, rr0 = 2 * qb - 2 + ((lane >> 1) & 3);
    const int rr = rr0 < 0 ? 0 : (ty == 0 && rr0 < 2 * qb ? 2 * qb : rr0);
    const unsigned fQ = FL_Q + wg_of(rr, 5 * ty + ((ch + (lane & 1) * 2) >> 4)) * 128;
    unsigned ro[5], co[3];
#pragma unroll
    for (int k = 0; k < 5; ++k) ro[k] = OFF_RS + (rt * 10 + 2 * k + (lane >> 5)) * 2048 + (32 * lw + (lane & 31)) * 16;
#pragma unroll
    for (int k = 0; k < 3; ++k) co[k] = OFF_CS + (cg * 10 + (k == 2 && g >= 2 ? g : g + 4 * k)) * 1024 + (16 * lw + l15) * 16;
    const unsigned c_on = g < 2 ? 0xffffffffu : 0u;
    auto col_sums = [&](int part) {
#pragma unroll
      for (int k = 0; k < 3; ++k) PIN(gc[k]);
      float c0, c1;
      if (!sum_pairs<3>(gc, c_on, tg, c0, c1)) regather<3>(rs, co, c_on, tg, c0, c1);
      c0 += __shfl_xor(c0, 16); c1 += __shfl_xor(c1, 16);
      c0 += __shfl_xor(c0, 32); c1 += __shfl_xor(c1, 32);
      if (lane < 16) *(LAS f32x2*)(smem + CS_OFF + part * 512 + (16 * lw + lane) * 8) = f32x2{c0, c1};
      WAIT_LGKM0();
      lds_arrive(sync + 7 + part);
    };
    auto stage = [&](auto S) {
      constexpr int s = decltype(S)::value;
      if constexpr (s == 0 || s == 10) {
        const unsigned sb = lds0 + (s & 3) * STG;
#pragma unroll
        for (int i = 0; i < 2; ++i) {
          const int lux = ((s == 0 ? 0 : lu1) + 2 * lw + i) & 7;
          const unsigned wq = sb + 16384 + (lux >> 2) * 8192 + (8 * g + q4) * 128 + (((lux & 3) ^ sigl) << 5) + p4 * 8;
          sw_[i][0] = cat44(trread(wq), trread(wq + 512));
          sw_[i][1] = cat44(trread(wq + 4096), trread(wq + 4608));
          if (s == 0) {
            const unsigned xq = sb + (32 * lw + 16 * i + l15) * 128;
            sx_[i][0] = *(LAS bf16x8*)(size_t)(xq + (xsw << 4));
            sx_[i][1] = *(LAS bf16x8*)(size_t)(xq + ((xsw ^ 4) << 4));
          }
        }
      }
      wait_vm<(s == 6 || s == 7) ? 24 : (s >= 10 && s <= 12 ? 16 + 5 * WOPRE : ((s == 15 || s == 16) ? 19 : (s == 17 ? 0 : 16)))>();
      WAIT_LGKM0();
      BAR();
      if constexpr (s < 16) issue(IC<s + 4>{});
      if constexpr (s == 2 || s == 3) {
        if (b < 170) {
          const unsigned dstb = rkb < 24 ? OFF_W2F : OFF_WOF;
          const int nks = rkb < 24 ? 48 : 20, kb = rkb < 24 ? rkb : rkb - 24;
          const int nl = 2 * lw + s - 2, nt = 8 * rnb + nl, c5 = nt / 5, jj = nt - 5 * c5;
          const unsigned rp = lds0 + RT_OFF + (nl >> 2) * 8192 + (8 * g + q4) * 128 + (((nl & 3) ^ sigl) << 5) + p4 * 8;
#pragma unroll
          for (int ks = 0; ks < 2; ++ks) {
            const bf16x8 f = cat44(trread(rp + ks * 4096), trread(rp + ks * 4096 + 512));
            st16(__builtin_bit_cast(u32x4, f), rs, lane * 16, dstb + ((c5 * nks + 2 * kb + ks) * 5 + jj) * 1024);
          }
        }
      }
      if constexpr (s == 0 || s == 10) {
        __builtin_amdgcn_s_setprio(3);
        const u32x4 c0 = __builtin_bit_cast(u32x4, mfma16(sw_[0][1], ONE8, mfma16(sw_[0][0], ONE8, z4)));
        const u32x4 c1 = __builtin_bit_cast(u32x4, mfma16(sw_[1][1], ONE8, mfma16(sw_[1][0], ONE8, z4)));
        const u32x4 cu = l15 == 0 ? c0 : c1;
        if (l15 < 2) {
          const unsigned o = OFF_CS + (((s == 10 ? 16 : 0) + cg) * 10 + rot) * 1024 + 2 * lw * 128;
          st16(u32x4{cu[0], tg, cu[1], tg}, rs, l15 * 128 + g * 32, o);
          st16(u32x4{cu[2], tg, cu[3], tg}, rs, l15 * 128 + g * 32 + 16, o);
        }
        __builtin_amdgcn_s_setprio(0);
      }
      if constexpr (s == 1) {
        __builtin_amdgcn_s_setprio(3);
#pragma unroll
        for (int i = 0; i < 2; ++i) {
          const f32x4 sx = mfma16(ONE8, sx_[i][1], mfma16(ONE8, sx_[i][0], z4));
          const f32x4 sq = mfma16(sx_[i][1], sx_[i][1], mfma16(sx_[i][0], sx_[i][0], z4));
          const int r = l15 & 3;
          const float sxx = r == 0 ? sq[0] : (r == 1 ? sq[1] : (r == 2 ? sq[2] : sq[3]));
          if (q4 == g)
            st16(u32x4{__float_as_uint(sx[0]), tg, __float_as_uint(sxx), tg}, rs, (32 * lw + 16 * i + l15) * 16,
                 OFF_RS + (rt * 10 + rot) * 2048);
        }
        __builtin_amdgcn_s_setprio(0);
      }
      if constexpr (s == 4) {
        if (lt < 12) dma1(b1 + cg * 192, lane * 16, lds0 + B1_OFF);
        if (lw == 1 && lane < 10) dma1(b2 + ct * 160, lane * 16, lds0 + B1_OFF + 256);
      }
      if constexpr (s == 6 || s == 11) {
        if (lt == 0 && (s == 6 || cg < 15)) __hip_atomic_store((GAS u64*)(ws + (s == 6 ? FL_W : FL_Q) + b * 128), tag, RLX_AGENT);
      }
      if constexpr (s == 5) {
#pragma unroll
        for (int k = 0; k < 5; ++k) gr[k] = aload16(ws, ro[k]);
      }
      if constexpr (s == 5 || s == 14) {
#pragma unroll
        for (int k = 0; k < 3; ++k) { if (s == 14) co[k] += 16 * 10240; gc[k] = aload16(ws, co[k]); }
      }
      if constexpr (s == 8) {
        __builtin_amdgcn_s_setprio(3);
#pragma unroll
        for (int k = 0; k < 5; ++k) PIN(gr[k]);
        float sx, sxx;
        if (!sum_pairs<5>(gr, 0xffffffffu, tg, sx, sxx)) regather<5>(rs, ro, 0xffffffffu, tg, sx, sxx);
        sx += __shfl_xor(sx, 32); sxx += __shfl_xor(sxx, 32);
        const float mean = sx * (1.f / 640.f);
        float var = sxx * (1.f / 640.f) - mean * mean;
        var = var < 0.f ? 0.f : var;
        if (lane < 32) *(LAS f32x2*)(smem + ST_OFF + (32 * lw + lane) * 8) = f32x2{mean, rsqrtf(var + 1e-5f)};
        col_sums(0);
        __builtin_amdgcn_s_setprio(0);
      }
      if constexpr (s == 9) {
        poll_flags(ws + fW, true, tag);
#pragma unroll
        for (int k = 0; k < WOPRE; ++k)
#pragma unroll
          for (int j = 0; j < 5; ++j) wpre[k][j] = ld16(rs, lane * 16, wb + k * 5120 + j * 1024);
      }
      if constexpr (s == 16) fl = aload8(ws, fQ);
    };
    stage(IC<0>{}); stage(IC<1>{}); stage(IC<2>{}); stage(IC<3>{}); stage(IC<4>{}); stage(IC<5>{});
    stage(IC<6>{}); stage(IC<7>{}); stage(IC<8>{}); stage(IC<9>{}); stage(IC<10>{}); stage(IC<11>{});
    stage(IC<12>{}); stage(IC<13>{}); stage(IC<14>{}); stage(IC<15>{}); stage(IC<16>{}); stage(IC<17>{});
    STAMP(2);
    PIN(fl);
    if (!__all(fl[0] == tg && fl[1] == (unsigned)(tag >> 32))) poll_flags(ws + fQ, true, tag);
    STAMP(11);
#pragma unroll
    for (int i = 0; i < 2; ++i) {
      const int seg = 2 * lw + i;
      int tb = 4 * qb - 4 + seg; tb = tb < 0 ? 0 : tb;
      dma_kv(ws + OFF_QKV + (size_t)(tb * 240 + 80 + ch) * 1024, lane * 16, lane * 16 + 81920, lds0 + K_OFF + seg * 1024);
    }
    const int l31 = lane & 31, hh = lane >> 5;
    u32x4 qraw[2][2];
#pragma unroll
    for (int a = 0; a < 2; ++a)
#pragma unroll
      for (int ks = 0; ks < 2; ++ks)
        qraw[a][ks] = __builtin_amdgcn_raw_buffer_load_b128(rs, (ks ? 2 : hh) * 1024 + (32 * a + l31) * 16,
                                                            OFF_QKV + ((4 * qb + lw) * 240 + ch) * 1024, SC1);
    col_sums(1);
    {
      const u32x4 one4 = {0x3f803f80u, 0x3f803f80u, 0x3f803f80u, 0x3f803f80u};
      *(LAS u32x4*)(smem + V_OFF + 3 * VP + lt * 16) = one4;
      if (lt < 40) *(LAS u32x4*)(smem + V_OFF + 3 * VP + 4096 + lt * 16) = one4;
      *(LAS u32x4*)(smem + K_OFF + 3 * KP + lt * 16) = u32x4{qb == 0 ? 0xff80u : 0u, 0u, 0u, 0u};
      *(LAS u32x4*)(smem + K_OFF + 3 * KP + 4096 + lt * 16) = u32x4{0u, 0u, 0u, 0u};
    }
    bf16x8 qf[2][2];
#pragma unroll
    for (int a = 0; a < 2; ++a) {
      u32x4 v0 = qraw[a][0], v1 = qraw[a][1];
      if (odd && hh == 0) { v0[0] = 0u; v0[1] = 0u; }
      if (hh) v1 = u32x4{0x3f80u, 0u, 0u, 0u};
      else if (!odd) { v1[2] = 0u; v1[3] = 0u; }
      qf[a][0] = __builtin_bit_cast(bf16x8, v0);
      qf[a][1] = __builtin_bit_cast(bf16x8, v1);
    }
    f32x16 mF, z16;
#pragma unroll
    for (int e = 0; e < 16; ++e) {
      mF[e] = (e & 3) + 8 * (e >> 2) + 4 * hh > l31 ? 0.f : -INFINITY;
      z16[e] = 0.f;
    }
    asm volatile("" : "+v"(mF), "+v"(qf[0][0]), "+v"(qf[0][1]), "+v"(qf[1][0]), "+v"(qf[1][1]));
    WAIT_VM(0);
    WAIT_LGKM0();
    lds_arrive(sync);
    lds_wait(sync, 4);
    __builtin_amdgcn_s_setprio(2);
    STAMP(5);
    unsigned kp = lds0 + K_OFF + hh * KP + (64 * lw + l31) * 16;
    const bool ones = (g & 1) && (p4 >> 1);
    unsigned vp = lds0 + V_OFF + (2 * (g & 1) + (p4 >> 1)) * VP + ((ones ? 0 : 64 * lw) + 4 * hh + q4) * 16 + (p4 & 1) * 8;
    unsigned ov = l31 * 16 + hh * 8;
    bf16x8 q0 = qf[0][0], q1 = qf[0][1];
#define LDK(T, ks) (*(LAS bf16x8*)(size_t)(kp + (T) * 512 + (ks) * 2 * KP))
#define MF32(a, b, c) __builtin_amdgcn_mfma_f32_32x32x16_bf16(a, b, c, 0, 0, 0)
#define EX2(s_, e) pk2(__builtin_amdgcn_exp2f((s_)[e]), __builtin_amdgcn_exp2f((s_)[(e) + 1]))
    auto tile_max = [&](int T) {
      f32x16 sp = MF32(LDK(T, 1), q1, MF32(LDK(T, 0), q0, z16));
      float m0 = -INFINITY, m1 = -INFINITY;
#pragma unroll
      for (int e = 0; e < 16; ++e) {
        const bool off = T == 0 ? mF[e] != 0.f : (T == 8 ? mF[e] == 0.f : false);
        sp[e] = off ? -INFINITY : sp[e];
      }
#pragma unroll
      for (int e = 0; e < 16; e += 4) { m0 = MAX3(m0, sp[e], sp[e + 1]); m1 = MAX3(m1, sp[e + 2], sp[e + 3]); }
      m0 = fmaxf(m0, m1);
      return fmaxf(m0, __shfl_xor(m0, 32));
    };
#pragma unroll 1
    for (int un = 0; un < 2; ++un) {
      float mx = tile_max(8);
      f32x16 oa;
#pragma unroll 1
      for (int pass = 0; pass < 2; ++pass) {
      f32x16 ca;
#pragma unroll
      for (int e = 0; e < 16; ++e) ca[e] = -mx;
      oa = z16;
      {
        bf16x8 kc[2][2], vf[2][2];
        f32x16 sc[2];
        u32x4 pa[2] = {{0u, 0u, 0u, 0u}, {0u, 0u, 0u, 0u}};
        kc[0][0] = LDK(0, 0); kc[0][1] = LDK(0, 1); kc[1][0] = LDK(1, 0); kc[1][1] = LDK(1, 1);
        sc[0] = MF32(kc[0][1], q1, MF32(kc[0][0], q0, ca + mF));
        sc[1] = z16;
        asm volatile("" :: "v"(sc[0]));
        SB();
#pragma unroll
        for (int T = 0; T < 10; ++T) {
          const int c = T & 1, n = c ^ 1;
          if (T < 9) {
#pragma unroll
            for (int ks = 0; ks < 2; ++ks) vf[c][ks] = cat44(trread(vp + T * 512 + ks * 256), trread(vp + T * 512 + ks * 256 + 128));
          }
          if (T < 8) sc[n] = MF32(kc[n][1], q1, MF32(kc[n][0], q0, ca));
          if (T < 7) { kc[c][0] = LDK(T + 2, 0); kc[c][1] = LDK(T + 2, 1); }
          if (T > 0) oa = MF32(vf[n][1], __builtin_bit_cast(bf16x8, pa[1]), MF32(vf[n][0], __builtin_bit_cast(bf16x8, pa[0]), oa));
          if (T == 8) {
#pragma unroll
            for (int e = 0; e < 16; ++e) sc[c][e] = mF[e] == 0.f ? -INFINITY : sc[c][e];
          }
          if (T < 9) {
#pragma unroll
            for (int e = 0; e < 4; ++e) { pa[0][e] = EX2(sc[c], 2 * e); pa[1][e] = EX2(sc[c], 8 + 2 * e); }
          }
          SGB(0x100, 6);
          if (T == 8) SGB(0x2, 16);
          SGB(0x8, 1); SGB(0x400, 4); SGB(0x2, 1);
#pragma unroll
          for (int k = 0; k < 3; ++k) { SGB(0x8, 1); SGB(0x400, 2); SGB(0x2, 1); SGB(0x400, 2); SGB(0x2, 1); }
          SGB(0x2, 1);
          asm volatile("" :: "v"(sc[n]), "v"(oa), "v"(pa[0]), "v"(pa[1]));
          SB();
        }
      }
      if (pass || __all(oa[12] < 1.8446744e19f)) break;
#pragma unroll 1
      for (int T = 0; T < 8; ++T) mx = fmaxf(mx, tile_max(T));
      }
      const float il = __builtin_amdgcn_rcpf(oa[12]);
#pragma unroll
      for (int pl = 0; pl < 3; ++pl) {
        const bool ok = pl == 1 || (pl == 0 ? !(odd && hh == 0) : (odd || hh == 0));
        if (ok) {
          const u32x2 pkd = {pk2(oa[4 * pl] * il, oa[4 * pl + 1] * il), pk2(oa[4 * pl + 2] * il, oa[4 * pl + 3] * il)};
          __builtin_amdgcn_raw_buffer_store_b64(pkd, rs, pl * 1024 + ov, OFF_O + ((4 * qb + lw) * 80 + ch) * 1024, SC1);
        }
      }
      kp += 512; vp += ones ? 0 : 512; ov += 512;
      q0 = qf[1][0]; q1 = qf[1][1];
    }
    __builtin_amdgcn_s_setprio(0);
    lds_arrive(sync + 4);
    STAMP(6);
    WAIT_VM(0);
    if (lane == 0) __hip_atomic_store((GAS u64*)(ws + FL_O + (b * 4 + lw) * 128), tag, RLX_AGENT);
#pragma unroll
    for (int k = WOPRE; k < 5; ++k)
#pragma unroll
      for (int j = 0; j < 5; ++j) wpre[k][j] = ld16(rs, lane * 16, wb + k * 5120 + j * 1024);
    const unsigned orow = orow_f();
#pragma unroll
    for (int j = 0; j < 2; ++j) xv[j] = *(const u32x2*)(x + orow + (3 + j) * 32);
    const unsigned ab = OFF_O + (mt * 80 + 20 * lw) * 1024;
    {
      const int hd = 8 * lw + (lane & 7);
      poll_flags(ws + FL_O + ((((((mt >> 2) << 2) | (hd & 3)) << 3) | (hd >> 2)) * 4 + (mt & 3)) * 128, lane < 8, tag);
    }
    STAMP(7);
    bf16x8 af[4][5];
#pragma unroll
    for (int i = 0; i < 4; ++i)
#pragma unroll
      for (int k = 0; k < 5; ++k) af[i][k] = ld16(rs, va + ((lw + 1 + i) & 3) * 256, ab + k * 4096);
    SB();
    lds_wait(sync + 4, 4);
    lds_wait(sync + 6, 4);
    f32x4 own[2];
#pragma unroll
    for (int i = 0; i < 4; ++i) {
      const int mi = (lw + 1 + i) & 3;
      f32x4 a5[5] = {z4, z4, z4, z4, z4};
#pragma unroll
      for (int k = 0; k < 5; ++k)
#pragma unroll
        for (int j = 0; j < 5; ++j) a5[j] = mfma16(wpre[k][j], af[i][k], a5[j]);
#pragma unroll
      for (int j = 0; j < 3; ++j) *(LAS f32x4*)(smem + REDS1_OFF + ((mi * 4 + lw) * 3 + j) * 1024 + lane * 16) = a5[j];
      if (i < 3) {
#pragma unroll
        for (int j = 0; j < 2; ++j) *(LAS f32x4*)(smem + REDS2_OFF + ((mi * 3 + 2 - i) * 2 + j) * 1024 + lane * 16) = a5[3 + j];
      } else { own[0] = a5[3]; own[1] = a5[4]; }
    }
    STAMP(8);
    __syncthreads();
    STAMP(9);
#pragma unroll
    for (int j = 0; j < 2; ++j) {
      f32x4 sv = own[j];
#pragma unroll
      for (int k = 0; k < 3; ++k) sv += *(LAS f32x4*)(smem + REDS2_OFF + ((lw * 3 + k) * 2 + j) * 1024 + lane * 16);
      const f32x4 gt = *(LAS f32x4*)(smem + GX_OFF + (lw * 2 + j) * 1024 + lane * 16);
      *(u32x2*)(out + orow + (3 + j) * 32) = fin4(xv[j], sv, gt);
    }

  } else {
    if (idx * 4 + w < CODE_KB) {
      const u32x4 t0 = *(const u32x4*)(pc0 + (idx * 4 + w) * 1024 + lane * 16);
      asm volatile("" :: "v"(t0));
    }
    {
      const int mw = w & 1, nw = w >> 1;
      unsigned xa[2];
      xa[0] = lds0 + (64 * mw + l15) * 128 + (xsw << 4);
      xa[1] = lds0 + (64 * mw + l15) * 128 + ((xsw ^ 4) << 4);
      const unsigned wl = lds0 + 16384 + (8 * g + q4) * 128 + p4 * 8;
      const unsigned vo = (g >> 1) * 1024 + (16 * (g & 1) + l15) * 16;
      float sh[4], sc[4];
      BAR();
      STAMP(1);
      mfma_part<4, 0>(xa, wl, 4 * nw, sigl, lds0, sh, sc, 64 * mw + l15, cg < 5 ? 0.22360679774997896f * 1.4426950408889634f : 1.f, rs, vo,
                      cg < 15 ? OFF_QKV + ((2 * rt + mw) * 240 + 16 * cg + 8 * nw) * 1024 : 0x80000000u, 4 * nw, g, ws);
      STAMP(14);
      mfma_part<3, 10>(xa, wl, lu1 + 3 * nw, sigl, lds0, sh, sc, 0, 1.f, rs, vo,
                       OFF_H + ((2 * rt + mw) * 192 + 12 * cg + 6 * nw) * 1024, 3 * nw, g, ws);
    }
    STAMP(3);
    WAIT_VM(0);
    if (lds_arrive(sync + 2) == 3 && lane == 0) __hip_atomic_store((GAS u64*)(ws + FL_H + b * 128), tag, RLX_AGENT);
    STAMP(4);
#pragma unroll
    for (int k = 0; k < 4; ++k)
#pragma unroll
      for (int j = 0; j < 5; ++j) wpre[k][j] = ld16(rs, lane * 16, wb + k * 5120 + j * 1024);
    const unsigned wl_ = lds0 + slab_of(w);
    lds_wait(sync + 2, 4);
    dma5_sc1(ws + wb + 4 * 5120, lane * 16, wl_);
    dma5_sc1(ws + wb + 5 * 5120, lane * 16, wl_ + 5120);
    const unsigned ab = OFF_H + (mt * 192 + 48 * w) * 1024;
    if (b == 0 && w == 0) {
#pragma unroll 1
      for (int spins = 0; spins < (1 << 20); ++spins) {
        u64 v[4];
#pragma unroll
        for (int i = 0; i < 4; ++i) v[i] = __hip_atomic_load((const GAS u64*)(ws + FL_W + (lane + 64 * i) * 128), RLX_AGENT);
        if (__all(((v[0] ^ tag) | (v[1] ^ tag) | (v[2] ^ tag) | (v[3] ^ tag)) == 0)) break;
        __builtin_amdgcn_s_sleep(1);
      }
      if (lane == 0) __hip_atomic_store((GAS u64*)(ws + OFF_FL), tag, RLX_AGENT);
    }
    poll_flags(ws + FL_H + wg_of(mt >> 1, 4 * w + (lane & 3)) * 128, lane < 4, tag);
    STAMP(7);
    unsigned vi[4];
#pragma unroll
    for (int i = 0; i < 4; ++i) vi[i] = va + ((w + 1 + i) & 3) * 256;
    dma5_sc1(ws + ab + 4 * 4096, lane * 16, wl_ + 10240);
    dma5_sc1(ws + ab + 4 * 4096 + 3072, lane * 16, wl_ + 10240 + 3072);
    bf16x8 af[4][4];
#pragma unroll
    for (int k = 0; k < 4; ++k)
#pragma unroll
      for (int i = 0; i < 4; ++i) af[k][i] = ld16(rs, vi[i], ab + k * 4096);
    SB();
#pragma unroll
    for (int i = 0; i < 4; ++i)
#pragma unroll
      for (int j = 0; j < 5; ++j) acc[i][j] = f32x4{0.f, 0.f, 0.f, 0.f};
#pragma unroll
    for (int k = 0; k < 12; ++k) {
      const int cs = k & 3;
      if (k == 6)
        asm volatile("s_mov_b64 exec, 1\n\tds_add_u32 %0, %1\n\ts_mov_b64 exec, -1" :: "v"(lds0 + SYNC_OFF + 24), "v"(1u) : "memory");
      if (k == W2HOLD) {
        unsigned tv, ts;
        asm volatile(".Lp%=:\n\tds_read_b32 %0, %2\n\ts_waitcnt lgkmcnt(0)\n\tv_readfirstlane_b32 %1, %0\n\t"
                     "s_cmp_ge_u32 %1, 4\n\ts_cbranch_scc1 .Le%=\n\ts_sleep 2\n\ts_branch .Lp%=\n.Le%=:"
                     : "=&v"(tv), "=&s"(ts) : "v"(lds0 + SYNC_OFF + 16) : "memory", "scc");
      }
#pragma unroll
      for (int i = 0; i < 4; ++i) {
#pragma unroll
        for (int j = 0; j < 5; ++j) acc[i][j] = mfma16(wpre[cs][j], af[cs][i], acc[i][j]);
        if (k == 11 && i < 3) {
#pragma unroll
          for (int j = 0; j < 5; ++j)
            *(LAS f32x4*)(smem + REDG_OFF + ((((w + 1 + i) & 3) * 3 + 2 - i) * 5 + j) * 1024 + lane * 16) = acc[i][j];
        }
      }
      SB();
      if (k < 2) {
#pragma unroll
        for (int j = 0; j < 5; ++j) wpre[cs][j] = *(LAS bf16x8*)(size_t)(wl_ + k * 5120 + j * 1024 + lane * 16);
#pragma unroll
        for (int i = 0; i < 4; ++i) af[cs][i] = *(LAS bf16x8*)(size_t)(wl_ + 10240 + k * 4096 + vi[i]);
      } else if (k < 8) {
#pragma unroll
        for (int i = 0; i < 4; ++i) af[cs][i] = ld16(rs, vi[i], ab + (k + 4) * 4096);
#pragma unroll
        for (int j = 0; j < 5; ++j) wpre[cs][j] = ld16(rs, lane * 16, wb + (k + 4) * 5120 + j * 1024);
      }
      if (k == 10) {
        const unsigned orow = orow_f();
#pragma unroll
        for (int j = 0; j < 3; ++j) xv[j] = *(const u32x2*)(x + orow + j * 32);
      }
      SB();
    }
    STAMP(8);
    u32x2 bb[5];
#pragma unroll
    for (int j = 0; j < 5; ++j) bb[j] = *(LAS u32x2*)(smem + B1_OFF + 256 + (16 * j + 4 * g) * 2);
    WAIT_LGKM0();
    lds_arrive(sync + 3);
    lds_wait(sync + 3, 4);
    f32x4 gt[5];
#pragma unroll
    for (int j = 0; j < 5; ++j) {
      gt[j] = acc[3][j];
#pragma unroll
      for (int k = 0; k < 3; ++k) gt[j] += *(LAS f32x4*)(smem + REDG_OFF + ((w * 3 + k) * 5 + j) * 1024 + lane * 16);
    }
#pragma unroll
    for (int j = 0; j < 5; ++j) {
      const float bv[4] = {bflo(bb[j][0]), bfhi(bb[j][0]), bflo(bb[j][1]), bfhi(bb[j][1])};
#pragma unroll
      for (int e = 0; e < 4; ++e) {
        const float t = rbf(rbf(gt[j][e]) + bv[e]);
        gt[j][e] = rbf(t * __builtin_amdgcn_rcpf(1.f + __expf(-t)));
      }
      if (j >= 3) *(LAS f32x4*)(smem + GX_OFF + (w * 2 + j - 3) * 1024 + lane * 16) = gt[j];
    }
    __syncthreads();
    STAMP(9);
    const unsigned orow = orow_f();
#pragma unroll
    for (int j = 0; j < 3; ++j) {
      f32x4 sv = *(LAS f32x4*)(smem + REDS1_OFF + ((w * 4) * 3 + j) * 1024 + lane * 16);
#pragma unroll
      for (int k = 1; k < 4; ++k) sv += *(LAS f32x4*)(smem + REDS1_OFF + ((w * 4 + k) * 3 + j) * 1024 + lane * 16);
      *(u32x2*)(out + orow + j * 32) = fin4(xv[j], sv, gt[j]);
    }
  STAMP(10);
    __builtin_amdgcn_endpgm();
  }
  STAMP(10);
}

extern "C" void kernel_launch(void* const* d_in, const int* in_sizes, int n_in, void* d_out, int out_size, void* d_ws,
                              size_t ws_size, hipStream_t stream) {
  (void)hipFuncSetAttribute((const void*)k_all, hipFuncAttributeMaxDynamicSharedMemorySize, LDS_ALL);
  k_all<<<256, 512, LDS_ALL, stream>>>((const char*)d_in[0], (const char*)d_in[1], (const char*)d_in[4], (const char*)d_in[5],
                                       (const char*)d_in[6], (const char*)d_in[2], (const char*)d_in[7], (char*)d_ws, (char*)d_out);
}
```

```cpp
#include <hip/hip_runtime.h>
#include <stdint.h>

#define LAS __attribute__((address_space(3)))
#define GAS __attribute__((address_space(1)))
#define CAS __attribute__((address_space(4)))
typedef short bf16x8 __attribute__((ext_vector_type(8)));
typedef short s16x4 __attribute__((ext_vector_type(4)));
typedef float f32x4 __attribute__((ext_vector_type(4)));
typedef unsigned u32x4 __attribute__((ext_vector_type(4)));
typedef unsigned u32x2 __attribute__((ext_vector_type(2)));
typedef __bf16 bf16x2_t __attribute__((ext_vector_type(2)));
typedef float f32x2 __attribute__((ext_vector_type(2)));
typedef float f32x16 __attribute__((ext_vector_type(16)));
typedef unsigned long long u64;

constexpr unsigned OFF_FL = 0;
constexpr unsigned FL_W = 1024, FL_Q = FL_W + 32768, FL_H = FL_Q + 32768, FL_O = FL_H + 32768;
constexpr unsigned OFF_RS = 262144;
constexpr unsigned OFF_CS = OFF_RS + 16 * 10 * 2048;
constexpr unsigned OFF_QKV = OFF_CS + 2 * 16 * 10 * 1024;
constexpr unsigned OFF_H = OFF_QKV + 32 * 240 * 1024;
constexpr unsigned OFF_O = OFF_H + 32 * 192 * 1024;
constexpr unsigned OFF_W2F = OFF_O + 32 * 80 * 1024;
constexpr unsigned OFF_WOF = OFF_W2F + 8 * 48 * 5120;
constexpr unsigned WS_USED = OFF_WOF + 8 * 20 * 5120;

__device__ __forceinline__ f32x4 mfma16(bf16x8 a, bf16x8 b, f32x4 c) {
  return __builtin_amdgcn_mfma_f32_16x16x32_bf16(a, b, c, 0, 0, 0);
}
__device__ __forceinline__ unsigned pk2(float a, float b) {
  f32x2 v = {a, b};
  bf16x2_t r = __builtin_convertvector(v, bf16x2_t);
  return __builtin_bit_cast(unsigned, r);
}
__device__ __forceinline__ float rbf(float a) { return __builtin_bit_cast(float, pk2(0.f, a) & 0xffff0000u); }
__device__ __forceinline__ float bflo(unsigned u) { return __builtin_bit_cast(float, u << 16); }
__device__ __forceinline__ float bfhi(unsigned u) { return __builtin_bit_cast(float, u & 0xffff0000u); }
__device__ __forceinline__ u32x2 fin4(u32x2 xv, f32x4 sv, f32x4 gt) {
  return u32x2{pk2(bflo(xv[0]) + rbf(rbf(sv[0]) * gt[0]), bfhi(xv[0]) + rbf(rbf(sv[1]) * gt[1])),
               pk2(bflo(xv[1]) + rbf(rbf(sv[2]) * gt[2]), bfhi(xv[1]) + rbf(rbf(sv[3]) * gt[3]))};
}
__device__ __forceinline__ s16x4 trread(unsigned a) {
  return __builtin_amdgcn_ds_read_tr16_b64_v4i16((LAS s16x4*)(size_t)a);
}
__device__ __forceinline__ bf16x8 cat44(s16x4 a, s16x4 b) {
  return __builtin_shufflevector(a, b, 0, 1, 2, 3, 4, 5, 6, 7);
}
#define WAIT_VM(n) asm volatile("s_waitcnt vmcnt(" #n ")" ::: "memory")
#define WAIT_LGKM0() __builtin_amdgcn_s_waitcnt(0xC07F)
#define BAR() __builtin_amdgcn_s_barrier()
#define SB() __builtin_amdgcn_sched_barrier(0)
#define SGB(m, n) __builtin_amdgcn_sched_group_barrier(m, n, 0)
#define MAX3(a, b, c) __builtin_fmaxf(__builtin_fmaxf(a, b), c)
#define RLX_AGENT __ATOMIC_RELAXED, __HIP_MEMORY_SCOPE_AGENT
constexpr int SC1 = 16;
constexpr int CODE_KB = 39;
template <int N> struct IC { static constexpr int value = N; };

#define STAMP(i)
#define CLK(v)

__device__ __forceinline__ void dma1(const char* sbase, unsigned voff, unsigned lds) {
  unsigned keep;
  asm volatile("s_mov_b32 %0, m0\n\ts_mov_b32 m0, %3\n\ts_nop 0\n\tglobal_load_lds_dwordx4 %1, %2\n\ts_mov_b32 m0, %0"
               : "=&s"(keep) : "v"(voff), "s"(sbase), "s"(lds) : "memory");
}
__device__ __forceinline__ void dma_stage(const char* sa, unsigned va0, unsigned va1, unsigned va2, unsigned va3, unsigned la,
                                          const char* sb, unsigned vb0, unsigned vb1, unsigned lb) {
  unsigned keep;
  asm volatile("s_mov_b32 %0, m0\n\ts_mov_b32 m0, %10\n\ts_nop 0\n\tglobal_load_lds_dwordx4 %8, %7\n\t"
               "s_add_u32 m0, m0, 1024\n\ts_nop 0\n\tglobal_load_lds_dwordx4 %9, %7\n\t"
               "s_add_u32 m0, m0, 7040\n\ts_nop 0\n\tglobal_load_lds_dwordx4 %8, %7 offset:128\n\t"
               "s_add_u32 m0, m0, 1024\n\ts_nop 0\n\tglobal_load_lds_dwordx4 %9, %7 offset:128\n\t"
               "s_mov_b32 m0, %6\n\ts_nop 0\n\tglobal_load_lds_dwordx4 %2, %1\n\t"
               "s_add_u32 m0, m0, 1024\n\ts_nop 0\n\tglobal_load_lds_dwordx4 %3, %1\n\t"
               "s_add_u32 m0, m0, 1024\n\ts_nop 0\n\tglobal_load_lds_dwordx4 %4, %1\n\t"
               "s_add_u32 m0, m0, 1024\n\ts_nop 0\n\tglobal_load_lds_dwordx4 %5, %1\n\ts_mov_b32 m0, %0"
               : "=&s"(keep) : "s"(sa), "v"(va0), "v"(va1), "v"(va2), "v"(va3), "s"(la), "s"(sb), "v"(vb0), "v"(vb1), "s"(lb)
               : "memory", "scc");
}
__device__ __forceinline__ void dma_kv(const char* sb, unsigned vk, unsigned vv, unsigned l) {
  unsigned keep;
  asm volatile("s_mov_b32 %0, m0\n\ts_mov_b32 m0, %4\n\ts_nop 0\n\tglobal_load_lds_dwordx4 %2, %1 sc1\n\t"
               "s_add_u32 m0, m0, 7168\n\ts_nop 0\n\tglobal_load_lds_dwordx4 %2, %1 offset:1024 sc1\n\t"
               "s_add_u32 m0, m0, 7168\n\ts_nop 0\n\tglobal_load_lds_dwordx4 %2, %1 offset:2048 sc1\n\t"
               "s_add_u32 m0, m0, 18432\n\ts_nop 0\n\tglobal_load_lds_dwordx4 %3, %1 sc1\n\t"
               "s_add_u32 m0, m0, 7232\n\ts_nop 0\n\tglobal_load_lds_dwordx4 %3, %1 offset:1024 sc1\n\t"
               "s_add_u32 m0, m0, 7232\n\ts_nop 0\n\tglobal_load_lds_dwordx4 %3, %1 offset:2048 sc1\n\t"
               "s_mov_b32 m0, %0"
               : "=&s"(keep) : "s"(sb), "v"(vk), "v"(vv), "s"(l) : "memory", "scc");
}
__device__ __forceinline__ void dma5_sc1(const char* sb, unsigned v, unsigned l) {
  unsigned keep;
  asm volatile("s_mov_b32 %0, m0\n\ts_mov_b32 m0, %3\n\ts_nop 0\n\tglobal_load_lds_dwordx4 %2, %1 sc1\n\t"
               "global_load_lds_dwordx4 %2, %1 offset:1024 sc1\n\tglobal_load_lds_dwordx4 %2, %1 offset:2048 sc1\n\t"
               "global_load_lds_dwordx4 %2, %1 offset:3072 sc1\n\ts_add_u32 m0, m0, 1024\n\ts_nop 0\n\t"
               "global_load_lds_dwordx4 %4, %1 offset:3072 sc1\n\ts_mov_b32 m0, %0"
               : "=&s"(keep) : "s"(sb), "v"(v), "s"(l), "v"(v + 1024) : "memory", "scc");
}
__device__ __forceinline__ u32x2 aload8(const char* sbase, unsigned voff) {
  u32x2 r;
  asm volatile("global_load_dwordx2 %0, %1, %2 sc1" : "=v"(r) : "v"(voff), "s"(sbase) : "memory");
  return r;
}
__device__ __forceinline__ u32x4 aload16(const char* sbase, unsigned voff) {
  u32x4 r;
  asm volatile("global_load_dwordx4 %0, %1, %2 sc1" : "=v"(r) : "v"(voff), "s"(sbase) : "memory");
  return r;
}
#define PIN(x) asm volatile("" : "+v"(x))
template <int N> __device__ __forceinline__ void wait_vm() { asm volatile("s_waitcnt vmcnt(%0)" :: "i"(N) : "memory"); }
__device__ __forceinline__ void poll_flags(const char* flag, bool need, u64 tag) {
#pragma unroll 1
  for (int spins = 0; spins < (1 << 20); ++spins) {
    const u64 v = need ? __hip_atomic_load((const GAS u64*)flag, RLX_AGENT) : tag;
    if (__all(v == tag)) break;
    __builtin_amdgcn_s_sleep(1);
  }
  __builtin_amdgcn_fence(__ATOMIC_ACQUIRE, "wavefront");
}
__device__ __forceinline__ unsigned lds_arrive(LAS unsigned* c) {
  unsigned r = 0;
  if ((threadIdx.x & 63) == 0) r = __hip_atomic_fetch_add(c, 1u, __ATOMIC_RELAXED, __HIP_MEMORY_SCOPE_WORKGROUP);
  return __builtin_amdgcn_readfirstlane(r);
}
__device__ __forceinline__ void lds_wait_sleep(LAS unsigned* c, unsigned n) {
#pragma unroll 1
  for (int spins = 0; spins < (1 << 22); ++spins) {
    if (__builtin_amdgcn_readfirstlane(__hip_atomic_load(c, __ATOMIC_RELAXED, __HIP_MEMORY_SCOPE_WORKGROUP)) >= n) break;
    __builtin_amdgcn_s_sleep(2);
  }
  __builtin_amdgcn_fence(__ATOMIC_ACQUIRE, "wavefront");
}
__device__ __forceinline__ void lds_wait(LAS unsigned* c, unsigned n) {
#pragma unroll 1
  for (int spins = 0; spins < (1 << 22); ++spins) {
    if (__builtin_amdgcn_readfirstlane(__hip_atomic_load(c, __ATOMIC_RELAXED, __HIP_MEMORY_SCOPE_WORKGROUP)) >= n) break;
  }
  __builtin_amdgcn_fence(__ATOMIC_ACQUIRE, "wavefront");
}

#define WOPRE 4
#define W2HOLD 10
constexpr int STG = 32768, RT_OFF = 4 * STG, LDS_ALL = 163840;
constexpr int B1_OFF = 159744;
constexpr int ST_OFF = 160768;
constexpr int CS_OFF = ST_OFF + 1024, SYNC_OFF = ST_OFF + 2048;
constexpr int KP = 8192, VP = 8192 + 64;
constexpr int K_OFF = 0, V_OFF = K_OFF + 4 * KP;
__device__ __forceinline__ constexpr int slab_of(int w) { return 65536 + w * 20480; }
constexpr int REDG_OFF = 0, GX_OFF = 61440, REDS1_OFF = GX_OFF + 8192, REDS2_OFF = REDS1_OFF + 49152;

__device__ __forceinline__ void st16(u32x4 v, __amdgpu_buffer_rsrc_t rs, unsigned voff, unsigned soff) {
  __builtin_amdgcn_raw_buffer_store_b128(v, rs, voff + soff, 0, SC1);
}
__device__ __forceinline__ bf16x8 ld16(__amdgpu_buffer_rsrc_t rs, unsigned voff, unsigned soff) {
  return __builtin_bit_cast(bf16x8, __builtin_amdgcn_raw_buffer_load_b128(rs, voff, soff, SC1));
}
__device__ __forceinline__ int wg_of(int rt, int cg) { return (((cg & 3) * 8 + (rt & 7)) << 3) | ((cg >> 2) << 1) | (rt >> 3); }
template <int N>
__device__ __forceinline__ bool sum_pairs(const u32x4 (&v)[N], unsigned last_on, unsigned tg, float& a, float& b) {
  bool ok = true;
  a = 0.f; b = 0.f;
#pragma unroll
  for (int k = 0; k < N; ++k) {
    ok = ok && v[k][1] == tg && v[k][3] == tg;
    const unsigned m = k == N - 1 ? last_on : 0xffffffffu;
    a += __uint_as_float(v[k][0] & m);
    b += __uint_as_float(v[k][2] & m);
  }
  return __all(ok);
}
template <int N>
__device__ __forceinline__ void regather(__amdgpu_buffer_rsrc_t rs, const unsigned (&vo)[N], unsigned last_on, unsigned tg, float& a, float& b) {
#pragma unroll 1
  for (int spins = 0; spins < (1 << 20); ++spins) {
    u32x4 v[N];
#pragma unroll
    for (int k = 0; k < N; ++k) v[k] = __builtin_amdgcn_raw_buffer_load_b128(rs, vo[k], 0, SC1);
    if (sum_pairs<N>(v, last_on, tg, a, b)) break;
    __builtin_amdgcn_s_sleep(2);
  }
}

#define STG_STAMP(s_)

#define LDX(set, i, ks) xf[set][i] = *(LAS bf16x8*)(size_t)(xa[ks] + (i) * 2048)
#define LDW(set, j, ks) wf[set][j] = cat44(trread(wa[j] + (ks) * 4096), trread(wa[j] + (ks) * 4096 + 512))
#define HALF(set, ks) do { \
    _Pragma("unroll") for (int j = 0; j < NT; ++j) { \
      if (j == 0) { _Pragma("unroll") for (int i = 0; i < 4; ++i) LDX((set) ^ 1, i, ks); } \
      else if (2 * j - 2 < NT) { LDW((set) ^ 1, 2 * j - 2, ks); if (2 * j - 1 < NT) LDW((set) ^ 1, 2 * j - 1, ks); } \
      _Pragma("unroll") for (int i = 0; i < 4; ++i) acc[i][j] = mfma16(wf[set][j], xf[set][i], acc[i][j]); \
      SGB(0x8, 1); SGB(0x100, 1); SGB(0x8, 1); SGB(0x100, 1); SGB(0x8, 1); SGB(0x100, 1); SGB(0x8, 1); SGB(0x100, 1); \
      asm volatile("" :: "v"(acc[0][j]), "v"(acc[1][j]), "v"(acc[2][j]), "v"(acc[3][j])); \
      SB(); \
    } } while (0)
template <int NT, int S0>
__device__ __forceinline__ void mfma_part(unsigned (&xa)[2], unsigned wl, int tl0, int sigl, unsigned lds0, float (&sh)[4], float (&sc)[4],
                                          int row0, float qs, __amdgpu_buffer_rsrc_t rs, unsigned vo, unsigned so, int cs0, int g, char* ws) {
  constexpr bool ISH = S0 == 10;
  unsigned wa[NT];
  f32x4 acc[4][NT];
#pragma unroll
  for (int j = 0; j < NT; ++j) {
    const int tl = tl0 + j;
    wa[j] = wl + (S0 & 3) * STG + (tl >> 2) * 8192 + (((tl & 3) ^ sigl) << 5);
#pragma unroll
    for (int i = 0; i < 4; ++i) acc[i][j] = f32x4{0.f, 0.f, 0.f, 0.f};
  }
  bf16x8 xf[2][4], wf[2][NT];
#pragma unroll
  for (int i = 0; i < 4; ++i) LDX(0, i, 0);
#pragma unroll
  for (int j = 0; j < NT; ++j) LDW(0, j, 0);
  WAIT_LGKM0();
  int buf = S0 & 3;
#pragma unroll 1
  for (int t = 0; t < 10; ++t) {
    HALF(0, 1);
    {
      const int d = buf == 3 ? -3 * STG : STG;
      buf = (buf + 1) & 3;
      xa[0] += d; xa[1] += d;
#pragma unroll
      for (int j = 0; j < NT; ++j) wa[j] += d;
    }
    WAIT_LGKM0();
    if (ISH) {
      asm volatile("s_cmp_lg_u32 %0, 1\n\ts_cbranch_scc1 .Lk%=\n\ts_waitcnt vmcnt(0)\n.Lk%=:\n\t"
                   "s_cmp_ge_u32 %0, 8\n\ts_cbranch_scc1 .Lc%=\n\ts_barrier\n.Lc%=:" :: "s"(t) : "memory", "scc");
    } else BAR();
    STG_STAMP(S0 + t);
    SB();
    HALF(1, 0);
    WAIT_LGKM0();
  }
  STAMP(ISH ? 2 : 13);
  lds_wait((LAS unsigned*)(size_t)(lds0 + SYNC_OFF + (ISH ? 32 : 28)), 4);
  if (!ISH) {
#pragma unroll
    for (int i = 0; i < 4; ++i) {
      const f32x2 st = *(LAS f32x2*)(size_t)(lds0 + ST_OFF + (row0 + 16 * i) * 8);
      sc[i] = st[1]; sh[i] = -st[0] * st[1];
    }
  }
#pragma unroll
  for (int j = 0; j < NT; ++j) {
    const f32x4 c4 = *(LAS f32x4*)(size_t)(lds0 + CS_OFF + (ISH ? 512 : 0) + ((cs0 + j) * 16 + 4 * g) * 4);
    f32x4 bv = {0.f, 0.f, 0.f, 0.f};
    if (ISH) {
      const u32x2 bb = *(LAS u32x2*)(size_t)(lds0 + B1_OFF + ((cs0 + j) * 16 + 4 * g) * 2);
      bv = f32x4{bflo(bb[0]), bfhi(bb[0]), bflo(bb[1]), bfhi(bb[1])};
    }
#pragma unroll
    for (int pp = 0; pp < 2; ++pp) {
      float v[2][4];
#pragma unroll
      for (int i = 0; i < 2; ++i)
#pragma unroll
        for (int r = 0; r < 4; ++r) {
          float t = __builtin_fmaf(sc[2 * pp + i], acc[2 * pp + i][j][r], __builtin_fmaf(sh[2 * pp + i], c4[r], bv[r]));
          if (ISH) { t = t > 0.f ? t : 0.f; t = t * t; } else t *= qs;
          v[i][r] = t;
        }
      const unsigned x0 = pk2(v[0][0], v[0][1]), x1 = pk2(v[0][2], v[0][3]);
      const unsigned y0 = pk2(v[1][0], v[1][1]), y1 = pk2(v[1][2], v[1][3]);
      const auto s0 = __builtin_amdgcn_permlane16_swap(x0, y0, false, false);
      const auto s1 = __builtin_amdgcn_permlane16_swap(x1, y1, false, false);
      const u32x4 o = {s0[0], s1[0], s0[1], s1[1]};
      st16(o, rs, vo + pp * 512, so + 2 * j * 1024);
    }
  }
}

__global__ __launch_bounds__(512, 2) void k_all(const char* __restrict__ x, const char* __restrict__ wqkv, const char* __restrict__ w1,
                                                const char* __restrict__ b1, const char* __restrict__ w2, const char* __restrict__ wo,
                                                const char* __restrict__ b2, char* ws, char* __restrict__ out) {
  extern __shared__ __attribute__((aligned(16))) char smem_[];
  LAS char* smem = (LAS char*)smem_;
  const int tid = threadIdx.x, lane = tid & 63, w = __builtin_amdgcn_readfirstlane(tid >> 6);
  const int b = blockIdx.x, xcd = b & 7, idx = b >> 3;
  const unsigned lds0 = (unsigned)(size_t)smem;
  asm volatile("" :: "s"(x), "s"(wqkv), "s"(w1), "s"(b1), "s"(w2), "s"(wo), "s"(ws));
  u64 pc_;
  asm volatile("s_getpc_b64 %0" : "=s"(pc_));
  const int rt = (xcd & 1) * 8 + (idx & 7), cg = (xcd >> 1) * 4 + (idx >> 3);
  const int rot = (rt + 3 * cg) % 10;
  const int lw = w - 4, lr = lane >> 3, ls = lane & 7;
  const int bk = 16 * lw + lr, sw0 = (ls ^ (bk & 2)) << 4, sw1 = sw0 ^ 64;
  const unsigned vq0 = bk * 3840 + (cg < 15 ? cg : 14) * 256 + sw0, vq1 = (bk + 8) * 3840 + (cg < 15 ? cg : 14) * 256 + sw1;
  const unsigned vA = (rt * 128 + 32 * lw + lr) * 1280 + ((ls ^ (lr >> 1)) << 4);
  const unsigned vA1 = (vA + 10240) ^ 64u, vA2 = vA + 20480, vA3 = (vA + 30720) ^ 64u;
  const int rkb = b / 5, rnb = b - 5 * rkb;
  if (w >= 4) {
    dma_stage(x + rot * 128, vA, vA1, vA2, vA3, lds0 + lw * 4096, wqkv + (size_t)rot * (64 * 3840), vq0, vq1, lds0 + 16384 + lw * 2048);
    if (b < 170) {
      const char* Wr = rkb < 24 ? w2 + (size_t)rkb * 64 * 1280 : wo + (size_t)(rkb - 24) * 64 * 1280;
      const unsigned r0 = bk * 1280 + rnb * 256 + sw0, r1 = (bk + 8) * 1280 + rnb * 256 + sw1;
      dma1(Wr, r0, lds0 + RT_OFF + lw * 2048);
      dma1(Wr, r1, lds0 + RT_OFF + lw * 2048 + 1024);
      dma1(Wr + 128, r0, lds0 + RT_OFF + 8192 + lw * 2048);
      dma1(Wr + 128, r1, lds0 + RT_OFF + 8192 + lw * 2048 + 1024);
    }
#pragma unroll
    for (int s = 1; s < 2; ++s) {
      const int kt = rot + s >= 10 ? rot + s - 10 : rot + s;
      dma_stage(x + kt * 128, vA, vA1, vA2, vA3, lds0 + s * STG + lw * 4096, wqkv + (size_t)kt * (64 * 3840), vq0, vq1,
                lds0 + s * STG + 16384 + lw * 2048);
    }
  }
  SB();
  const int g = lane >> 4, l15 = lane & 15, q4 = l15 >> 2, p4 = lane & 3;
  const char* pc0 = (const char*)(pc_ & ~(u64)63);
  const __amdgpu_buffer_rsrc_t rs = __builtin_amdgcn_make_buffer_rsrc(ws, 0, WS_USED, 0x00020000);
  LAS unsigned* sync = (LAS unsigned*)(smem + SYNC_OFF);
  STAMP(0);
  u64 tag;
  {
    const char* p = ws;
    asm volatile("" : "+s"(p));
    tag = *(const CAS u64*)(p + OFF_FL) + 1;
  }
  const int lu1 = (6 * cg) & 3;
  const int sigl = ((q4 >> 1) & 1) | ((g & 1) << 1), xsw = g ^ (l15 >> 1);
  const int mt = (xcd >> 1) * 8 + (idx & 7), ct = (xcd & 1) * 4 + (idx >> 3);
  const int head = 4 * xcd + (idx & 3), qb = idx >> 2;
  const int ch = (5 * head) >> 1, odd = head & 1;
  auto orow_f = [&]() {
    unsigned ln = lane;
    asm volatile("" : "+v"(ln));
    return (64 * mt + 16 * (w & 3) + (ln & 15)) * 1280 + (80 * ct + 4 * (ln >> 4)) * 2;
  };
  u32x2 xv[3];
  f32x4 acc[4][5];
  const unsigned va = g * 1024 + l15 * 16;
  bf16x8 wpre[5][5];
  const unsigned wb = w < 4 ? OFF_W2F + (ct * 48 + 12 * w) * 5120 : OFF_WOF + (ct * 20 + 5 * (w - 4)) * 5120;

  if (w >= 4) {
    const int lt = tid - 256;
    const unsigned tg = (unsigned)tag;
    const unsigned vh0 = bk * 3072 + ((6 * cg) >> 2) * 128 + sw0, vh1 = (bk + 8) * 3072 + ((6 * cg) >> 2) * 128 + sw1;
    int ktn = rot + 2 >= 10 ? rot - 8 : rot + 2;
    auto issue = [&](auto S) {
      constexpr int s = decltype(S)::value;
      const unsigned dst = lds0 + (s & 3) * STG;
      const char* sw = s >= 10 ? w1 + (size_t)ktn * (64 * 3072) : wqkv + (size_t)ktn * (64 * 3840);
      dma_stage(x + ktn * 128, vA, vA1, vA2, vA3, dst + lw * 4096, sw, s >= 10 ? vh0 : vq0, s >= 10 ? vh1 : vq1, dst + 16384 + lw * 2048);
      ktn = ktn == 9 ? 0 : ktn + 1;
    };
    if (lt < 16) sync[lt] = 0u;
    WAIT_VM(8);
    BAR();
    STAMP(1);
    issue(IC<2>{}); issue(IC<3>{});
    const bf16x8 ONE8 = {0x3f80, 0x3f80, 0x3f80, 0x3f80, 0x3f80, 0x3f80, 0x3f80, 0x3f80};
    const f32x4 z4 = {0.f, 0.f, 0.f, 0.f};
    bf16x8 sw_[2][2], sx_[2][2];
    u32x4 gr[5], gc[3];
    u32x2 fl;
    const int fkb = lane < 12 ? 6 * lw + (lane >> 1) : 24 + ((5 * lw) >> 1) + (lane < 18 ? (lane - 12) >> 1 : 0);
    const unsigned fW = FL_W + (fkb * 5 + ((5 * ct + (lane & 1) * 4) >> 3)) * 128;
    const int ty = lane < 24 ? lane >> 3 : 0, rr0 = 2 * qb - 2 + ((lane >> 1) & 3);
    const int rr = rr0 < 0 ? 0 : (ty == 0 && rr0 < 2 * qb ? 2 * qb : rr0);
    const unsigned fQ = FL_Q + wg_of(rr, 5 * ty + ((ch + (lane & 1) * 2) >> 4)) * 128;
    unsigned ro[5], co[3];
#pragma unroll
    for (int k = 0; k < 5; ++k) ro[k] = OFF_RS + (rt * 10 + 2 * k + (lane >> 5)) * 2048 + (32 * lw + (lane & 31)) * 16;
#pragma unroll
    for (int k = 0; k < 3; ++k) co[k] = OFF_CS + (cg * 10 + (k == 2 && g >= 2 ? g : g + 4 * k)) * 1024 + (16 * lw + l15) * 16;
    const unsigned c_on = g < 2 ? 0xffffffffu : 0u;
    auto col_sums = [&](int part) {
#pragma unroll
      for (int k = 0; k < 3; ++k) PIN(gc[k]);
      float c0, c1;
      if (!sum_pairs<3>(gc, c_on, tg, c0, c1)) regather<3>(rs, co, c_on, tg, c0, c1);
      c0 += __shfl_xor(c0, 16); c1 += __shfl_xor(c1, 16);
      c0 += __shfl_xor(c0, 32); c1 += __shfl_xor(c1, 32);
      if (lane < 16) *(LAS f32x2*)(smem + CS_OFF + part * 512 + (16 * lw + lane) * 8) = f32x2{c0, c1};
      WAIT_LGKM0();
      lds_arrive(sync + 7 + part);
    };
    auto stage = [&](auto S) {
      constexpr int s = decltype(S)::value;
      if constexpr (s == 0 || s == 10) {
        const unsigned sb = lds0 + (s & 3) * STG;
#pragma unroll
        for (int i = 0; i < 2; ++i) {
          const int lux = ((s == 0 ? 0 : lu1) + 2 * lw + i) & 7;
          const unsigned wq = sb + 16384 + (lux >> 2) * 8192 + (8 * g + q4) * 128 + (((lux & 3) ^ sigl) << 5) + p4 * 8;
          sw_[i][0] = cat44(trread(wq), trread(wq + 512));
          sw_[i][1] = cat44(trread(wq + 4096), trread(wq + 4608));
          if (s == 0) {
            const unsigned xq = sb + (32 * lw + 16 * i + l15) * 128;
            sx_[i][0] = *(LAS bf16x8*)(size_t)(xq + (xsw << 4));
            sx_[i][1] = *(LAS bf16x8*)(size_t)(xq + ((xsw ^ 4) << 4));
          }
        }
      }
      wait_vm<(s == 6 || s == 7) ? 24 : (s >= 10 && s <= 12 ? 16 + 5 * WOPRE : ((s == 15 || s == 16) ? 19 : (s == 17 ? 0 : 16)))>();
      WAIT_LGKM0();
      BAR();
      if constexpr (s < 16) issue(IC<s + 4>{});
      if constexpr (s == 2 || s == 3) {
        if (b < 170) {
          const unsigned dstb = rkb < 24 ? OFF_W2F : OFF_WOF;
          const int nks = rkb < 24 ? 48 : 20, kb = rkb < 24 ? rkb : rkb - 24;
          const int nl = 2 * lw + s - 2, nt = 8 * rnb + nl, c5 = nt / 5, jj = nt - 5 * c5;
          const unsigned rp = lds0 + RT_OFF + (nl >> 2) * 8192 + (8 * g + q4) * 128 + (((nl & 3) ^ sigl) << 5) + p4 * 8;
#pragma unroll
          for (int ks = 0; ks < 2; ++ks) {
            const bf16x8 f = cat44(trread(rp + ks * 4096), trread(rp + ks * 4096 + 512));
            st16(__builtin_bit_cast(u32x4, f), rs, lane * 16, dstb + ((c5 * nks + 2 * kb + ks) * 5 + jj) * 1024);
          }
        }
      }
      if constexpr (s == 0 || s == 10) {
        __builtin_amdgcn_s_setprio(3);
        const u32x4 c0 = __builtin_bit_cast(u32x4, mfma16(sw_[0][1], ONE8, mfma16(sw_[0][0], ONE8, z4)));
        const u32x4 c1 = __builtin_bit_cast(u32x4, mfma16(sw_[1][1], ONE8, mfma16(sw_[1][0], ONE8, z4)));
        const u32x4 cu = l15 == 0 ? c0 : c1;
        if (l15 < 2) {
          const unsigned o = OFF_CS + (((s == 10 ? 16 : 0) + cg) * 10 + rot) * 1024 + 2 * lw * 128;
          st16(u32x4{cu[0], tg, cu[1], tg}, rs, l15 * 128 + g * 32, o);
          st16(u32x4{cu[2], tg, cu[3], tg}, rs, l15 * 128 + g * 32 + 16, o);
        }
        __builtin_amdgcn_s_setprio(0);
      }
      if constexpr (s == 1) {
        __builtin_amdgcn_s_setprio(3);
#pragma unroll
        for (int i = 0; i < 2; ++i) {
          const f32x4 sx = mfma16(ONE8, sx_[i][1], mfma16(ONE8, sx_[i][0], z4));
          const f32x4 sq = mfma16(sx_[i][1], sx_[i][1], mfma16(sx_[i][0], sx_[i][0], z4));
          const int r = l15 & 3;
          const float sxx = r == 0 ? sq[0] : (r == 1 ? sq[1] : (r == 2 ? sq[2] : sq[3]));
          if (q4 == g)
            st16(u32x4{__float_as_uint(sx[0]), tg, __float_as_uint(sxx), tg}, rs, (32 * lw + 16 * i + l15) * 16,
                 OFF_RS + (rt * 10 + rot) * 2048);
        }
        __builtin_amdgcn_s_setprio(0);
      }
      if constexpr (s == 4) {
        if (lt < 12) dma1(b1 + cg * 192, lane * 16, lds0 + B1_OFF);
        if (lw == 1 && lane < 10) dma1(b2 + ct * 160, lane * 16, lds0 + B1_OFF + 256);
      }
      if constexpr (s == 6 || s == 11) {
        if (lt == 0 && (s == 6 || cg < 15)) __hip_atomic_store((GAS u64*)(ws + (s == 6 ? FL_W : FL_Q) + b * 128), tag, RLX_AGENT);
      }
      if constexpr (s == 5) {
#pragma unroll
        for (int k = 0; k < 5; ++k) gr[k] = aload16(ws, ro[k]);
      }
      if constexpr (s == 5 || s == 14) {
#pragma unroll
        for (int k = 0; k < 3; ++k) { if (s == 14) co[k] += 16 * 10240; gc[k] = aload16(ws, co[k]); }
      }
      if constexpr (s == 8) {
        __builtin_amdgcn_s_setprio(3);
#pragma unroll
        for (int k = 0; k < 5; ++k) PIN(gr[k]);
        float sx, sxx;
        if (!sum_pairs<5>(gr, 0xffffffffu, tg, sx, sxx)) regather<5>(rs, ro, 0xffffffffu, tg, sx, sxx);
        sx += __shfl_xor(sx, 32); sxx += __shfl_xor(sxx, 32);
        const float mean = sx * (1.f / 640.f);
        float var = sxx * (1.f / 640.f) - mean * mean;
        var = var < 0.f ? 0.f : var;
        if (lane < 32) *(LAS f32x2*)(smem + ST_OFF + (32 * lw + lane) * 8) = f32x2{mean, rsqrtf(var + 1e-5f)};
        col_sums(0);
        __builtin_amdgcn_s_setprio(0);
      }
      if constexpr (s == 9) {
        poll_flags(ws + fW, true, tag);
#pragma unroll
        for (int k = 0; k < WOPRE; ++k)
#pragma unroll
          for (int j = 0; j < 5; ++j) wpre[k][j] = ld16(rs, lane * 16, wb + k * 5120 + j * 1024);
      }
      if constexpr (s == 16) fl = aload8(ws, fQ);
    };
    stage(IC<0>{}); stage(IC<1>{}); stage(IC<2>{}); stage(IC<3>{}); stage(IC<4>{}); stage(IC<5>{});
    stage(IC<6>{}); stage(IC<7>{}); stage(IC<8>{}); stage(IC<9>{}); stage(IC<10>{}); stage(IC<11>{});
    stage(IC<12>{}); stage(IC<13>{}); stage(IC<14>{}); stage(IC<15>{}); stage(IC<16>{}); stage(IC<17>{});
    STAMP(2);
    PIN(fl);
    if (!__all(fl[0] == tg && fl[1] == (unsigned)(tag >> 32))) poll_flags(ws + fQ, true, tag);
    STAMP(11);
#pragma unroll
    for (int i = 0; i < 2; ++i) {
      const int seg = 2 * lw + i;
      int tb = 4 * qb - 4 + seg; tb = tb < 0 ? 0 : tb;
      dma_kv(ws + OFF_QKV + (size_t)(tb * 240 + 80 + ch) * 1024, lane * 16, lane * 16 + 81920, lds0 + K_OFF + seg * 1024);
    }
    const int l31 = lane & 31, hh = lane >> 5;
    u32x4 qraw[2][2];
#pragma unroll
    for (int a = 0; a < 2; ++a)
#pragma unroll
      for (int ks = 0; ks < 2; ++ks)
        qraw[a][ks] = __builtin_amdgcn_raw_buffer_load_b128(rs, (ks ? 2 : hh) * 1024 + (32 * a + l31) * 16,
                                                            OFF_QKV + ((4 * qb + lw) * 240 + ch) * 1024, SC1);
    col_sums(1);
    {
      const u32x4 one4 = {0x3f803f80u, 0x3f803f80u, 0x3f803f80u, 0x3f803f80u};
      *(LAS u32x4*)(smem + V_OFF + 3 * VP + lt * 16) = one4;
      if (lt < 40) *(LAS u32x4*)(smem + V_OFF + 3 * VP + 4096 + lt * 16) = one4;
      *(LAS u32x4*)(smem + K_OFF + 3 * KP + lt * 16) = u32x4{qb == 0 ? 0xff80u : 0u, 0u, 0u, 0u};
      *(LAS u32x4*)(smem + K_OFF + 3 * KP + 4096 + lt * 16) = u32x4{0u, 0u, 0u, 0u};
    }
    bf16x8 qf[2][2];
#pragma unroll
    for (int a = 0; a < 2; ++a) {
      u32x4 v0 = qraw[a][0], v1 = qraw[a][1];
      if (odd && hh == 0) { v0[0] = 0u; v0[1] = 0u; }
      if (hh) v1 = u32x4{0x3f80u, 0u, 0u, 0u};
      else if (!odd) { v1[2] = 0u; v1[3] = 0u; }
      qf[a][0] = __builtin_bit_cast(bf16x8, v0);
      qf[a][1] = __builtin_bit_cast(bf16x8, v1);
    }
    f32x16 mF, z16;
#pragma unroll
    for (int e = 0; e < 16; ++e) {
      mF[e] = (e & 3) + 8 * (e >> 2) + 4 * hh > l31 ? 0.f : -INFINITY;
      z16[e] = 0.f;
    }
    asm volatile("" : "+v"(mF), "+v"(qf[0][0]), "+v"(qf[0][1]), "+v"(qf[1][0]), "+v"(qf[1][1]));
    WAIT_VM(0);
    WAIT_LGKM0();
    lds_arrive(sync);
    lds_wait(sync, 4);
    __builtin_amdgcn_s_setprio(2);
    STAMP(5);
    unsigned kp = lds0 + K_OFF + hh * KP + (64 * lw + l31) * 16;
    const bool ones = (g & 1) && (p4 >> 1);
    unsigned vp = lds0 + V_OFF + (2 * (g & 1) + (p4 >> 1)) * VP + ((ones ? 0 : 64 * lw) + 4 * hh + q4) * 16 + (p4 & 1) * 8;
    unsigned ov = l31 * 16 + hh * 8;
    bf16x8 q0 = qf[0][0], q1 = qf[0][1];
#define LDK(T, ks) (*(LAS bf16x8*)(size_t)(kp + (T) * 512 + (ks) * 2 * KP))
#define MF32(a, b, c) __builtin_amdgcn_mfma_f32_32x32x16_bf16(a, b, c, 0, 0, 0)
#define EX2(s_, e) pk2(__builtin_amdgcn_exp2f((s_)[e]), __builtin_amdgcn_exp2f((s_)[(e) + 1]))
    auto tile_max = [&](int T) {
      f32x16 sp = MF32(LDK(T, 1), q1, MF32(LDK(T, 0), q0, z16));
      float m0 = -INFINITY, m1 = -INFINITY;
#pragma unroll
      for (int e = 0; e < 16; ++e) {
        const bool off = T == 0 ? mF[e] != 0.f : (T == 8 ? mF[e] == 0.f : false);
        sp[e] = off ? -INFINITY : sp[e];
      }
#pragma unroll
      for (int e = 0; e < 16; e += 4) { m0 = MAX3(m0, sp[e], sp[e + 1]); m1 = MAX3(m1, sp[e + 2], sp[e + 3]); }
      m0 = fmaxf(m0, m1);
      return fmaxf(m0, __shfl_xor(m0, 32));
    };
#pragma unroll 1
    for (int un = 0; un < 2; ++un) {
      float mx = tile_max(8);
      f32x16 oa;
#pragma unroll 1
      for (int pass = 0; pass < 2; ++pass) {
      f32x16 ca;
#pragma unroll
      for (int e = 0; e < 16; ++e) ca[e] = -mx;
      oa = z16;
      {
        bf16x8 kc[2][2], vf[2][2];
        f32x16 sc[2];
        u32x4 pa[2] = {{0u, 0u, 0u, 0u}, {0u, 0u, 0u, 0u}};
        kc[0][0] = LDK(0, 0); kc[0][1] = LDK(0, 1); kc[1][0] = LDK(1, 0); kc[1][1] = LDK(1, 1);
        sc[0] = MF32(kc[0][1], q1, MF32(kc[0][0], q0, ca + mF));
        sc[1] = z16;
        asm volatile("" :: "v"(sc[0]));
        SB();
#pragma unroll
        for (int T = 0; T < 10; ++T) {
          const int c = T & 1, n = c ^ 1;
          if (T < 9) {
#pragma unroll
            for (int ks = 0; ks < 2; ++ks) vf[c][ks] = cat44(trread(vp + T * 512 + ks * 256), trread(vp + T * 512 + ks * 256 + 128));
          }
          if (T < 8) sc[n] = MF32(kc[n][1], q1, MF32(kc[n][0], q0, ca));
          if (T < 7) { kc[c][0] = LDK(T + 2, 0); kc[c][1] = LDK(T + 2, 1); }
          if (T > 0) oa = MF32(vf[n][1], __builtin_bit_cast(bf16x8, pa[1]), MF32(vf[n][0], __builtin_bit_cast(bf16x8, pa[0]), oa));
          if (T == 8) {
#pragma unroll
            for (int e = 0; e < 16; ++e) sc[c][e] = mF[e] == 0.f ? -INFINITY : sc[c][e];
          }
          if (T < 9) {
#pragma unroll
            for (int e = 0; e < 4; ++e) { pa[0][e] = EX2(sc[c], 2 * e); pa[1][e] = EX2(sc[c], 8 + 2 * e); }
          }
          SGB(0x100, 6);
          if (T == 8) SGB(0x2, 16);
          SGB(0x8, 1); SGB(0x400, 4); SGB(0x2, 1);
#pragma unroll
          for (int k = 0; k < 3; ++k) { SGB(0x8, 1); SGB(0x400, 2); SGB(0x2, 1); SGB(0x400, 2); SGB(0x2, 1); }
          SGB(0x2, 1);
          asm volatile("" :: "v"(sc[n]), "v"(oa), "v"(pa[0]), "v"(pa[1]));
          SB();
        }
      }
      if (pass || __all(oa[12] < 1.8446744e19f)) break;
#pragma unroll 1
      for (int T = 0; T < 8; ++T) mx = fmaxf(mx, tile_max(T));
      }
      const float il = __builtin_amdgcn_rcpf(oa[12]);
#pragma unroll
      for (int pl = 0; pl < 3; ++pl) {
        const bool ok = pl == 1 || (pl == 0 ? !(odd && hh == 0) : (odd || hh == 0));
        if (ok) {
          const u32x2 pkd = {pk2(oa[4 * pl] * il, oa[4 * pl + 1] * il), pk2(oa[4 * pl + 2] * il, oa[4 * pl + 3] * il)};
          __builtin_amdgcn_raw_buffer_store_b64(pkd, rs, pl * 1024 + ov, OFF_O + ((4 * qb + lw) * 80 + ch) * 1024, SC1);
        }
      }
      kp += 512; vp += ones ? 0 : 512; ov += 512;
      q0 = qf[1][0]; q1 = qf[1][1];
    }
    __builtin_amdgcn_s_setprio(0);
    lds_arrive(sync + 4);
    STAMP(6);
    WAIT_VM(0);
    if (lane == 0) __hip_atomic_store((GAS u64*)(ws + FL_O + (b * 4 + lw) * 128), tag, RLX_AGENT);
#pragma unroll
    for (int k = WOPRE; k < 5; ++k)
#pragma unroll
      for (int j = 0; j < 5; ++j) wpre[k][j] = ld16(rs, lane * 16, wb + k * 5120 + j * 1024);
    const unsigned orow = orow_f();
#pragma unroll
    for (int j = 0; j < 2; ++j) xv[j] = *(const u32x2*)(x + orow + (3 + j) * 32);
    const unsigned ab = OFF_O + (mt * 80 + 20 * lw) * 1024;
    {
      const int hd = 8 * lw + (lane & 7);
      poll_flags(ws + FL_O + ((((((mt >> 2) << 2) | (hd & 3)) << 3) | (hd >> 2)) * 4 + (mt & 3)) * 128, lane < 8, tag);
    }
    STAMP(7);
    bf16x8 af[4][5];
#pragma unroll
    for (int i = 0; i < 4; ++i)
#pragma unroll
      for (int k = 0; k < 5; ++k) af[i][k] = ld16(rs, va + ((lw + 1 + i) & 3) * 256, ab + k * 4096);
    SB();
    lds_wait(sync + 4, 4);
    lds_wait(sync + 6, 4);
    f32x4 own[2];
#pragma unroll
    for (int i = 0; i < 4; ++i) {
      const int mi = (lw + 1 + i) & 3;
      f32x4 a5[5] = {z4, z4, z4, z4, z4};
#pragma unroll
      for (int k = 0; k < 5; ++k)
#pragma unroll
        for (int j = 0; j < 5; ++j) a5[j] = mfma16(wpre[k][j], af[i][k], a5[j]);
#pragma unroll
      for (int j = 0; j < 3; ++j) *(LAS f32x4*)(smem + REDS1_OFF + ((mi * 4 + lw) * 3 + j) * 1024 + lane * 16) = a5[j];
      if (i < 3) {
#pragma unroll
        for (int j = 0; j < 2; ++j) *(LAS f32x4*)(smem + REDS2_OFF + ((mi * 3 + 2 - i) * 2 + j) * 1024 + lane * 16) = a5[3 + j];
      } else { own[0] = a5[3]; own[1] = a5[4]; }
    }
    STAMP(8);
    __syncthreads();
    STAMP(9);
#pragma unroll
    for (int j = 0; j < 2; ++j) {
      f32x4 sv = own[j];
#pragma unroll
      for (int k = 0; k < 3; ++k) sv += *(LAS f32x4*)(smem + REDS2_OFF + ((lw * 3 + k) * 2 + j) * 1024 + lane * 16);
      const f32x4 gt = *(LAS f32x4*)(smem + GX_OFF + (lw * 2 + j) * 1024 + lane * 16);
      *(u32x2*)(out + orow + (3 + j) * 32) = fin4(xv[j], sv, gt);
    }

  } else {
    if (idx * 4 + w < CODE_KB) {
      const u32x4 t0 = *(const u32x4*)(pc0 + (idx * 4 + w) * 1024 + lane * 16);
      asm volatile("" :: "v"(t0));
    }
    {
      const int mw = w & 1, nw = w >> 1;
      unsigned xa[2];
      xa[0] = lds0 + (64 * mw + l15) * 128 + (xsw << 4);
      xa[1] = lds0 + (64 * mw + l15) * 128 + ((xsw ^ 4) << 4);
      const unsigned wl = lds0 + 16384 + (8 * g + q4) * 128 + p4 * 8;
      const unsigned vo = (g >> 1) * 1024 + (16 * (g & 1) + l15) * 16;
      float sh[4], sc[4];
      BAR();
      STAMP(1);
      mfma_part<4, 0>(xa, wl, 4 * nw, sigl, lds0, sh, sc, 64 * mw + l15, cg < 5 ? 0.22360679774997896f * 1.4426950408889634f : 1.f, rs, vo,
                      cg < 15 ? OFF_QKV + ((2 * rt + mw) * 240 + 16 * cg + 8 * nw) * 1024 : 0x80000000u, 4 * nw, g, ws);
      STAMP(14);
      mfma_part<3, 10>(xa, wl, lu1 + 3 * nw, sigl, lds0, sh, sc, 0, 1.f, rs, vo,
                       OFF_H + ((2 * rt + mw) * 192 + 12 * cg + 6 * nw) * 1024, 3 * nw, g, ws);
    }
    STAMP(3);
    WAIT_VM(0);
    if (lds_arrive(sync + 2) == 3 && lane == 0) __hip_atomic_store((GAS u64*)(ws + FL_H + b * 128), tag, RLX_AGENT);
    STAMP(4);
#pragma unroll
    for (int k = 0; k < 4; ++k)
#pragma unroll
      for (int j = 0; j < 5; ++j) wpre[k][j] = ld16(rs, lane * 16, wb + k * 5120 + j * 1024);
    const unsigned wl_ = lds0 + slab_of(w);
    lds_wait(sync + 2, 4);
    dma5_sc1(ws + wb + 4 * 5120, lane * 16, wl_);
    dma5_sc1(ws + wb + 5 * 5120, lane * 16, wl_ + 5120);
    const unsigned ab = OFF_H + (mt * 192 + 48 * w) * 1024;
    if (b == 0 && w == 0) {
#pragma unroll 1
      for (int spins = 0; spins < (1 << 20); ++spins) {
        u64 v[4];
#pragma unroll
        for (int i = 0; i < 4; ++i) v[i] = __hip_atomic_load((const GAS u64*)(ws + FL_W + (lane + 64 * i) * 128), RLX_AGENT);
        if (__all(((v[0] ^ tag) | (v[1] ^ tag) | (v[2] ^ tag) | (v[3] ^ tag)) == 0)) break;
        __builtin_amdgcn_s_sleep(1);
      }
      if (lane == 0) __hip_atomic_store((GAS u64*)(ws + OFF_FL), tag, RLX_AGENT);
    }
    poll_flags(ws + FL_H + wg_of(mt >> 1, 4 * w + (lane & 3)) * 128, lane < 4, tag);
    STAMP(7);
    unsigned vi[4];
#pragma unroll
    for (int i = 0; i < 4; ++i) vi[i] = va + ((w + 1 + i) & 3) * 256;
    dma5_sc1(ws + ab + 4 * 4096, lane * 16, wl_ + 10240);
    dma5_sc1(ws + ab + 4 * 4096 + 3072, lane * 16, wl_ + 10240 + 3072);
    bf16x8 af[4][4];
#pragma unroll
    for (int k = 0; k < 4; ++k)
#pragma unroll
      for (int i = 0; i < 4; ++i) af[k][i] = ld16(rs, vi[i], ab + k * 4096);
    SB();
#pragma unroll
    for (int i = 0; i < 4; ++i)
#pragma unroll
      for (int j = 0; j < 5; ++j) acc[i][j] = f32x4{0.f, 0.f, 0.f, 0.f};
#pragma unroll
    for (int k = 0; k < 12; ++k) {
      const int cs = k & 3;
      if (k == 6)
        asm volatile("s_mov_b64 exec, 1\n\tds_add_u32 %0, %1\n\ts_mov_b64 exec, -1" :: "v"(lds0 + SYNC_OFF + 24), "v"(1u) : "memory");
      if (k == W2HOLD) {
        unsigned tv, ts;
        asm volatile(".Lp%=:\n\tds_read_b32 %0, %2\n\ts_waitcnt lgkmcnt(0)\n\tv_readfirstlane_b32 %1, %0\n\t"
                     "s_cmp_ge_u32 %1, 4\n\ts_cbranch_scc1 .Le%=\n\ts_sleep 2\n\ts_branch .Lp%=\n.Le%=:"
                     : "=&v"(tv), "=&s"(ts) : "v"(lds0 + SYNC_OFF + 16) : "memory", "scc");
      }
#pragma unroll
      for (int i = 0; i < 4; ++i) {
#pragma unroll
        for (int j = 0; j < 5; ++j) acc[i][j] = mfma16(wpre[cs][j], af[cs][i], acc[i][j]);
        if (k == 11 && i < 3) {
#pragma unroll
          for (int j = 0; j < 5; ++j)
            *(LAS f32x4*)(smem + REDG_OFF + ((((w + 1 + i) & 3) * 3 + 2 - i) * 5 + j) * 1024 + lane * 16) = acc[i][j];
        }
      }
      SB();
      if (k < 2) {
#pragma unroll
        for (int j = 0; j < 5; ++j) wpre[cs][j] = *(LAS bf16x8*)(size_t)(wl_ + k * 5120 + j * 1024 + lane * 16);
#pragma unroll
        for (int i = 0; i < 4; ++i) af[cs][i] = *(LAS bf16x8*)(size_t)(wl_ + 10240 + k * 4096 + vi[i]);
      } else if (k < 8) {
#pragma unroll
        for (int i = 0; i < 4; ++i) af[cs][i] = ld16(rs, vi[i], ab + (k + 4) * 4096);
#pragma unroll
        for (int j = 0; j < 5; ++j) wpre[cs][j] = ld16(rs, lane * 16, wb + (k + 4) * 5120 + j * 1024);
      }
      if (k == 10) {
        const unsigned orow = orow_f();
#pragma unroll
        for (int j = 0; j < 3; ++j) xv[j] = *(const u32x2*)(x + orow + j * 32);
      }
      SB();
    }
    STAMP(8);
    u32x2 bb[5];
#pragma unroll
    for (int j = 0; j < 5; ++j) bb[j] = *(LAS u32x2*)(smem + B1_OFF + 256 + (16 * j + 4 * g) * 2);
    WAIT_LGKM0();
    lds_arrive(sync + 3);
    lds_wait(sync + 3, 4);
    f32x4 gt[5];
#pragma unroll
    for (int j = 0; j < 5; ++j) {
      gt[j] = acc[3][j];
#pragma unroll
      for (int k = 0; k < 3; ++k) gt[j] += *(LAS f32x4*)(smem + REDG_OFF + ((w * 3 + k) * 5 + j) * 1024 + lane * 16);
    }
#pragma unroll
    for (int j = 0; j < 5; ++j) {
      const float bv[4] = {bflo(bb[j][0]), bfhi(bb[j][0]), bflo(bb[j][1]), bfhi(bb[j][1])};
#pragma unroll
      for (int e = 0; e < 4; ++e) {
        const float t = rbf(rbf(gt[j][e]) + bv[e]);
        gt[j][e] = rbf(t * __builtin_amdgcn_rcpf(1.f + __expf(-t)));
      }
      if (j >= 3) *(LAS f32x4*)(smem + GX_OFF + (w * 2 + j - 3) * 1024 + lane * 16) = gt[j];
    }
    __syncthreads();
    STAMP(9);
    const unsigned orow = orow_f();
#pragma unroll
    for (int j = 0; j < 3; ++j) {
      f32x4 sv = *(LAS f32x4*)(smem + REDS1_OFF + ((w * 4) * 3 + j) * 1024 + lane * 16);
#pragma unroll
      for (int k = 1; k < 4; ++k) sv += *(LAS f32x4*)(smem + REDS1_OFF + ((w * 4 + k) * 3 + j) * 1024 + lane * 16);
      *(u32x2*)(out + orow + j * 32) = fin4(xv[j], sv, gt[j]);
    }
  STAMP(10);
    __builtin_amdgcn_endpgm();
  }
  STAMP(10);
}

extern "C" void kernel_launch(void* const* d_in, const int* in_sizes, int n_in, void* d_out, int out_size, void* d_ws,
                              size_t ws_size, hipStream_t stream) {
  (void)hipFuncSetAttribute((const void*)k_all, hipFuncAttributeMaxDynamicSharedMemorySize, LDS_ALL);
  k_all<<<256, 512, LDS_ALL, stream>>>((const char*)d_in[0], (const char*)d_in[1], (const char*)d_in[4], (const char*)d_in[5],
                                       (const char*)d_in[6], (const char*)d_in[2], (const char*)d_in[7], (char*)d_ws, (char*)d_out);
}
```

```cpp
#include <hip/hip_runtime.h>
#include <stdint.h>

#define LAS __attribute__((address_space(3)))
#define GAS __attribute__((address_space(1)))
#define CAS __attribute__((address_space(4)))
typedef short bf16x8 __attribute__((ext_vector_type(8)));
typedef short s16x4 __attribute__((ext_vector_type(4)));
typedef float f32x4 __attribute__((ext_vector_type(4)));
typedef unsigned u32x4 __attribute__((ext_vector_type(4)));
typedef unsigned u32x2 __attribute__((ext_vector_type(2)));
typedef __bf16 bf16x2_t __attribute__((ext_vector_type(2)));
typedef float f32x2 __attribute__((ext_vector_type(2)));
typedef float f32x16 __attribute__((ext_vector_type(16)));
typedef unsigned long long u64;

constexpr unsigned OFF_FL = 0;
constexpr unsigned FL_W = 1024, FL_Q = FL_W + 32768, FL_H = FL_Q + 32768, FL_O = FL_H + 32768;
constexpr unsigned OFF_RS = 262144;
constexpr unsigned OFF_CS = OFF_RS + 16 * 10 * 2048;
constexpr unsigned OFF_QKV = OFF_CS + 2 * 16 * 10 * 1024;
constexpr unsigned OFF_H = OFF_QKV + 32 * 240 * 1024;
constexpr unsigned OFF_O = OFF_H + 32 * 192 * 1024;
constexpr unsigned OFF_W2F = OFF_O + 32 * 80 * 1024;
constexpr unsigned OFF_WOF = OFF_W2F + 8 * 48 * 5120;
constexpr unsigned WS_USED = OFF_WOF + 8 * 20 * 5120;

__device__ __forceinline__ f32x4 mfma16(bf16x8 a, bf16x8 b, f32x4 c) {
  return __builtin_amdgcn_mfma_f32_16x16x32_bf16(a, b, c, 0, 0, 0);
}
__device__ __forceinline__ unsigned pk2(float a, float b) {
  f32x2 v = {a, b};
  bf16x2_t r = __builtin_convertvector(v, bf16x2_t);
  return __builtin_bit_cast(unsigned, r);
}
__device__ __forceinline__ float rbf(float a) { return __builtin_bit_cast(float, pk2(0.f, a) & 0xffff0000u); }
__device__ __forceinline__ float bflo(unsigned u) { return __builtin_bit_cast(float, u << 16); }
__device__ __forceinline__ float bfhi(unsigned u) { return __builtin_bit_cast(float, u & 0xffff0000u); }
__device__ __forceinline__ u32x2 fin4(u32x2 xv, f32x4 sv, f32x4 gt) {
  return u32x2{pk2(bflo(xv[0]) + rbf(rbf(sv[0]) * gt[0]), bfhi(xv[0]) + rbf(rbf(sv[1]) * gt[1])),
               pk2(bflo(xv[1]) + rbf(rbf(sv[2]) * gt[2]), bfhi(xv[1]) + rbf(rbf(sv[3]) * gt[3]))};
}
__device__ __forceinline__ s16x4 trread(unsigned a) {
  return __builtin_amdgcn_ds_read_tr16_b64_v4i16((LAS s16x4*)(size_t)a);
}
__device__ __forceinline__ bf16x8 cat44(s16x4 a, s16x4 b) {
  return __builtin_shufflevector(a, b, 0, 1, 2, 3, 4, 5, 6, 7);
}
#define WAIT_VM(n) asm volatile("s_waitcnt vmcnt(" #n ")" ::: "memory")
#define WAIT_LGKM0() __builtin_amdgcn_s_waitcnt(0xC07F)
#define BAR() __builtin_amdgcn_s_barrier()
#define SB() __builtin_amdgcn_sched_barrier(0)
#define SGB(m, n) __builtin_amdgcn_sched_group_barrier(m, n, 0)
#define MAX3(a, b, c) __builtin_fmaxf(__builtin_fmaxf(a, b), c)
#define RLX_AGENT __ATOMIC_RELAXED, __HIP_MEMORY_SCOPE_AGENT
constexpr int SC1 = 16;
constexpr int CODE_KB = 38;
template <int N> struct IC { static constexpr int value = N; };

__device__ __forceinline__ void dma1(const char* sbase, unsigned voff, unsigned lds) {
  unsigned keep;
  asm volatile("s_mov_b32 %0, m0\n\ts_mov_b32 m0, %3\n\ts_nop 2\n\tglobal_load_lds_dwordx4 %1, %2\n\ts_mov_b32 m0, %0"
               : "=&s"(keep) : "v"(voff), "s"(sbase), "s"(lds) : "memory");
}
__device__ __forceinline__ void dma_stage(const char* sa, unsigned va0, unsigned va1, unsigned va2, unsigned va3, unsigned la,
                                          const char* sb, unsigned vb0, unsigned vb1, unsigned lb) {
  unsigned keep;
  asm volatile("s_mov_b32 %0, m0\n\ts_mov_b32 m0, %10\n\ts_nop 2\n\tglobal_load_lds_dwordx4 %8, %7\n\t"
               "s_add_u32 m0, m0, 1024\n\ts_nop 0\n\tglobal_load_lds_dwordx4 %9, %7\n\t"
               "s_add_u32 m0, m0, 7040\n\ts_nop 0\n\tglobal_load_lds_dwordx4 %8, %7 offset:128\n\t"
               "s_add_u32 m0, m0, 1024\n\ts_nop 0\n\tglobal_load_lds_dwordx4 %9, %7 offset:128\n\t"
               "s_mov_b32 m0, %6\n\ts_nop 0\n\tglobal_load_lds_dwordx4 %2, %1\n\t"
               "s_add_u32 m0, m0, 1024\n\ts_nop 0\n\tglobal_load_lds_dwordx4 %3, %1\n\t"
               "s_add_u32 m0, m0, 1024\n\ts_nop 0\n\tglobal_load_lds_dwordx4 %4, %1\n\t"
               "s_add_u32 m0, m0, 1024\n\ts_nop 0\n\tglobal_load_lds_dwordx4 %5, %1\n\ts_mov_b32 m0, %0"
               : "=&s"(keep) : "s"(sa), "v"(va0), "v"(va1), "v"(va2), "v"(va3), "s"(la), "s"(sb), "v"(vb0), "v"(vb1), "s"(lb)
               : "memory", "scc");
}
__device__ __forceinline__ void dma_kv(const char* sb, unsigned vk, unsigned vv, unsigned l) {
  unsigned keep;
  asm volatile("s_mov_b32 %0, m0\n\ts_mov_b32 m0, %4\n\ts_nop 2\n\tglobal_load_lds_dwordx4 %2, %1 sc1\n\t"
               "s_add_u32 m0, m0, 7168\n\ts_nop 0\n\tglobal_load_lds_dwordx4 %2, %1 offset:1024 sc1\n\t"
               "s_add_u32 m0, m0, 7168\n\ts_nop 0\n\tglobal_load_lds_dwordx4 %2, %1 offset:2048 sc1\n\t"
               "s_add_u32 m0, m0, 18432\n\ts_nop 0\n\tglobal_load_lds_dwordx4 %3, %1 sc1\n\t"
               "s_add_u32 m0, m0, 7232\n\ts_nop 0\n\tglobal_load_lds_dwordx4 %3, %1 offset:1024 sc1\n\t"
               "s_add_u32 m0, m0, 7232\n\ts_nop 0\n\tglobal_load_lds_dwordx4 %3, %1 offset:2048 sc1\n\t"
               "s_mov_b32 m0, %0"
               : "=&s"(keep) : "s"(sb), "v"(vk), "v"(vv), "s"(l) : "memory", "scc");
}
__device__ __forceinline__ void dma5_sc1(const char* sb, unsigned v, unsigned l) {
  unsigned keep;
  asm volatile("s_mov_b32 %0, m0\n\ts_mov_b32 m0, %3\n\ts_nop 2\n\tglobal_load_lds_dwordx4 %2, %1 sc1\n\t"
               "global_load_lds_dwordx4 %2, %1 offset:1024 sc1\n\tglobal_load_lds_dwordx4 %2, %1 offset:2048 sc1\n\t"
               "global_load_lds_dwordx4 %2, %1 offset:3072 sc1\n\ts_add_u32 m0, m0, 1024\n\ts_nop 0\n\t"
               "global_load_lds_dwordx4 %4, %1 offset:3072 sc1\n\ts_mov_b32 m0, %0"
               : "=&s"(keep) : "s"(sb), "v"(v), "s"(l), "v"(v + 1024) : "memory", "scc");
}
__device__ __forceinline__ u32x2 aload8(const char* sbase, unsigned voff) {
  u32x2 r;
  asm volatile("s_nop 4\n\tglobal_load_dwordx2 %0, %1, %2 sc1" : "=v"(r) : "v"(voff), "s"(sbase) : "memory");
  return r;
}
__device__ __forceinline__ u32x4 aload16(const char* sbase, unsigned voff) {
  u32x4 r;
  asm volatile("s_nop 4\n\tglobal_load_dwordx4 %0, %1, %2 sc1" : "=v"(r) : "v"(voff), "s"(sbase) : "memory");
  return r;
}
#define PIN(x) asm volatile("" : "+v"(x))
template <int N> __device__ __forceinline__ void wait_vm() { asm volatile("s_waitcnt vmcnt(%0)" :: "i"(N) : "memory"); }
__device__ __forceinline__ void poll_flags(const char* flag, bool need, u64 tag) {
#pragma unroll 1
  for (int spins = 0; spins < (1 << 20); ++spins) {
    const u64 v = need ? __hip_atomic_load((const GAS u64*)flag, RLX_AGENT) : tag;
    if (__all(v == tag)) break;
    __builtin_amdgcn_s_sleep(1);
  }
  __builtin_amdgcn_fence(__ATOMIC_ACQUIRE, "wavefront");
}
__device__ __forceinline__ unsigned lds_arrive(LAS unsigned* c) {
  unsigned r = 0;
  if ((threadIdx.x & 63) == 0) r = __hip_atomic_fetch_add(c, 1u, __ATOMIC_RELAXED, __HIP_MEMORY_SCOPE_WORKGROUP);
  return __builtin_amdgcn_readfirstlane(r);
}
__device__ __forceinline__ void lds_wait(LAS unsigned* c, unsigned n) {
#pragma unroll 1
  for (int spins = 0; spins < (1 << 22); ++spins) {
    if (__builtin_amdgcn_readfirstlane(__hip_atomic_load(c, __ATOMIC_RELAXED, __HIP_MEMORY_SCOPE_WORKGROUP)) >= n) break;
  }
  __builtin_amdgcn_fence(__ATOMIC_ACQUIRE, "wavefront");
}

constexpr int WOPRE = 4;
constexpr int W2HOLD = 10;
constexpr int STG = 32768, RT_OFF = 4 * STG, LDS_ALL = 163840;
constexpr int B1_OFF = 159744;
constexpr int ST_OFF = 160768;
constexpr int CS_OFF = ST_OFF + 1024, SYNC_OFF = ST_OFF + 2048;
constexpr int KP = 8192, VP = 8192 + 64;
constexpr int K_OFF = 0, V_OFF = K_OFF + 4 * KP;
__device__ __forceinline__ constexpr int slab_of(int w) { return 65536 + w * 20480; }
constexpr int REDG_OFF = 0, GX_OFF = 61440, REDS1_OFF = GX_OFF + 8192, REDS2_OFF = REDS1_OFF + 49152;

__device__ __forceinline__ void st16(u32x4 v, __amdgpu_buffer_rsrc_t rs, unsigned voff, unsigned soff) {
  __builtin_amdgcn_raw_buffer_store_b128(v, rs, voff + soff, 0, SC1);
}
__device__ __forceinline__ bf16x8 ld16(__amdgpu_buffer_rsrc_t rs, unsigned voff, unsigned soff) {
  return __builtin_bit_cast(bf16x8, __builtin_amdgcn_raw_buffer_load_b128(rs, voff, soff, SC1));
}
__device__ __forceinline__ int wg_of(int rt, int cg) { return (((cg & 3) * 8 + (rt & 7)) << 3) | ((cg >> 2) << 1) | (rt >> 3); }
template <int N>
__device__ __forceinline__ bool sum_pairs(const u32x4 (&v)[N], unsigned last_on, unsigned tg, float& a, float& b) {
  bool ok = true;
  a = 0.f; b = 0.f;
#pragma unroll
  for (int k = 0; k < N; ++k) {
    ok = ok && v[k][1] == tg && v[k][3] == tg;
    const unsigned m = k == N - 1 ? last_on : 0xffffffffu;
    a += __uint_as_float(v[k][0] & m);
    b += __uint_as_float(v[k][2] & m);
  }
  return __all(ok);
}
template <int N>
__device__ __forceinline__ void regather(__amdgpu_buffer_rsrc_t rs, const unsigned (&vo)[N], unsigned last_on, unsigned tg, float& a, float& b) {
#pragma unroll 1
  for (int spins = 0; spins < (1 << 20); ++spins) {
    u32x4 v[N];
#pragma unroll
    for (int k = 0; k < N; ++k) v[k] = __builtin_amdgcn_raw_buffer_load_b128(rs, vo[k], 0, SC1);
    if (sum_pairs<N>(v, last_on, tg, a, b)) break;
    __builtin_amdgcn_s_sleep(2);
  }
}

#define LDX(set, i, ks) xf[set][i] = *(LAS bf16x8*)(size_t)(xa[ks] + (i) * 2048)
#define LDW(set, j, ks) wf[set][j] = cat44(trread(wa[j] + (ks) * 4096), trread(wa[j] + (ks) * 4096 + 512))
#define HALF(set, ks) do { \
    _Pragma("unroll") for (int j = 0; j < NT; ++j) { \
      if (j == 0) { _Pragma("unroll") for (int i = 0; i < 4; ++i) LDX((set) ^ 1, i, ks); } \
      else if (2 * j - 2 < NT) { LDW((set) ^ 1, 2 * j - 2, ks); if (2 * j - 1 < NT) LDW((set) ^ 1, 2 * j - 1, ks); } \
      _Pragma("unroll") for (int i = 0; i < 4; ++i) acc[i][j] = mfma16(wf[set][j], xf[set][i], acc[i][j]); \
      SGB(0x8, 1); SGB(0x100, 1); SGB(0x8, 1); SGB(0x100, 1); SGB(0x8, 1); SGB(0x100, 1); SGB(0x8, 1); SGB(0x100, 1); \
      asm volatile("" :: "v"(acc[0][j]), "v"(acc[1][j]), "v"(acc[2][j]), "v"(acc[3][j])); \
      SB(); \
    } } while (0)
template <int NT, int S0>
__device__ __forceinline__ void mfma_part(unsigned (&xa)[2], unsigned wl, int tl0, int sigl, unsigned lds0, float (&sh)[4], float (&sc)[4],
                                          int row0, float qs, __amdgpu_buffer_rsrc_t rs, unsigned vo, unsigned so, int cs0, int g) {
  constexpr bool ISH = S0 == 10;
  unsigned wa[NT];
  f32x4 acc[4][NT];
#pragma unroll
  for (int j = 0; j < NT; ++j) {
    const int tl = tl0 + j;
    wa[j] = wl + (S0 & 3) * STG + (tl >> 2) * 8192 + (((tl & 3) ^ sigl) << 5);
#pragma unroll
    for (int i = 0; i < 4; ++i) acc[i][j] = f32x4{0.f, 0.f, 0.f, 0.f};
  }
  bf16x8 xf[2][4], wf[2][NT];
#pragma unroll
  for (int i = 0; i < 4; ++i) LDX(0, i, 0);
#pragma unroll
  for (int j = 0; j < NT; ++j) LDW(0, j, 0);
  WAIT_LGKM0();
  int buf = S0 & 3;
#pragma unroll 1
  for (int t = 0; t < 10; ++t) {
    HALF(0, 1);
    {
      const int d = buf == 3 ? -3 * STG : STG;
      buf = (buf + 1) & 3;
      xa[0] += d; xa[1] += d;
#pragma unroll
      for (int j = 0; j < NT; ++j) wa[j] += d;
    }
    WAIT_LGKM0();
    if (ISH) {
      asm volatile("s_cmp_lg_u32 %0, 1\n\ts_cbranch_scc1 .Lk%=\n\ts_waitcnt vmcnt(0)\n.Lk%=:\n\t"
                   "s_cmp_ge_u32 %0, 8\n\ts_cbranch_scc1 .Lc%=\n\ts_barrier\n.Lc%=:" :: "s"(t) : "memory", "scc");
    } else BAR();
    SB();
    HALF(1, 0);
    WAIT_LGKM0();
  }
  lds_wait((LAS unsigned*)(size_t)(lds0 + SYNC_OFF + (ISH ? 32 : 28)), 4);
  if (!ISH) {
#pragma unroll
    for (int i = 0; i < 4; ++i) {
      const f32x2 st = *(LAS f32x2*)(size_t)(lds0 + ST_OFF + (row0 + 16 * i) * 8);
      sc[i] = st[1]; sh[i] = -st[0] * st[1];
    }
  }
#pragma unroll
  for (int j = 0; j < NT; ++j) {
    const f32x4 c4 = *(LAS f32x4*)(size_t)(lds0 + CS_OFF + (ISH ? 512 : 0) + ((cs0 + j) * 16 + 4 * g) * 4);
    f32x4 bv = {0.f, 0.f, 0.f, 0.f};
    if (ISH) {
      const u32x2 bb = *(LAS u32x2*)(size_t)(lds0 + B1_OFF + ((cs0 + j) * 16 + 4 * g) * 2);
      bv = f32x4{bflo(bb[0]), bfhi(bb[0]), bflo(bb[1]), bfhi(bb[1])};
    }
#pragma unroll
    for (int pp = 0; pp < 2; ++pp) {
      float v[2][4];
#pragma unroll
      for (int i = 0; i < 2; ++i)
#pragma unroll
        for (int r = 0; r < 4; ++r) {
          float t = __builtin_fmaf(sc[2 * pp + i], acc[2 * pp + i][j][r], __builtin_fmaf(sh[2 * pp + i], c4[r], bv[r]));
          if (ISH) { t = t > 0.f ? t : 0.f; t = t * t; } else t *= qs;
          v[i][r] = t;
        }
      const unsigned x0 = pk2(v[0][0], v[0][1]), x1 = pk2(v[0][2], v[0][3]);
      const unsigned y0 = pk2(v[1][0], v[1][1]), y1 = pk2(v[1][2], v[1][3]);
      const auto s0 = __builtin_amdgcn_permlane16_swap(x0, y0, false, false);
      const auto s1 = __builtin_amdgcn_permlane16_swap(x1, y1, false, false);
      const u32x4 o = {s0[0], s1[0], s0[1], s1[1]};
      st16(o, rs, vo + pp * 512, so + 2 * j * 1024);
    }
  }
}

__global__ __launch_bounds__(512, 2) void k_all(const char* __restrict__ x, const char* __restrict__ wqkv, const char* __restrict__ w1,
                                                const char* __restrict__ b1, const char* __restrict__ w2, const char* __restrict__ wo,
                                                const char* __restrict__ b2, char* ws, char* __restrict__ out) {
  extern __shared__ __attribute__((aligned(16))) char smem_[];
  LAS char* smem = (LAS char*)smem_;
  const int tid = threadIdx.x, lane = tid & 63, w = __builtin_amdgcn_readfirstlane(tid >> 6);
  const int b = blockIdx.x, xcd = b & 7, idx = b >> 3;
  const unsigned lds0 = (unsigned)(size_t)smem;
  asm volatile("" :: "s"(x), "s"(wqkv), "s"(w1), "s"(b1), "s"(w2), "s"(wo), "s"(ws));
  u64 pc_;
  asm volatile("s_getpc_b64 %0" : "=s"(pc_));
  const int rt = (xcd & 1) * 8 + (idx & 7), cg = (xcd >> 1) * 4 + (idx >> 3);
  const int rot = (rt + 3 * cg) % 10;
  const int lw = w - 4, lr = lane >> 3, ls = lane & 7;
  const int bk = 16 * lw + lr, sw0 = (ls ^ (bk & 2)) << 4, sw1 = sw0 ^ 64;
  const unsigned vq0 = bk * 3840 + (cg < 15 ? cg : 14) * 256 + sw0, vq1 = (bk + 8) * 3840 + (cg < 15 ? cg : 14) * 256 + sw1;
  const unsigned vA = (rt * 128 + 32 * lw + lr) * 1280 + ((ls ^ (lr >> 1)) << 4);
  const unsigned vA1 = (vA + 10240) ^ 64u, vA2 = vA + 20480, vA3 = (vA + 30720) ^ 64u;
  const int rkb = b / 5, rnb = b - 5 * rkb;
  if (w >= 4) {
    dma_stage(x + rot * 128, vA, vA1, vA2, vA3, lds0 + lw * 4096, wqkv + (size_t)rot * (64 * 3840), vq0, vq1, lds0 + 16384 + lw * 2048);
    if (b < 170) {
      const char* Wr = rkb < 24 ? w2 + (size_t)rkb * 64 * 1280 : wo + (size_t)(rkb - 24) * 64 * 1280;
      const unsigned r0 = bk * 1280 + rnb * 256 + sw0, r1 = (bk + 8) * 1280 + rnb * 256 + sw1;
      dma1(Wr, r0, lds0 + RT_OFF + lw * 2048);
      dma1(Wr, r1, lds0 + RT_OFF + lw * 2048 + 1024);
      dma1(Wr + 128, r0, lds0 + RT_OFF + 8192 + lw * 2048);
      dma1(Wr + 128, r1, lds0 + RT_OFF + 8192 + lw * 2048 + 1024);
    }
    const int kt = rot == 9 ? 0 : rot + 1;
    dma_stage(x + kt * 128, vA, vA1, vA2, vA3, lds0 + STG + lw * 4096, wqkv + (size_t)kt * (64 * 3840), vq0, vq1,
              lds0 + STG + 16384 + lw * 2048);
  }
  SB();
  const int g = lane >> 4, l15 = lane & 15, q4 = l15 >> 2, p4 = lane & 3;
  const char* pc0 = (const char*)(pc_ & ~(u64)63);
  const __amdgpu_buffer_rsrc_t rs = __builtin_amdgcn_make_buffer_rsrc(ws, 0, WS_USED, 0x00020000);
  LAS unsigned* sync = (LAS unsigned*)(smem + SYNC_OFF);
  u64 tag;
  {
    const char* p = ws;
    asm volatile("" : "+s"(p));
    tag = *(const CAS u64*)(p + OFF_FL) + 1;
  }
  const int lu1 = (6 * cg) & 3;
  const int sigl = ((q4 >> 1) & 1) | ((g & 1) << 1), xsw = g ^ (l15 >> 1);
  const int mt = (xcd >> 1) * 8 + (idx & 7), ct = (xcd & 1) * 4 + (idx >> 3);
  const int head = 4 * xcd + (idx & 3), qb = idx >> 2;
  const int ch = (5 * head) >> 1, odd = head & 1;
  auto orow_f = [&]() {
    unsigned ln = lane;
    asm volatile("" : "+v"(ln));
    return (64 * mt + 16 * (w & 3) + (ln & 15)) * 1280 + (80 * ct + 4 * (ln >> 4)) * 2;
  };
  u32x2 xv[3];
  f32x4 acc[4][5];
  const unsigned va = g * 1024 + l15 * 16;
  bf16x8 wpre[5][5];
  const unsigned wb = w < 4 ? OFF_W2F + (ct * 48 + 12 * w) * 5120 : OFF_WOF + (ct * 20 + 5 * (w - 4)) * 5120;

  if (w >= 4) {
    const int lt = tid - 256;
    const unsigned tg = (unsigned)tag;
    const unsigned vh0 = bk * 3072 + ((6 * cg) >> 2) * 128 + sw0, vh1 = (bk + 8) * 3072 + ((6 * cg) >> 2) * 128 + sw1;
    int ktn = rot + 2 >= 10 ? rot - 8 : rot + 2;
    auto issue = [&](auto S) {
      constexpr int s = decltype(S)::value;
      const unsigned dst = lds0 + (s & 3) * STG;
      const char* sw = s >= 10 ? w1 + (size_t)ktn * (64 * 3072) : wqkv + (size_t)ktn * (64 * 3840);
      dma_stage(x + ktn * 128, vA, vA1, vA2, vA3, dst + lw * 4096, sw, s >= 10 ? vh0 : vq0, s >= 10 ? vh1 : vq1, dst + 16384 + lw * 2048);
      ktn = ktn == 9 ? 0 : ktn + 1;
    };
    if (lt < 16) sync[lt] = 0u;
    WAIT_VM(8);
    BAR();
    issue(IC<2>{}); issue(IC<3>{});
    const bf16x8 ONE8 = {0x3f80, 0x3f80, 0x3f80, 0x3f80, 0x3f80, 0x3f80, 0x3f80, 0x3f80};
    const f32x4 z4 = {0.f, 0.f, 0.f, 0.f};
    bf16x8 sw_[2][2], sx_[2][2];
    u32x4 gr[5], gc[3];
    u32x2 fl;
    const int fkb = lane < 12 ? 6 * lw + (lane >> 1) : 24 + ((5 * lw) >> 1) + (lane < 18 ? (lane - 12) >> 1 : 0);
    const unsigned fW = FL_W + (fkb * 5 + ((5 * ct + (lane & 1) * 4) >> 3)) * 128;
    const int ty = lane < 24 ? lane >> 3 : 0, rr0 = 2 * qb - 2 + ((lane >> 1) & 3);
    const int rr = rr0 < 0 ? 0 : (ty == 0 && rr0 < 2 * qb ? 2 * qb : rr0);
    const unsigned fQ = FL_Q + wg_of(rr, 5 * ty + ((ch + (lane & 1) * 2) >> 4)) * 128;
    unsigned ro[5], co[3];
#pragma unroll
    for (int k = 0; k < 5; ++k) ro[k] = OFF_RS + (rt * 10 + 2 * k + (lane >> 5)) * 2048 + (32 * lw + (lane & 31)) * 16;
#pragma unroll
    for (int k = 0; k < 3; ++k) co[k] = OFF_CS + (cg * 10 + (k == 2 && g >= 2 ? g : g + 4 * k)) * 1024 + (16 * lw + l15) * 16;
    const unsigned c_on = g < 2 ? 0xffffffffu : 0u;
    auto col_sums = [&](int part) {
#pragma unroll
      for (int k = 0; k < 3; ++k) PIN(gc[k]);
      float c0, c1;
      if (!sum_pairs<3>(gc, c_on, tg, c0, c1)) regather<3>(rs, co, c_on, tg, c0, c1);
      c0 += __shfl_xor(c0, 16); c1 += __shfl_xor(c1, 16);
      c0 += __shfl_xor(c0, 32); c1 += __shfl_xor(c1, 32);
      if (lane < 16) *(LAS f32x2*)(smem + CS_OFF + part * 512 + (16 * lw + lane) * 8) = f32x2{c0, c1};
      WAIT_LGKM0();
      lds_arrive(sync + 7 + part);
    };
    auto stage = [&](auto S) {
      constexpr int s = decltype(S)::value;
      if constexpr (s == 0 || s == 10) {
        const unsigned sb = lds0 + (s & 3) * STG;
#pragma unroll
        for (int i = 0; i < 2; ++i) {
          const int lux = ((s == 0 ? 0 : lu1) + 2 * lw + i) & 7;
          const unsigned wq = sb + 16384 + (lux >> 2) * 8192 + (8 * g + q4) * 128 + (((lux & 3) ^ sigl) << 5) + p4 * 8;
          sw_[i][0] = cat44(trread(wq), trread(wq + 512));
          sw_[i][1] = cat44(trread(wq + 4096), trread(wq + 4608));
          if (s == 0) {
            const unsigned xq = sb + (32 * lw + 16 * i + l15) * 128;
            sx_[i][0] = *(LAS bf16x8*)(size_t)(xq + (xsw << 4));
            sx_[i][1] = *(LAS bf16x8*)(size_t)(xq + ((xsw ^ 4) << 4));
          }
        }
      }
      wait_vm<(s == 6 || s == 7) ? 24 : (s >= 10 && s <= 12 ? 16 + 5 * WOPRE : ((s == 15 || s == 16) ? 19 : (s == 17 ? 0 : 16)))>();
      WAIT_LGKM0();
      BAR();
      if constexpr (s < 16) issue(IC<s + 4>{});
      if constexpr (s == 2 || s == 3) {
        if (b < 170) {
          const unsigned dstb = rkb < 24 ? OFF_W2F : OFF_WOF;
          const int nks = rkb < 24 ? 48 : 20, kb = rkb < 24 ? rkb : rkb - 24;
          const int nl = 2 * lw + s - 2, nt = 8 * rnb + nl, c5 = nt / 5, jj = nt - 5 * c5;
          const unsigned rp = lds0 + RT_OFF + (nl >> 2) * 8192 + (8 * g + q4) * 128 + (((nl & 3) ^ sigl) << 5) + p4 * 8;
#pragma unroll
          for (int ks = 0; ks < 2; ++ks) {
            const bf16x8 f = cat44(trread(rp + ks * 4096), trread(rp + ks * 4096 + 512));
            st16(__builtin_bit_cast(u32x4, f), rs, lane * 16, dstb + ((c5 * nks + 2 * kb + ks) * 5 + jj) * 1024);
          }
        }
      }
      if constexpr (s == 0 || s == 10) {
        __builtin_amdgcn_s_setprio(3);
        const u32x4 c0 = __builtin_bit_cast(u32x4, mfma16(sw_[0][1], ONE8, mfma16(sw_[0][0], ONE8, z4)));
        const u32x4 c1 = __builtin_bit_cast(u32x4, mfma16(sw_[1][1], ONE8, mfma16(sw_[1][0], ONE8, z4)));
        const u32x4 cu = l15 == 0 ? c0 : c1;
        if (l15 < 2) {
          const unsigned o = OFF_CS + (((s == 10 ? 16 : 0) + cg) * 10 + rot) * 1024 + 2 * lw * 128;
          st16(u32x4{cu[0], tg, cu[1], tg}, rs, l15 * 128 + g * 32, o);
          st16(u32x4{cu[2], tg, cu[3], tg}, rs, l15 * 128 + g * 32 + 16, o);
        }
        __builtin_amdgcn_s_setprio(0);
      }
      if constexpr (s == 1) {
        __builtin_amdgcn_s_setprio(3);
#pragma unroll
        for (int i = 0; i < 2; ++i) {
          const f32x4 sx = mfma16(ONE8, sx_[i][1], mfma16(ONE8, sx_[i][0], z4));
          const f32x4 sq = mfma16(sx_[i][1], sx_[i][1], mfma16(sx_[i][0], sx_[i][0], z4));
          const int r = l15 & 3;
          const float sxx = r == 0 ? sq[0] : (r == 1 ? sq[1] : (r == 2 ? sq[2] : sq[3]));
          if (q4 == g)
            st16(u32x4{__float_as_uint(sx[0]), tg, __float_as_uint(sxx), tg}, rs, (32 * lw + 16 * i + l15) * 16,
                 OFF_RS + (rt * 10 + rot) * 2048);
        }
        __builtin_amdgcn_s_setprio(0);
      }
      if constexpr (s == 4) {
        if (lt < 12) dma1(b1 + cg * 192, lane * 16, lds0 + B1_OFF);
        if (lw == 1 && lane < 10) dma1(b2 + ct * 160, lane * 16, lds0 + B1_OFF + 256);
      }
      if constexpr (s == 6 || s == 11) {
        if (lt == 0 && (s == 6 || cg < 15)) __hip_atomic_store((GAS u64*)(ws + (s == 6 ? FL_W : FL_Q) + b * 128), tag, RLX_AGENT);
      }
      if constexpr (s == 5) {
#pragma unroll
        for (int k = 0; k < 5; ++k) gr[k] = aload16(ws, ro[k]);
      }
      if constexpr (s == 5 || s == 14) {
#pragma unroll
        for (int k = 0; k < 3; ++k) { if (s == 14) co[k] += 16 * 10240; gc[k] = aload16(ws, co[k]); }
      }
      if constexpr (s == 8) {
        __builtin_amdgcn_s_setprio(3);
#pragma unroll
        for (int k = 0; k < 5; ++k) PIN(gr[k]);
        float sx, sxx;
        if (!sum_pairs<5>(gr, 0xffffffffu, tg, sx, sxx)) regather<5>(rs, ro, 0xffffffffu, tg, sx, sxx);
        sx += __shfl_xor(sx, 32); sxx += __shfl_xor(sxx, 32);
        const float mean = sx * (1.f / 640.f);
        float var = sxx * (1.f / 640.f) - mean * mean;
        var = var < 0.f ? 0.f : var;
        if (lane < 32) *(LAS f32x2*)(smem + ST_OFF + (32 * lw + lane) * 8) = f32x2{mean, rsqrtf(var + 1e-5f)};
        col_sums(0);
        __builtin_amdgcn_s_setprio(0);
      }
      if constexpr (s == 9) {
        poll_flags(ws + fW, true, tag);
#pragma unroll
        for (int k = 0; k < WOPRE; ++k)
#pragma unroll
          for (int j = 0; j < 5; ++j) wpre[k][j] = ld16(rs, lane * 16, wb + k * 5120 + j * 1024);
      }
      if constexpr (s == 16) fl = aload8(ws, fQ);
    };
    stage(IC<0>{}); stage(IC<1>{}); stage(IC<2>{}); stage(IC<3>{}); stage(IC<4>{}); stage(IC<5>{});
    stage(IC<6>{}); stage(IC<7>{}); stage(IC<8>{}); stage(IC<9>{}); stage(IC<10>{}); stage(IC<11>{});
    stage(IC<12>{}); stage(IC<13>{}); stage(IC<14>{}); stage(IC<15>{}); stage(IC<16>{}); stage(IC<17>{});
    PIN(fl);
    if (!__all(fl[0] == tg && fl[1] == (unsigned)(tag >> 32))) poll_flags(ws + fQ, true, tag);
#pragma unroll
    for (int i = 0; i < 2; ++i) {
      const int seg = 2 * lw + i;
      int tb = 4 * qb - 4 + seg; tb = tb < 0 ? 0 : tb;
      dma_kv(ws + OFF_QKV + (size_t)(tb * 240 + 80 + ch) * 1024, lane * 16, lane * 16 + 81920, lds0 + K_OFF + seg * 1024);
    }
    const int l31 = lane & 31, hh = lane >> 5;
    u32x4 qraw[2][2];
#pragma unroll
    for (int a = 0; a < 2; ++a)
#pragma unroll
      for (int ks = 0; ks < 2; ++ks)
        qraw[a][ks] = __builtin_amdgcn_raw_buffer_load_b128(rs, (ks ? 2 : hh) * 1024 + (32 * a + l31) * 16,
                                                            OFF_QKV + ((4 * qb + lw) * 240 + ch) * 1024, SC1);
    col_sums(1);
    {
      const u32x4 one4 = {0x3f803f80u, 0x3f803f80u, 0x3f803f80u, 0x3f803f80u};
      *(LAS u32x4*)(smem + V_OFF + 3 * VP + lt * 16) = one4;
      if (lt < 40) *(LAS u32x4*)(smem + V_OFF + 3 * VP + 4096 + lt * 16) = one4;
      *(LAS u32x4*)(smem + K_OFF + 3 * KP + lt * 16) = u32x4{qb == 0 ? 0xff80u : 0u, 0u, 0u, 0u};
      *(LAS u32x4*)(smem + K_OFF + 3 * KP + 4096 + lt * 16) = u32x4{0u, 0u, 0u, 0u};
    }
    bf16x8 qf[2][2];
#pragma unroll
    for (int a = 0; a < 2; ++a) {
      u32x4 v0 = qraw[a][0], v1 = qraw[a][1];
      if (odd && hh == 0) { v0[0] = 0u; v0[1] = 0u; }
      if (hh) v1 = u32x4{0x3f80u, 0u, 0u, 0u};
      else if (!odd) { v1[2] = 0u; v1[3] = 0u; }
      qf[a][0] = __builtin_bit_cast(bf16x8, v0);
      qf[a][1] = __builtin_bit_cast(bf16x8, v1);
    }
    f32x16 mF, z16;
#pragma unroll
    for (int e = 0; e < 16; ++e) {
      mF[e] = (e & 3) + 8 * (e >> 2) + 4 * hh > l31 ? 0.f : -INFINITY;
      z16[e] = 0.f;
    }
    asm volatile("" : "+v"(mF), "+v"(qf[0][0]), "+v"(qf[0][1]), "+v"(qf[1][0]), "+v"(qf[1][1]));
    WAIT_VM(0);
    WAIT_LGKM0();
    lds_arrive(sync);
    lds_wait(sync, 4);
    __builtin_amdgcn_s_setprio(2);
    unsigned kp = lds0 + K_OFF + hh * KP + (64 * lw + l31) * 16;
    const bool ones = (g & 1) && (p4 >> 1);
    unsigned vp = lds0 + V_OFF + (2 * (g & 1) + (p4 >> 1)) * VP + ((ones ? 0 : 64 * lw) + 4 * hh + q4) * 16 + (p4 & 1) * 8;
    unsigned ov = l31 * 16 + hh * 8;
    bf16x8 q0 = qf[0][0], q1 = qf[0][1];
#define LDK(T, ks) (*(LAS bf16x8*)(size_t)(kp + (T) * 512 + (ks) * 2 * KP))
#define MF32(a, b, c) __builtin_amdgcn_mfma_f32_32x32x16_bf16(a, b, c, 0, 0, 0)
#define EX2(s_, e) pk2(__builtin_amdgcn_exp2f((s_)[e]), __builtin_amdgcn_exp2f((s_)[(e) + 1]))
    auto tile_max = [&](int T) {
      f32x16 sp = MF32(LDK(T, 1), q1, MF32(LDK(T, 0), q0, z16));
      float m0 = -INFINITY, m1 = -INFINITY;
#pragma unroll
      for (int e = 0; e < 16; ++e) {
        const bool off = T == 0 ? mF[e] != 0.f : (T == 8 ? mF[e] == 0.f : false);
        sp[e] = off ? -INFINITY : sp[e];
      }
#pragma unroll
      for (int e = 0; e < 16; e += 4) { m0 = MAX3(m0, sp[e], sp[e + 1]); m1 = MAX3(m1, sp[e + 2], sp[e + 3]); }
      m0 = fmaxf(m0, m1);
      return fmaxf(m0, __shfl_xor(m0, 32));
    };
#pragma unroll 1
    for (int un = 0; un < 2; ++un) {
      float mx = tile_max(8);
      f32x16 oa;
#pragma unroll 1
      for (int pass = 0; pass < 2; ++pass) {
      f32x16 ca;
#pragma unroll
      for (int e = 0; e < 16; ++e) ca[e] = -mx;
      oa = z16;
      {
        bf16x8 kc[2][2], vf[2][2];
        f32x16 sc[2];
        u32x4 pa[2] = {{0u, 0u, 0u, 0u}, {0u, 0u, 0u, 0u}};
        kc[0][0] = LDK(0, 0); kc[0][1] = LDK(0, 1); kc[1][0] = LDK(1, 0); kc[1][1] = LDK(1, 1);
        sc[0] = MF32(kc[0][1], q1, MF32(kc[0][0], q0, ca + mF));
        sc[1] = z16;
        asm volatile("" :: "v"(sc[0]));
        SB();
#pragma unroll
        for (int T = 0; T < 10; ++T) {
          const int c = T & 1, n = c ^ 1;
          if (T < 9) {
#pragma unroll
            for (int ks = 0; ks < 2; ++ks) vf[c][ks] = cat44(trread(vp + T * 512 + ks * 256), trread(vp + T * 512 + ks * 256 + 128));
          }
          if (T < 8) sc[n] = MF32(kc[n][1], q1, MF32(kc[n][0], q0, ca));
          if (T < 7) { kc[c][0] = LDK(T + 2, 0); kc[c][1] = LDK(T + 2, 1); }
          if (T > 0) oa = MF32(vf[n][1], __builtin_bit_cast(bf16x8, pa[1]), MF32(vf[n][0], __builtin_bit_cast(bf16x8, pa[0]), oa));
          if (T == 8) {
#pragma unroll
            for (int e = 0; e < 16; ++e) sc[c][e] = mF[e] == 0.f ? -INFINITY : sc[c][e];
          }
          if (T < 9) {
#pragma unroll
            for (int e = 0; e < 4; ++e) { pa[0][e] = EX2(sc[c], 2 * e); pa[1][e] = EX2(sc[c], 8 + 2 * e); }
          }
          SGB(0x100, 6);
          if (T == 8) SGB(0x2, 16);
          SGB(0x8, 1); SGB(0x400, 4); SGB(0x2, 1);
#pragma unroll
          for (int k = 0; k < 3; ++k) { SGB(0x8, 1); SGB(0x400, 2); SGB(0x2, 1); SGB(0x400, 2); SGB(0x2, 1); }
          SGB(0x2, 1);
          asm volatile("" :: "v"(sc[n]), "v"(oa), "v"(pa[0]), "v"(pa[1]));
          SB();
        }
      }
      if (pass || __all(oa[12] < 1.8446744e19f)) break;
#pragma unroll 1
      for (int T = 0; T < 8; ++T) mx = fmaxf(mx, tile_max(T));
      }
      const float il = __builtin_amdgcn_rcpf(oa[12]);
#pragma unroll
      for (int pl = 0; pl < 3; ++pl) {
        const bool ok = pl == 1 || (pl == 0 ? !(odd && hh == 0) : (odd || hh == 0));
        if (ok) {
          const u32x2 pkd = {pk2(oa[4 * pl] * il, oa[4 * pl + 1] * il), pk2(oa[4 * pl + 2] * il, oa[4 * pl + 3] * il)};
          __builtin_amdgcn_raw_buffer_store_b64(pkd, rs, pl * 1024 + ov, OFF_O + ((4 * qb + lw) * 80 + ch) * 1024, SC1);
        }
      }
      kp += 512; vp += ones ? 0 : 512; ov += 512;
      q0 = qf[1][0]; q1 = qf[1][1];
    }
    __builtin_amdgcn_s_setprio(0);
    lds_arrive(sync + 4);
    WAIT_VM(0);
    if (lane == 0) __hip_atomic_store((GAS u64*)(ws + FL_O + (b * 4 + lw) * 128), tag, RLX_AGENT);
#pragma unroll
    for (int k = WOPRE; k < 5; ++k)
#pragma unroll
      for (int j = 0; j < 5; ++j) wpre[k][j] = ld16(rs, lane * 16, wb + k * 5120 + j * 1024);
    const unsigned orow = orow_f();
#pragma unroll
    for (int j = 0; j < 2; ++j) xv[j] = *(const u32x2*)(x + orow + (3 + j) * 32);
    const unsigned ab = OFF_O + (mt * 80 + 20 * lw) * 1024;
    {
      const int hd = 8 * lw + (lane & 7);
      poll_flags(ws + FL_O + ((((((mt >> 2) << 2) | (hd & 3)) << 3) | (hd >> 2)) * 4 + (mt & 3)) * 128, lane < 8, tag);
    }
    bf16x8 af[4][5];
#pragma unroll
    for (int i = 0; i < 4; ++i)
#pragma unroll
      for (int k = 0; k < 5; ++k) af[i][k] = ld16(rs, va + ((lw + 1 + i) & 3) * 256, ab + k * 4096);
    SB();
    lds_wait(sync + 4, 4);
    lds_wait(sync + 6, 4);
    f32x4 own[2];
#pragma unroll
    for (int i = 0; i < 4; ++i) {
      const int mi = (lw + 1 + i) & 3;
      f32x4 a5[5] = {z4, z4, z4, z4, z4};
#pragma unroll
      for (int k = 0; k < 5; ++k)
#pragma unroll
        for (int j = 0; j < 5; ++j) a5[j] = mfma16(wpre[k][j], af[i][k], a5[j]);
#pragma unroll
      for (int j = 0; j < 3; ++j) *(LAS f32x4*)(smem + REDS1_OFF + ((mi * 4 + lw) * 3 + j) * 1024 + lane * 16) = a5[j];
      if (i < 3) {
#pragma unroll
        for (int j = 0; j < 2; ++j) *(LAS f32x4*)(smem + REDS2_OFF + ((mi * 3 + 2 - i) * 2 + j) * 1024 + lane * 16) = a5[3 + j];
      } else { own[0] = a5[3]; own[1] = a5[4]; }
    }
    __syncthreads();
#pragma unroll
    for (int j = 0; j < 2; ++j) {
      f32x4 sv = own[j];
#pragma unroll
      for (int k = 0; k < 3; ++k) sv += *(LAS f32x4*)(smem + REDS2_OFF + ((lw * 3 + k) * 2 + j) * 1024 + lane * 16);
      const f32x4 gt = *(LAS f32x4*)(smem + GX_OFF + (lw * 2 + j) * 1024 + lane * 16);
      *(u32x2*)(out + orow + (3 + j) * 32) = fin4(xv[j], sv, gt);
    }

  } else {
    if (idx * 4 + w < CODE_KB) {
      const u32x4 t0 = *(const u32x4*)(pc0 + (idx * 4 + w) * 1024 + lane * 16);
      asm volatile("" :: "v"(t0));
    }
    {
      const int mw = w & 1, nw = w >> 1;
      unsigned xa[2];
      xa[0] = lds0 + (64 * mw + l15) * 128 + (xsw << 4);
      xa[1] = lds0 + (64 * mw + l15) * 128 + ((xsw ^ 4) << 4);
      const unsigned wl = lds0 + 16384 + (8 * g + q4) * 128 + p4 * 8;
      const unsigned vo = (g >> 1) * 1024 + (16 * (g & 1) + l15) * 16;
      float sh[4], sc[4];
      BAR();
      mfma_part<4, 0>(xa, wl, 4 * nw, sigl, lds0, sh, sc, 64 * mw + l15, cg < 5 ? 0.22360679774997896f * 1.4426950408889634f : 1.f, rs, vo,
                      cg < 15 ? OFF_QKV + ((2 * rt + mw) * 240 + 16 * cg + 8 * nw) * 1024 : 0x80000000u, 4 * nw, g);
      mfma_part<3, 10>(xa, wl, lu1 + 3 * nw, sigl, lds0, sh, sc, 0, 1.f, rs, vo,
                       OFF_H + ((2 * rt + mw) * 192 + 12 * cg + 6 * nw) * 1024, 3 * nw, g);
    }
    WAIT_VM(0);
    if (lds_arrive(sync + 2) == 3 && lane == 0) __hip_atomic_store((GAS u64*)(ws + FL_H + b * 128), tag, RLX_AGENT);
#pragma unroll
    for (int k = 0; k < 4; ++k)
#pragma unroll
      for (int j = 0; j < 5; ++j) wpre[k][j] = ld16(rs, lane * 16, wb + k * 5120 + j * 1024);
    const unsigned wl_ = lds0 + slab_of(w);
    lds_wait(sync + 2, 4);
    dma5_sc1(ws + wb + 4 * 5120, lane * 16, wl_);
    dma5_sc1(ws + wb + 5 * 5120, lane * 16, wl_ + 5120);
    const unsigned ab = OFF_H + (mt * 192 + 48 * w) * 1024;
    if (b == 0 && w == 0) {
#pragma unroll 1
      for (int spins = 0; spins < (1 << 20); ++spins) {
        u64 v[4];
#pragma unroll
        for (int i = 0; i < 4; ++i) v[i] = __hip_atomic_load((const GAS u64*)(ws + FL_W + (lane + 64 * i) * 128), RLX_AGENT);
        if (__all(((v[0] ^ tag) | (v[1] ^ tag) | (v[2] ^ tag) | (v[3] ^ tag)) == 0)) break;
        __builtin_amdgcn_s_sleep(1);
      }
      if (lane == 0) __hip_atomic_store((GAS u64*)(ws + OFF_FL), tag, RLX_AGENT);
    }
    poll_flags(ws + FL_H + wg_of(mt >> 1, 4 * w + (lane & 3)) * 128, lane < 4, tag);
    unsigned vi[4];
#pragma unroll
    for (int i = 0; i < 4; ++i) vi[i] = va + ((w + 1 + i) & 3) * 256;
    dma5_sc1(ws + ab + 4 * 4096, lane * 16, wl_ + 10240);
    dma5_sc1(ws + ab + 4 * 4096 + 3072, lane * 16, wl_ + 10240 + 3072);
    bf16x8 af[4][4];
#pragma unroll
    for (int k = 0; k < 4; ++k)
#pragma unroll
      for (int i = 0; i < 4; ++i) af[k][i] = ld16(rs, vi[i], ab + k * 4096);
    SB();
#pragma unroll
    for (int i = 0; i < 4; ++i)
#pragma unroll
      for (int j = 0; j < 5; ++j) acc[i][j] = f32x4{0.f, 0.f, 0.f, 0.f};
#pragma unroll
    for (int k = 0; k < 12; ++k) {
      const int cs = k & 3;
      if (k == 6)
        asm volatile("s_mov_b64 exec, 1\n\tds_add_u32 %0, %1\n\ts_mov_b64 exec, -1" :: "v"(lds0 + SYNC_OFF + 24), "v"(1u) : "memory");
      if (k == W2HOLD) {
        unsigned tv, ts;
        asm volatile(".Lp%=:\n\tds_read_b32 %0, %2\n\ts_waitcnt lgkmcnt(0)\n\tv_readfirstlane_b32 %1, %0\n\t"
                     "s_cmp_ge_u32 %1, 4\n\ts_cbranch_scc1 .Le%=\n\ts_sleep 2\n\ts_branch .Lp%=\n.Le%=:"
                     : "=&v"(tv), "=&s"(ts) : "v"(lds0 + SYNC_OFF + 16) : "memory", "scc");
      }
#pragma unroll
      for (int i = 0; i < 4; ++i) {
#pragma unroll
        for (int j = 0; j < 5; ++j) acc[i][j] = mfma16(wpre[cs][j], af[cs][i], acc[i][j]);
        if (k == 11 && i < 3) {
#pragma unroll
          for (int j = 0; j < 5; ++j)
            *(LAS f32x4*)(smem + REDG_OFF + ((((w + 1 + i) & 3) * 3 + 2 - i) * 5 + j) * 1024 + lane * 16) = acc[i][j];
        }
      }
      SB();
      if (k < 2) {
#pragma unroll
        for (int j = 0; j < 5; ++j) wpre[cs][j] = *(LAS bf16x8*)(size_t)(wl_ + k * 5120 + j * 1024 + lane * 16);
#pragma unroll
        for (int i = 0; i < 4; ++i) af[cs][i] = *(LAS bf16x8*)(size_t)(wl_ + 10240 + k * 4096 + vi[i]);
      } else if (k < 8) {
#pragma unroll
        for (int i = 0; i < 4; ++i) af[cs][i] = ld16(rs, vi[i], ab + (k + 4) * 4096);
#pragma unroll
        for (int j = 0; j < 5; ++j) wpre[cs][j] = ld16(rs, lane * 16, wb + (k + 4) * 5120 + j * 1024);
      }
      if (k == 10) {
        const unsigned orow = orow_f();
#pragma unroll
        for (int j = 0; j < 3; ++j) xv[j] = *(const u32x2*)(x + orow + j * 32);
      }
      SB();
    }
    u32x2 bb[5];
#pragma unroll
    for (int j = 0; j < 5; ++j) bb[j] = *(LAS u32x2*)(smem + B1_OFF + 256 + (16 * j + 4 * g) * 2);
    WAIT_LGKM0();
    lds_arrive(sync + 3);
    lds_wait(sync + 3, 4);
    f32x4 gt[5];
#pragma unroll
    for (int j = 0; j < 5; ++j) {
      gt[j] = acc[3][j];
#pragma unroll
      for (int k = 0; k < 3; ++k) gt[j] += *(LAS f32x4*)(smem + REDG_OFF + ((w * 3 + k) * 5 + j) * 1024 + lane * 16);
    }
#pragma unroll
    for (int j = 0; j < 5; ++j) {
      const float bv[4] = {bflo(bb[j][0]), bfhi(bb[j][0]), bflo(bb[j][1]), bfhi(bb[j][1])};
#pragma unroll
      for (int e = 0; e < 4; ++e) {
        const float t = rbf(rbf(gt[j][e]) + bv[e]);
        gt[j][e] = rbf(t * __builtin_amdgcn_rcpf(1.f + __expf(-t)));
      }
      if (j >= 3) *(LAS f32x4*)(smem + GX_OFF + (w * 2 + j - 3) * 1024 + lane * 16) = gt[j];
    }
    __syncthreads();
    const unsigned orow = orow_f();
#pragma unroll
    for (int j = 0; j < 3; ++j) {
      f32x4 sv = *(LAS f32x4*)(smem + REDS1_OFF + ((w * 4) * 3 + j) * 1024 + lane * 16);
#pragma unroll
      for (int k = 1; k < 4; ++k) sv += *(LAS f32x4*)(smem + REDS1_OFF + ((w * 4 + k) * 3 + j) * 1024 + lane * 16);
      *(u32x2*)(out + orow + j * 32) = fin4(xv[j], sv, gt[j]);
    }
    __builtin_amdgcn_endpgm();
  }
}

extern "C" void kernel_launch(void* const* d_in, const int* in_sizes, int n_in, void* d_out, int out_size, void* d_ws,
                              size_t ws_size, hipStream_t stream) {
  (void)hipFuncSetAttribute((const void*)k_all, hipFuncAttributeMaxDynamicSharedMemorySize, LDS_ALL);
  k_all<<<256, 512, LDS_ALL, stream>>>((const char*)d_in[0], (const char*)d_in[1], (const char*)d_in[4], (const char*)d_in[5],
                                       (const char*)d_in[6], (const char*)d_in[2], (const char*)d_in[7], (char*)d_ws, (char*)d_out);
}
```

```cpp
#include <hip/hip_runtime.h>
#include <stdint.h>

#define LAS __attribute__((address_space(3)))
#define GAS __attribute__((address_space(1)))
#define CAS __attribute__((address_space(4)))
typedef short bf16x8 __attribute__((ext_vector_type(8)));
typedef short s16x4 __attribute__((ext_vector_type(4)));
typedef float f32x4 __attribute__((ext_vector_type(4)));
typedef unsigned u32x4 __attribute__((ext_vector_type(4)));
typedef unsigned u32x2 __attribute__((ext_vector_type(2)));
typedef __bf16 bf16x2_t __attribute__((ext_vector_type(2)));
typedef float f32x2 __attribute__((ext_vector_type(2)));
typedef float f32x16 __attribute__((ext_vector_type(16)));
typedef unsigned long long u64;

constexpr unsigned OFF_FL = 0;
constexpr unsigned FL_W = 1024, FL_Q = FL_W + 32768, FL_H = FL_Q + 32768, FL_O = FL_H + 32768;
constexpr unsigned OFF_RS = 262144;
constexpr unsigned OFF_CS = OFF_RS + 16 * 10 * 2048;
constexpr unsigned OFF_QKV = OFF_CS + 2 * 16 * 10 * 1024;
constexpr unsigned OFF_H = OFF_QKV + 32 * 240 * 1024;
constexpr unsigned OFF_O = OFF_H + 32 * 192 * 1024;
constexpr unsigned OFF_W2F = OFF_O + 32 * 80 * 1024;
constexpr unsigned OFF_WOF = OFF_W2F + 8 * 48 * 5120;
constexpr unsigned WS_USED = OFF_WOF + 8 * 20 * 5120;

__device__ __forceinline__ f32x4 mfma16(bf16x8 a, bf16x8 b, f32x4 c) {
  return __builtin_amdgcn_mfma_f32_16x16x32_bf16(a, b, c, 0, 0, 0);
}
__device__ __forceinline__ unsigned pk2(float a, float b) {
  f32x2 v = {a, b};
  bf16x2_t r = __builtin_convertvector(v, bf16x2_t);
  return __builtin_bit_cast(unsigned, r);
}
__device__ __forceinline__ float rbf(float a) { return __builtin_bit_cast(float, pk2(0.f, a) & 0xffff0000u); }
__device__ __forceinline__ float bflo(unsigned u) { return __builtin_bit_cast(float, u << 16); }
__device__ __forceinline__ float bfhi(unsigned u) { return __builtin_bit_cast(float, u & 0xffff0000u); }
__device__ __forceinline__ u32x2 fin4(u32x2 xv, f32x4 sv, f32x4 gt) {
  return u32x2{pk2(bflo(xv[0]) + rbf(rbf(sv[0]) * gt[0]), bfhi(xv[0]) + rbf(rbf(sv[1]) * gt[1])),
               pk2(bflo(xv[1]) + rbf(rbf(sv[2]) * gt[2]), bfhi(xv[1]) + rbf(rbf(sv[3]) * gt[3]))};
}
__device__ __forceinline__ s16x4 trread(unsigned a) {
  return __builtin_amdgcn_ds_read_tr16_b64_v4i16((LAS s16x4*)(size_t)a);
}
__device__ __forceinline__ bf16x8 cat44(s16x4 a, s16x4 b) {
  return __builtin_shufflevector(a, b, 0, 1, 2, 3, 4, 5, 6, 7);
}
#define WAIT_VM(n) asm volatile("s_waitcnt vmcnt(" #n ")" ::: "memory")
#define WAIT_LGKM0() __builtin_amdgcn_s_waitcnt(0xC07F)
#define BAR() __builtin_amdgcn_s_barrier()
#define SB() __builtin_amdgcn_sched_barrier(0)
#define SGB(m, n) __builtin_amdgcn_sched_group_barrier(m, n, 0)
#define MAX3(a, b, c) __builtin_fmaxf(__builtin_fmaxf(a, b), c)
#define RLX_AGENT __ATOMIC_RELAXED, __HIP_MEMORY_SCOPE_AGENT
constexpr int SC1 = 16;
constexpr int CODE_KB = 38;
template <int N> struct IC { static constexpr int value = N; };

__device__ __forceinline__ void dma1(const char* sbase, unsigned voff, unsigned lds) {
  unsigned keep;
  asm volatile("s_mov_b32 %0, m0\n\ts_mov_b32 m0, %3\n\ts_nop 2\n\tglobal_load_lds_dwordx4 %1, %2\n\ts_mov_b32 m0, %0"
               : "=&s"(keep) : "v"(voff), "s"(sbase), "s"(lds) : "memory");
}
__device__ __forceinline__ void dma_stage(const char* sa, unsigned va0, unsigned va1, unsigned va2, unsigned va3, unsigned la,
                                          const char* sb, unsigned vb0, unsigned vb1, unsigned lb) {
  unsigned keep;
  asm volatile("s_mov_b32 %0, m0\n\ts_mov_b32 m0, %10\n\ts_nop 2\n\tglobal_load_lds_dwordx4 %8, %7\n\t"
               "s_add_u32 m0, m0, 1024\n\ts_nop 0\n\tglobal_load_lds_dwordx4 %9, %7\n\t"
               "s_add_u32 m0, m0, 7040\n\ts_nop 0\n\tglobal_load_lds_dwordx4 %8, %7 offset:128\n\t"
               "s_add_u32 m0, m0, 1024\n\ts_nop 0\n\tglobal_load_lds_dwordx4 %9, %7 offset:128\n\t"
               "s_mov_b32 m0, %6\n\ts_nop 0\n\tglobal_load_lds_dwordx4 %2, %1\n\t"
               "s_add_u32 m0, m0, 1024\n\ts_nop 0\n\tglobal_load_lds_dwordx4 %3, %1\n\t"
               "s_add_u32 m0, m0, 1024\n\ts_nop 0\n\tglobal_load_lds_dwordx4 %4, %1\n\t"
               "s_add_u32 m0, m0, 1024\n\ts_nop 0\n\tglobal_load_lds_dwordx4 %5, %1\n\ts_mov_b32 m0, %0"
               : "=&s"(keep) : "s"(sa), "v"(va0), "v"(va1), "v"(va2), "v"(va3), "s"(la), "s"(sb), "v"(vb0), "v"(vb1), "s"(lb)
               : "memory", "scc");
}
__device__ __forceinline__ void dma_stage_w(const char* sb, unsigned vb0, unsigned vb1, unsigned lb) {
  unsigned keep;
  asm volatile("s_mov_b32 %0, m0\n\ts_mov_b32 m0, %4\n\ts_nop 2\n\tglobal_load_lds_dwordx4 %2, %1\n\t"
               "s_add_u32 m0, m0, 1024\n\ts_nop 0\n\tglobal_load_lds_dwordx4 %3, %1\n\t"
               "s_add_u32 m0, m0, 7040\n\ts_nop 0\n\tglobal_load_lds_dwordx4 %2, %1 offset:128\n\t"
               "s_add_u32 m0, m0, 1024\n\ts_nop 0\n\tglobal_load_lds_dwordx4 %3, %1 offset:128\n\ts_mov_b32 m0, %0"
               : "=&s"(keep) : "s"(sb), "v"(vb0), "v"(vb1), "s"(lb) : "memory", "scc");
}
__device__ __forceinline__ void dma_kv(const char* sb, unsigned vk, unsigned vv, unsigned l) {
  unsigned keep;
  asm volatile("s_mov_b32 %0, m0\n\ts_mov_b32 m0, %4\n\ts_nop 2\n\tglobal_load_lds_dwordx4 %2, %1 sc1\n\t"
               "s_add_u32 m0, m0, 7168\n\ts_nop 0\n\tglobal_load_lds_dwordx4 %2, %1 offset:1024 sc1\n\t"
               "s_add_u32 m0, m0, 7168\n\ts_nop 0\n\tglobal_load_lds_dwordx4 %2, %1 offset:2048 sc1\n\t"
               "s_add_u32 m0, m0, 18432\n\ts_nop 0\n\tglobal_load_lds_dwordx4 %3, %1 sc1\n\t"
               "s_add_u32 m0, m0, 7232\n\ts_nop 0\n\tglobal_load_lds_dwordx4 %3, %1 offset:1024 sc1\n\t"
               "s_add_u32 m0, m0, 7232\n\ts_nop 0\n\tglobal_load_lds_dwordx4 %3, %1 offset:2048 sc1\n\t"
               "s_mov_b32 m0, %0"
               : "=&s"(keep) : "s"(sb), "v"(vk), "v"(vv), "s"(l) : "memory", "scc");
}
__device__ __forceinline__ void dma5_sc1(const char* sb, unsigned v, unsigned l) {
  unsigned keep;
  asm volatile("s_mov_b32 %0, m0\n\ts_mov_b32 m0, %3\n\ts_nop 2\n\tglobal_load_lds_dwordx4 %2, %1 sc1\n\t"
               "global_load_lds_dwordx4 %2, %1 offset:1024 sc1\n\tglobal_load_lds_dwordx4 %2, %1 offset:2048 sc1\n\t"
               "global_load_lds_dwordx4 %2, %1 offset:3072 sc1\n\ts_add_u32 m0, m0, 1024\n\ts_nop 0\n\t"
               "global_load_lds_dwordx4 %4, %1 offset:3072 sc1\n\ts_mov_b32 m0, %0"
               : "=&s"(keep) : "s"(sb), "v"(v), "s"(l), "v"(v + 1024) : "memory", "scc");
}
__device__ __forceinline__ u32x2 aload8(const char* sbase, unsigned voff) {
  u32x2 r;
  asm volatile("s_nop 4\n\tglobal_load_dwordx2 %0, %1, %2 sc1" : "=v"(r) : "v"(voff), "s"(sbase) : "memory");
  return r;
}
__device__ __forceinline__ u32x4 aload16(const char* sbase, unsigned voff) {
  u32x4 r;
  asm volatile("s_nop 4\n\tglobal_load_dwordx4 %0, %1, %2 sc1" : "=v"(r) : "v"(voff), "s"(sbase) : "memory");
  return r;
}
#define PIN(x) asm volatile("" : "+v"(x))
template <int N> __device__ __forceinline__ void wait_vm() { asm volatile("s_waitcnt vmcnt(%0)" :: "i"(N) : "memory"); }
__device__ __forceinline__ void poll_flags(const char* flag, bool need, u64 tag) {
#pragma unroll 1
  for (int spins = 0; spins < (1 << 20); ++spins) {
    const u64 v = need ? __hip_atomic_load((const GAS u64*)flag, RLX_AGENT) : tag;
    if (__all(v == tag)) break;
    __builtin_amdgcn_s_sleep(1);
  }
  __builtin_amdgcn_fence(__ATOMIC_ACQUIRE, "wavefront");
}
__device__ __forceinline__ unsigned lds_arrive(LAS unsigned* c) {
  unsigned r = 0;
  if ((threadIdx.x & 63) == 0) r = __hip_atomic_fetch_add(c, 1u, __ATOMIC_RELAXED, __HIP_MEMORY_SCOPE_WORKGROUP);
  return __builtin_amdgcn_readfirstlane(r);
}
__device__ __forceinline__ void lds_wait(LAS unsigned* c, unsigned n) {
#pragma unroll 1
  for (int spins = 0; spins < (1 << 22); ++spins) {
    if (__builtin_amdgcn_readfirstlane(__hip_atomic_load(c, __ATOMIC_RELAXED, __HIP_MEMORY_SCOPE_WORKGROUP)) >= n) break;
  }
  __builtin_amdgcn_fence(__ATOMIC_ACQUIRE, "wavefront");
}

constexpr int WOPRE = 4;
constexpr int W2HOLD = 10;
constexpr int STG = 32768, RT_OFF = 4 * STG, LDS_ALL = 163840;
constexpr int B1_OFF = 159744;
constexpr int ST_OFF = 160768;
constexpr int CS_OFF = ST_OFF + 1024, SYNC_OFF = ST_OFF + 2048;
constexpr int KP = 8192, VP = 8192 + 64;
constexpr int K_OFF = 0, V_OFF = K_OFF + 4 * KP;
__device__ __forceinline__ constexpr int slab_of(int w) { return 65536 + w * 20480; }
constexpr int REDG_OFF = 0, GX_OFF = 61440, REDS1_OFF = GX_OFF + 8192, REDS2_OFF = REDS1_OFF + 49152;

__device__ __forceinline__ void st16(u32x4 v, __amdgpu_buffer_rsrc_t rs, unsigned voff, unsigned soff) {
  __builtin_amdgcn_raw_buffer_store_b128(v, rs, voff + soff, 0, SC1);
}
__device__ __forceinline__ bf16x8 ld16(__amdgpu_buffer_rsrc_t rs, unsigned voff, unsigned soff) {
  return __builtin_bit_cast(bf16x8, __builtin_amdgcn_raw_buffer_load_b128(rs, voff, soff, SC1));
}
__device__ __forceinline__ int wg_of(int rt, int cg) { return (((cg & 3) * 8 + (rt & 7)) << 3) | ((cg >> 2) << 1) | (rt >> 3); }
template <int N>
__device__ __forceinline__ bool sum_pairs(const u32x4 (&v)[N], unsigned last_on, unsigned tg, float& a, float& b) {
  bool ok = true;
  a = 0.f; b = 0.f;
#pragma unroll
  for (int k = 0; k < N; ++k) {
    ok = ok && v[k][1] == tg && v[k][3] == tg;
    const unsigned m = k == N - 1 ? last_on : 0xffffffffu;
    a += __uint_as_float(v[k][0] & m);
    b += __uint_as_float(v[k][2] & m);
  }
  return __all(ok);
}
template <int N>
__device__ __forceinline__ void regather(__amdgpu_buffer_rsrc_t rs, const unsigned (&vo)[N], unsigned last_on, unsigned tg, float& a, float& b) {
#pragma unroll 1
  for (int spins = 0; spins < (1 << 20); ++spins) {
    u32x4 v[N];
#pragma unroll
    for (int k = 0; k < N; ++k) v[k] = __builtin_amdgcn_raw_buffer_load_b128(rs, vo[k], 0, SC1);
    if (sum_pairs<N>(v, last_on, tg, a, b)) break;
    __builtin_amdgcn_s_sleep(2);
  }
}

#define LDX(set, i, ks) xf[set][i] = *(LAS bf16x8*)(size_t)(xa[ks] + (i) * 2048)
#define LDW(set, j, ks) wf[set][j] = cat44(trread(wa[j] + (ks) * 4096), trread(wa[j] + (ks) * 4096 + 512))
#define HALF(set, ks) do { \
    _Pragma("unroll") for (int j = 0; j < NT; ++j) { \
      if (j == 0) { _Pragma("unroll") for (int i = 0; i < 4; ++i) LDX((set) ^ 1, i, ks); } \
      else if (2 * j - 2 < NT) { LDW((set) ^ 1, 2 * j - 2, ks); if (2 * j - 1 < NT) LDW((set) ^ 1, 2 * j - 1, ks); } \
      _Pragma("unroll") for (int i = 0; i < 4; ++i) acc[i][j] = mfma16(wf[set][j], xf[set][i], acc[i][j]); \
      SGB(0x8, 1); SGB(0x100, 1); SGB(0x8, 1); SGB(0x100, 1); SGB(0x8, 1); SGB(0x100, 1); SGB(0x8, 1); SGB(0x100, 1); \
      asm volatile("" :: "v"(acc[0][j]), "v"(acc[1][j]), "v"(acc[2][j]), "v"(acc[3][j])); \
      SB(); \
    } } while (0)
template <int NT, int S0>
__device__ __forceinline__ void mfma_part(unsigned (&xa)[2], unsigned wl, int tl0, int sigl, unsigned lds0, float (&sh)[4], float (&sc)[4],
                                          int row0, float qs, __amdgpu_buffer_rsrc_t rs, unsigned vo, unsigned so, int cs0, int g) {
  constexpr bool ISH = S0 == 10;
  unsigned wa[NT];
  f32x4 acc[4][NT];
#pragma unroll
  for (int j = 0; j < NT; ++j) {
    const int tl = tl0 + j;
    wa[j] = wl + (S0 & 3) * STG + (tl >> 2) * 8192 + (((tl & 3) ^ sigl) << 5);
#pragma unroll
    for (int i = 0; i < 4; ++i) acc[i][j] = f32x4{0.f, 0.f, 0.f, 0.f};
  }
  bf16x8 xf[2][4], wf[2][NT];
#pragma unroll
  for (int i = 0; i < 4; ++i) LDX(0, i, 0);
#pragma unroll
  for (int j = 0; j < NT; ++j) LDW(0, j, 0);
  WAIT_LGKM0();
  int buf = S0 & 3;
#pragma unroll 1
  for (int t = 0; t < 10; ++t) {
    HALF(0, 1);
    {
      const int d = buf == 3 ? -3 * STG : STG;
      buf = (buf + 1) & 3;
      xa[0] += d; xa[1] += d;
#pragma unroll
      for (int j = 0; j < NT; ++j) wa[j] += d;
    }
    WAIT_LGKM0();
    if (ISH) {
      asm volatile("s_cmp_lg_u32 %0, 1\n\ts_cbranch_scc1 .Lk%=\n\ts_waitcnt vmcnt(0)\n.Lk%=:\n\t"
                   "s_cmp_ge_u32 %0, 8\n\ts_cbranch_scc1 .Lc%=\n\ts_barrier\n.Lc%=:" :: "s"(t) : "memory", "scc");
    } else BAR();
    SB();
    HALF(1, 0);
    WAIT_LGKM0();
  }
  lds_wait((LAS unsigned*)(size_t)(lds0 + SYNC_OFF + (ISH ? 32 : 28)), 4);
  if (!ISH) {
#pragma unroll
    for (int i = 0; i < 4; ++i) {
      const f32x2 st = *(LAS f32x2*)(size_t)(lds0 + ST_OFF + (row0 + 16 * i) * 8);
      sc[i] = st[1]; sh[i] = -st[0] * st[1];
    }
  }
#pragma unroll
  for (int j = 0; j < NT; ++j) {
    const f32x4 c4 = *(LAS f32x4*)(size_t)(lds0 + CS_OFF + (ISH ? 512 : 0) + ((cs0 + j) * 16 + 4 * g) * 4);
    f32x4 bv = {0.f, 0.f, 0.f, 0.f};
    if (ISH) {
      const u32x2 bb = *(LAS u32x2*)(size_t)(lds0 + B1_OFF + ((cs0 + j) * 16 + 4 * g) * 2);
      bv = f32x4{bflo(bb[0]), bfhi(bb[0]), bflo(bb[1]), bfhi(bb[1])};
    }
#pragma unroll
    for (int pp = 0; pp < 2; ++pp) {
      float v[2][4];
#pragma unroll
      for (int i = 0; i < 2; ++i)
#pragma unroll
        for (int r = 0; r < 4; ++r) {
          float t = __builtin_fmaf(sc[2 * pp + i], acc[2 * pp + i][j][r], __builtin_fmaf(sh[2 * pp + i], c4[r], bv[r]));
          if (ISH) { t = t > 0.f ? t : 0.f; t = t * t; } else t *= qs;
          v[i][r] = t;
        }
      const unsigned x0 = pk2(v[0][0], v[0][1]), x1 = pk2(v[0][2], v[0][3]);
      const unsigned y0 = pk2(v[1][0], v[1][1]), y1 = pk2(v[1][2], v[1][3]);
      const auto s0 = __builtin_amdgcn_permlane16_swap(x0, y0, false, false);
      const auto s1 = __builtin_amdgcn_permlane16_swap(x1, y1, false, false);
      const u32x4 o = {s0[0], s1[0], s0[1], s1[1]};
      st16(o, rs, vo + pp * 512, so + 2 * j * 1024);
    }
  }
}

__global__ __launch_bounds__(512, 2) void k_all(const char* __restrict__ x, const char* __restrict__ wqkv, const char* __restrict__ w1,
                                                const char* __restrict__ b1, const char* __restrict__ w2, const char* __restrict__ wo,
                                                const char* __restrict__ b2, char* ws, char* __restrict__ out) {
  extern __shared__ __attribute__((aligned(16))) char smem_[];
  LAS char* smem = (LAS char*)smem_;
  const int tid = threadIdx.x, lane = tid & 63, w = __builtin_amdgcn_readfirstlane(tid >> 6);
  const int b = blockIdx.x, xcd = b & 7, idx = b >> 3;
  const unsigned lds0 = (unsigned)(size_t)smem;
  asm volatile("" :: "s"(x), "s"(wqkv), "s"(w1), "s"(b1), "s"(w2), "s"(wo), "s"(ws));
  u64 pc_;
  asm volatile("s_getpc_b64 %0" : "=s"(pc_));
  const int rt = (xcd & 1) * 8 + (idx & 7), cg = (xcd >> 1) * 4 + (idx >> 3);
  const int rot = (rt + 3 * cg) % 10;
  const int lw = w - 4, lr = lane >> 3, ls = lane & 7;
  const int bk = 16 * lw + lr, sw0 = (ls ^ (bk & 2)) << 4, sw1 = sw0 ^ 64;
  const unsigned vq0 = bk * 3840 + (cg < 15 ? cg : 14) * 256 + sw0, vq1 = (bk + 8) * 3840 + (cg < 15 ? cg : 14) * 256 + sw1;
  const unsigned vA = (rt * 128 + 32 * lw + lr) * 1280 + ((ls ^ (lr >> 1)) << 4);
  const unsigned vA1 = (vA + 10240) ^ 64u, vA2 = vA + 20480, vA3 = (vA + 30720) ^ 64u;
  const int rkb = b / 5, rnb = b - 5 * rkb;
  if (w >= 4) {
    dma_stage(x + rot * 128, vA, vA1, vA2, vA3, lds0 + lw * 4096, wqkv + (size_t)rot * (64 * 3840), vq0, vq1, lds0 + 16384 + lw * 2048);
    if (b < 170) {
      const char* Wr = rkb < 24 ? w2 + (size_t)rkb * 64 * 1280 : wo + (size_t)(rkb - 24) * 64 * 1280;
      const unsigned r0 = bk * 1280 + rnb * 256 + sw0, r1 = (bk + 8) * 1280 + rnb * 256 + sw1;
      dma1(Wr, r0, lds0 + RT_OFF + lw * 2048);
      dma1(Wr, r1, lds0 + RT_OFF + lw * 2048 + 1024);
      dma1(Wr + 128, r0, lds0 + RT_OFF + 8192 + lw * 2048);
      dma1(Wr + 128, r1, lds0 + RT_OFF + 8192 + lw * 2048 + 1024);
    }
    const int kt = rot == 9 ? 0 : rot + 1;
    dma_stage(x + kt * 128, vA, vA1, vA2, vA3, lds0 + STG + lw * 4096, wqkv + (size_t)kt * (64 * 3840), vq0, vq1,
              lds0 + STG + 16384 + lw * 2048);
  }
  SB();
  const int g = lane >> 4, l15 = lane & 15, q4 = l15 >> 2, p4 = lane & 3;
  const char* pc0 = (const char*)(pc_ & ~(u64)63);
  const __amdgpu_buffer_rsrc_t rs = __builtin_amdgcn_make_buffer_rsrc(ws, 0, WS_USED, 0x00020000);
  LAS unsigned* sync = (LAS unsigned*)(smem + SYNC_OFF);
  u64 tag;
  {
    const char* p = ws;
    asm volatile("" : "+s"(p));
    tag = *(const CAS u64*)(p + OFF_FL) + 1;
  }
  const int lu1 = (6 * cg) & 3;
  const int sigl = ((q4 >> 1) & 1) | ((g & 1) << 1), xsw = g ^ (l15 >> 1);
  const int mt = (xcd >> 1) * 8 + (idx & 7), ct = (xcd & 1) * 4 + (idx >> 3);
  const int head = 4 * xcd + (idx & 3), qb = idx >> 2;
  const int ch = (5 * head) >> 1, odd = head & 1;
  auto orow_f = [&]() {
    unsigned ln = lane;
    asm volatile("" : "+v"(ln));
    return (64 * mt + 16 * (w & 3) + (ln & 15)) * 1280 + (80 * ct + 4 * (ln >> 4)) * 2;
  };
  u32x2 xv[3];
  f32x4 acc[4][5];
  const unsigned va = g * 1024 + l15 * 16;
  bf16x8 wpre[5][5];
  const unsigned wb = w < 4 ? OFF_W2F + (ct * 48 + 12 * w) * 5120 : OFF_WOF + (ct * 20 + 5 * (w - 4)) * 5120;

  if (w >= 4) {
    const int lt = tid - 256;
    const unsigned tg = (unsigned)tag;
    const unsigned vh0 = bk * 3072 + ((6 * cg) >> 2) * 128 + sw0, vh1 = (bk + 8) * 3072 + ((6 * cg) >> 2) * 128 + sw1;
    const int rot2 = rot + 6 >= 10 ? rot - 4 : rot + 6;
    int ktn = rot + 2 >= 10 ? rot - 8 : rot + 2;
    auto issue = [&](auto S) {
      constexpr int s = decltype(S)::value;
      const unsigned dst = lds0 + (s & 3) * STG;
      if (s == 10) ktn = rot2;
      const char* sw = s >= 10 ? w1 + (size_t)ktn * (64 * 3072) : wqkv + (size_t)ktn * (64 * 3840);
      if constexpr (s >= 10 && s < 14) dma_stage_w(sw, vh0, vh1, dst + 16384 + lw * 2048);
      else dma_stage(x + ktn * 128, vA, vA1, vA2, vA3, dst + lw * 4096, sw, s >= 10 ? vh0 : vq0, s >= 10 ? vh1 : vq1, dst + 16384 + lw * 2048);
      ktn = ktn == 9 ? 0 : ktn + 1;
    };
    if (lt < 16) sync[lt] = 0u;
    WAIT_VM(8);
    BAR();
    issue(IC<2>{}); issue(IC<3>{});
    const bf16x8 ONE8 = {0x3f80, 0x3f80, 0x3f80, 0x3f80, 0x3f80, 0x3f80, 0x3f80, 0x3f80};
    const f32x4 z4 = {0.f, 0.f, 0.f, 0.f};
    bf16x8 sw_[2][2], sx_[2][2];
    u32x4 gr[5], gc[3];
    u32x2 fl;
    const int fkb = lane < 12 ? 6 * lw + (lane >> 1) : 24 + ((5 * lw) >> 1) + (lane < 18 ? (lane - 12) >> 1 : 0);
    const unsigned fW = FL_W + (fkb * 5 + ((5 * ct + (lane & 1) * 4) >> 3)) * 128;
    const int ty = lane < 24 ? lane >> 3 : 0, rr0 = 2 * qb - 2 + ((lane >> 1) & 3);
    const int rr = rr0 < 0 ? 0 : (ty == 0 && rr0 < 2 * qb ? 2 * qb : rr0);
    const unsigned fQ = FL_Q + wg_of(rr, 5 * ty + ((ch + (lane & 1) * 2) >> 4)) * 128;
    unsigned ro[5], co[3];
#pragma unroll
    for (int k = 0; k < 5; ++k) ro[k] = OFF_RS + (rt * 10 + 2 * k + (lane >> 5)) * 2048 + (32 * lw + (lane & 31)) * 16;
#pragma unroll
    for (int k = 0; k < 3; ++k) co[k] = OFF_CS + (cg * 10 + (k == 2 && g >= 2 ? g : g + 4 * k)) * 1024 + (16 * lw + l15) * 16;
    const unsigned c_on = g < 2 ? 0xffffffffu : 0u;
    auto col_sums = [&](int part) {
#pragma unroll
      for (int k = 0; k < 3; ++k) PIN(gc[k]);
      float c0, c1;
      if (!sum_pairs<3>(gc, c_on, tg, c0, c1)) regather<3>(rs, co, c_on, tg, c0, c1);
      c0 += __shfl_xor(c0, 16); c1 += __shfl_xor(c1, 16);
      c0 += __shfl_xor(c0, 32); c1 += __shfl_xor(c1, 32);
      if (lane < 16) *(LAS f32x2*)(smem + CS_OFF + part * 512 + (16 * lw + lane) * 8) = f32x2{c0, c1};
      WAIT_LGKM0();
      lds_arrive(sync + 7 + part);
    };
    auto stage = [&](auto S) {
      constexpr int s = decltype(S)::value;
      if constexpr (s == 0 || s == 10) {
        const unsigned sb = lds0 + (s & 3) * STG;
#pragma unroll
        for (int i = 0; i < 2; ++i) {
          const int lux = ((s == 0 ? 0 : lu1) + 2 * lw + i) & 7;
          const unsigned wq = sb + 16384 + (lux >> 2) * 8192 + (8 * g + q4) * 128 + (((lux & 3) ^ sigl) << 5) + p4 * 8;
          sw_[i][0] = cat44(trread(wq), trread(wq + 512));
          sw_[i][1] = cat44(trread(wq + 4096), trread(wq + 4608));
          if (s == 0) {
            const unsigned xq = sb + (32 * lw + 16 * i + l15) * 128;
            sx_[i][0] = *(LAS bf16x8*)(size_t)(xq + (xsw << 4));
            sx_[i][1] = *(LAS bf16x8*)(size_t)(xq + ((xsw ^ 4) << 4));
          }
        }
      }
      constexpr int younger[18] = {16, 16, 16, 16, 16, 16, 24, 20, 8, 8, 8 + 5 * WOPRE, 12 + 5 * WOPRE, 16 + 5 * WOPRE, 16, 16, 19, 19, 0};
      wait_vm<younger[s]>();
      WAIT_LGKM0();
      BAR();
      if constexpr (s < 16) issue(IC<s + 4>{});
      if constexpr (s == 2 || s == 3) {
        if (b < 170) {
          const unsigned dstb = rkb < 24 ? OFF_W2F : OFF_WOF;
          const int nks = rkb < 24 ? 48 : 20, kb = rkb < 24 ? rkb : rkb - 24;
          const int nl = 2 * lw + s - 2, nt = 8 * rnb + nl, c5 = nt / 5, jj = nt - 5 * c5;
          const unsigned rp = lds0 + RT_OFF + (nl >> 2) * 8192 + (8 * g + q4) * 128 + (((nl & 3) ^ sigl) << 5) + p4 * 8;
#pragma unroll
          for (int ks = 0; ks < 2; ++ks) {
            const bf16x8 f = cat44(trread(rp + ks * 4096), trread(rp + ks * 4096 + 512));
            st16(__builtin_bit_cast(u32x4, f), rs, lane * 16, dstb + ((c5 * nks + 2 * kb + ks) * 5 + jj) * 1024);
          }
        }
      }
      if constexpr (s == 0 || s == 10) {
        __builtin_amdgcn_s_setprio(3);
        const u32x4 c0 = __builtin_bit_cast(u32x4, mfma16(sw_[0][1], ONE8, mfma16(sw_[0][0], ONE8, z4)));
        const u32x4 c1 = __builtin_bit_cast(u32x4, mfma16(sw_[1][1], ONE8, mfma16(sw_[1][0], ONE8, z4)));
        const u32x4 cu = l15 == 0 ? c0 : c1;
        if (l15 < 2) {
          const unsigned o = OFF_CS + (((s == 10 ? 16 : 0) + cg) * 10 + (s == 10 ? rot2 : rot)) * 1024 + 2 * lw * 128;
          st16(u32x4{cu[0], tg, cu[1], tg}, rs, l15 * 128 + g * 32, o);
          st16(u32x4{cu[2], tg, cu[3], tg}, rs, l15 * 128 + g * 32 + 16, o);
        }
        __builtin_amdgcn_s_setprio(0);
      }
      if constexpr (s == 1) {
        __builtin_amdgcn_s_setprio(3);
#pragma unroll
        for (int i = 0; i < 2; ++i) {
          const f32x4 sx = mfma16(ONE8, sx_[i][1], mfma16(ONE8, sx_[i][0], z4));
          const f32x4 sq = mfma16(sx_[i][1], sx_[i][1], mfma16(sx_[i][0], sx_[i][0], z4));
          const int r = l15 & 3;
          const float sxx = r == 0 ? sq[0] : (r == 1 ? sq[1] : (r == 2 ? sq[2] : sq[3]));
          if (q4 == g)
            st16(u32x4{__float_as_uint(sx[0]), tg, __float_as_uint(sxx), tg}, rs, (32 * lw + 16 * i + l15) * 16,
                 OFF_RS + (rt * 10 + rot) * 2048);
        }
        __builtin_amdgcn_s_setprio(0);
      }
      if constexpr (s == 4) {
        if (lt < 12) dma1(b1 + cg * 192, lane * 16, lds0 + B1_OFF);
        if (lw == 1 && lane < 10) dma1(b2 + ct * 160, lane * 16, lds0 + B1_OFF + 256);
      }
      if constexpr (s == 6 || s == 11) {
        if (lt == 0 && (s == 6 || cg < 15)) __hip_atomic_store((GAS u64*)(ws + (s == 6 ? FL_W : FL_Q) + b * 128), tag, RLX_AGENT);
      }
      if constexpr (s == 5) {
#pragma unroll
        for (int k = 0; k < 5; ++k) gr[k] = aload16(ws, ro[k]);
      }
      if constexpr (s == 5 || s == 14) {
#pragma unroll
        for (int k = 0; k < 3; ++k) { if (s == 14) co[k] += 16 * 10240; gc[k] = aload16(ws, co[k]); }
      }
      if constexpr (s == 8) {
        __builtin_amdgcn_s_setprio(3);
#pragma unroll
        for (int k = 0; k < 5; ++k) PIN(gr[k]);
        float sx, sxx;
        if (!sum_pairs<5>(gr, 0xffffffffu, tg, sx, sxx)) regather<5>(rs, ro, 0xffffffffu, tg, sx, sxx);
        sx += __shfl_xor(sx, 32); sxx += __shfl_xor(sxx, 32);
        const float mean = sx * (1.f / 640.f);
        float var = sxx * (1.f / 640.f) - mean * mean;
        var = var < 0.f ? 0.f : var;
        if (lane < 32) *(LAS f32x2*)(smem + ST_OFF + (32 * lw + lane) * 8) = f32x2{mean, rsqrtf(var + 1e-5f)};
        col_sums(0);
        __builtin_amdgcn_s_setprio(0);
      }
      if constexpr (s == 9) {
        poll_flags(ws + fW, true, tag);
#pragma unroll
        for (int k = 0; k < WOPRE; ++k)
#pragma unroll
          for (int j = 0; j < 5; ++j) wpre[k][j] = ld16(rs, lane * 16, wb + k * 5120 + j * 1024);
      }
      if constexpr (s == 16) fl = aload8(ws, fQ);
    };
    stage(IC<0>{}); stage(IC<1>{}); stage(IC<2>{}); stage(IC<3>{}); stage(IC<4>{}); stage(IC<5>{});
    stage(IC<6>{}); stage(IC<7>{}); stage(IC<8>{}); stage(IC<9>{}); stage(IC<10>{}); stage(IC<11>{});
    stage(IC<12>{}); stage(IC<13>{}); stage(IC<14>{}); stage(IC<15>{}); stage(IC<16>{}); stage(IC<17>{});
    PIN(fl);
    if (!__all(fl[0] == tg && fl[1] == (unsigned)(tag >> 32))) poll_flags(ws + fQ, true, tag);
#pragma unroll
    for (int i = 0; i < 2; ++i) {
      const int seg = 2 * lw + i;
      int tb = 4 * qb - 4 + seg; tb = tb < 0 ? 0 : tb;
      dma_kv(ws + OFF_QKV + (size_t)(tb * 240 + 80 + ch) * 1024, lane * 16, lane * 16 + 81920, lds0 + K_OFF + seg * 1024);
    }
    const int l31 = lane & 31, hh = lane >> 5;
    u32x4 qraw[2][2];
#pragma unroll
    for (int a = 0; a < 2; ++a)
#pragma unroll
      for (int ks = 0; ks < 2; ++ks)
        qraw[a][ks] = __builtin_amdgcn_raw_buffer_load_b128(rs, (ks ? 2 : hh) * 1024 + (32 * a + l31) * 16,
                                                            OFF_QKV + ((4 * qb + lw) * 240 + ch) * 1024, SC1);
    col_sums(1);
    {
      const u32x4 one4 = {0x3f803f80u, 0x3f803f80u, 0x3f803f80u, 0x3f803f80u};
      *(LAS u32x4*)(smem + V_OFF + 3 * VP + lt * 16) = one4;
      if (lt < 40) *(LAS u32x4*)(smem + V_OFF + 3 * VP + 4096 + lt * 16) = one4;
      *(LAS u32x4*)(smem + K_OFF + 3 * KP + lt * 16) = u32x4{qb == 0 ? 0xff80u : 0u, 0u, 0u, 0u};
      *(LAS u32x4*)(smem + K_OFF + 3 * KP + 4096 + lt * 16) = u32x4{0u, 0u, 0u, 0u};
    }
    bf16x8 qf[2][2];
#pragma unroll
    for (int a = 0; a < 2; ++a) {
      u32x4 v0 = qraw[a][0], v1 = qraw[a][1];
      if (odd && hh == 0) { v0[0] = 0u; v0[1] = 0u; }
      if (hh) v1 = u32x4{0x3f80u, 0u, 0u, 0u};
      else if (!odd) { v1[2] = 0u; v1[3] = 0u; }
      qf[a][0] = __builtin_bit_cast(bf16x8, v0);
      qf[a][1] = __builtin_bit_cast(bf16x8, v1);
    }
    f32x16 mF, z16;
#pragma unroll
    for (int e = 0; e < 16; ++e) {
      mF[e] = (e & 3) + 8 * (e >> 2) + 4 * hh > l31 ? 0.f : -INFINITY;
      z16[e] = 0.f;
    }
    asm volatile("" : "+v"(mF), "+v"(qf[0][0]), "+v"(qf[0][1]), "+v"(qf[1][0]), "+v"(qf[1][1]));
    WAIT_VM(0);
    WAIT_LGKM0();
    lds_arrive(sync);
    lds_wait(sync, 4);
    __builtin_amdgcn_s_setprio(2);
    unsigned kp = lds0 + K_OFF + hh * KP + (64 * lw + l31) * 16;
    const bool ones = (g & 1) && (p4 >> 1);
    unsigned vp = lds0 + V_OFF + (2 * (g & 1) + (p4 >> 1)) * VP + ((ones ? 0 : 64 * lw) + 4 * hh + q4) * 16 + (p4 & 1) * 8;
    unsigned ov = l31 * 16 + hh * 8;
    bf16x8 q0 = qf[0][0], q1 = qf[0][1];
#define LDK(T, ks) (*(LAS bf16x8*)(size_t)(kp + (T) * 512 + (ks) * 2 * KP))
#define MF32(a, b, c) __builtin_amdgcn_mfma_f32_32x32x16_bf16(a, b, c, 0, 0, 0)
#define EX2(s_, e) pk2(__builtin_amdgcn_exp2f((s_)[e]), __builtin_amdgcn_exp2f((s_)[(e) + 1]))
    auto tile_max = [&](int T) {
      f32x16 sp = MF32(LDK(T, 1), q1, MF32(LDK(T, 0), q0, z16));
      float m0 = -INFINITY, m1 = -INFINITY;
#pragma unroll
      for (int e = 0; e < 16; ++e) {
        const bool off = T == 0 ? mF[e] != 0.f : (T == 8 ? mF[e] == 0.f : false);
        sp[e] = off ? -INFINITY : sp[e];
      }
#pragma unroll
      for (int e = 0; e < 16; e += 4) { m0 = MAX3(m0, sp[e], sp[e + 1]); m1 = MAX3(m1, sp[e + 2], sp[e + 3]); }
      m0 = fmaxf(m0, m1);
      return fmaxf(m0, __shfl_xor(m0, 32));
    };
#pragma unroll 1
    for (int un = 0; un < 2; ++un) {
      float mx = tile_max(8);
      f32x16 oa;
#pragma unroll 1
      for (int pass = 0; pass < 2; ++pass) {
      f32x16 ca;
#pragma unroll
      for (int e = 0; e < 16; ++e) ca[e] = -mx;
      oa = z16;
      {
        bf16x8 kc[2][2], vf[2][2];
        f32x16 sc[2];
        u32x4 pa[2] = {{0u, 0u, 0u, 0u}, {0u, 0u, 0u, 0u}};
        kc[0][0] = LDK(0, 0); kc[0][1] = LDK(0, 1); kc[1][0] = LDK(1, 0); kc[1][1] = LDK(1, 1);
        sc[0] = MF32(kc[0][1], q1, MF32(kc[0][0], q0, ca + mF));
        sc[1] = z16;
        asm volatile("" :: "v"(sc[0]));
        SB();
#pragma unroll
        for (int T = 0; T < 10; ++T) {
          const int c = T & 1, n = c ^ 1;
          if (T < 9) {
#pragma unroll
            for (int ks = 0; ks < 2; ++ks) vf[c][ks] = cat44(trread(vp + T * 512 + ks * 256), trread(vp + T * 512 + ks * 256 + 128));
          }
          if (T < 8) sc[n] = MF32(kc[n][1], q1, MF32(kc[n][0], q0, ca));
          if (T < 7) { kc[c][0] = LDK(T + 2, 0); kc[c][1] = LDK(T + 2, 1); }
          if (T > 0) oa = MF32(vf[n][1], __builtin_bit_cast(bf16x8, pa[1]), MF32(vf[n][0], __builtin_bit_cast(bf16x8, pa[0]), oa));
          if (T == 8) {
#pragma unroll
            for (int e = 0; e < 16; ++e) sc[c][e] = mF[e] == 0.f ? -INFINITY : sc[c][e];
          }
          if (T < 9) {
#pragma unroll
            for (int e = 0; e < 4; ++e) { pa[0][e] = EX2(sc[c], 2 * e); pa[1][e] = EX2(sc[c], 8 + 2 * e); }
          }
          SGB(0x100, 6);
          if (T == 8) SGB(0x2, 16);
          SGB(0x8, 1); SGB(0x400, 4); SGB(0x2, 1);
#pragma unroll
          for (int k = 0; k < 3; ++k) { SGB(0x8, 1); SGB(0x400, 2); SGB(0x2, 1); SGB(0x400, 2); SGB(0x2, 1); }
          SGB(0x2, 1);
          asm volatile("" :: "v"(sc[n]), "v"(oa), "v"(pa[0]), "v"(pa[1]));
          SB();
        }
      }
      if (pass || __all(oa[12] < 1.8446744e19f)) break;
#pragma unroll 1
      for (int T = 0; T < 8; ++T) mx = fmaxf(mx, tile_max(T));
      }
      const float il = __builtin_amdgcn_rcpf(oa[12]);
#pragma unroll
      for (int pl = 0; pl < 3; ++pl) {
        const bool ok = pl == 1 || (pl == 0 ? !(odd && hh == 0) : (odd || hh == 0));
        if (ok) {
          const u32x2 pkd = {pk2(oa[4 * pl] * il, oa[4 * pl + 1] * il), pk2(oa[4 * pl + 2] * il, oa[4 * pl + 3] * il)};
          __builtin_amdgcn_raw_buffer_store_b64(pkd, rs, pl * 1024 + ov, OFF_O + ((4 * qb + lw) * 80 + ch) * 1024, SC1);
        }
      }
      kp += 512; vp += ones ? 0 : 512; ov += 512;
      q0 = qf[1][0]; q1 = qf[1][1];
    }
    __builtin_amdgcn_s_setprio(0);
    lds_arrive(sync + 4);
    WAIT_VM(0);
    if (lane == 0) __hip_atomic_store((GAS u64*)(ws + FL_O + (b * 4 + lw) * 128), tag, RLX_AGENT);
#pragma unroll
    for (int k = WOPRE; k < 5; ++k)
#pragma unroll
      for (int j = 0; j < 5; ++j) wpre[k][j] = ld16(rs, lane * 16, wb + k * 5120 + j * 1024);
    const unsigned orow = orow_f();
#pragma unroll
    for (int j = 0; j < 2; ++j) xv[j] = *(const u32x2*)(x + orow + (3 + j) * 32);
    const unsigned ab = OFF_O + (mt * 80 + 20 * lw) * 1024;
    {
      const int hd = 8 * lw + (lane & 7);
      poll_flags(ws + FL_O + ((((((mt >> 2) << 2) | (hd & 3)) << 3) | (hd >> 2)) * 4 + (mt & 3)) * 128, lane < 8, tag);
    }
    bf16x8 af[4][5];
#pragma unroll
    for (int i = 0; i < 4; ++i)
#pragma unroll
      for (int k = 0; k < 5; ++k) af[i][k] = ld16(rs, va + ((lw + 1 + i) & 3) * 256, ab + k * 4096);
    SB();
    lds_wait(sync + 4, 4);
    lds_wait(sync + 6, 4);
    f32x4 own[2];
#pragma unroll
    for (int i = 0; i < 4; ++i) {
      const int mi = (lw + 1 + i) & 3;
      f32x4 a5[5] = {z4, z4, z4, z4, z4};
#pragma unroll
      for (int k = 0; k < 5; ++k)
#pragma unroll
        for (int j = 0; j < 5; ++j) a5[j] = mfma16(wpre[k][j], af[i][k], a5[j]);
#pragma unroll
      for (int j = 0; j < 3; ++j) *(LAS f32x4*)(smem + REDS1_OFF + ((mi * 4 + lw) * 3 + j) * 1024 + lane * 16) = a5[j];
      if (i < 3) {
#pragma unroll
        for (int j = 0; j < 2; ++j) *(LAS f32x4*)(smem + REDS2_OFF + ((mi * 3 + 2 - i) * 2 + j) * 1024 + lane * 16) = a5[3 + j];
      } else { own[0] = a5[3]; own[1] = a5[4]; }
    }
    __syncthreads();
#pragma unroll
    for (int j = 0; j < 2; ++j) {
      f32x4 sv = own[j];
#pragma unroll
      for (int k = 0; k < 3; ++k) sv += *(LAS f32x4*)(smem + REDS2_OFF + ((lw * 3 + k) * 2 + j) * 1024 + lane * 16);
      const f32x4 gt = *(LAS f32x4*)(smem + GX_OFF + (lw * 2 + j) * 1024 + lane * 16);
      *(u32x2*)(out + orow + (3 + j) * 32) = fin4(xv[j], sv, gt);
    }

  } else {
    if (idx * 4 + w < CODE_KB) {
      const u32x4 t0 = *(const u32x4*)(pc0 + (idx * 4 + w) * 1024 + lane * 16);
      asm volatile("" :: "v"(t0));
    }
    {
      const int mw = w & 1, nw = w >> 1;
      unsigned xa[2];
      xa[0] = lds0 + (64 * mw + l15) * 128 + (xsw << 4);
      xa[1] = lds0 + (64 * mw + l15) * 128 + ((xsw ^ 4) << 4);
      const unsigned wl = lds0 + 16384 + (8 * g + q4) * 128 + p4 * 8;
      const unsigned vo = (g >> 1) * 1024 + (16 * (g & 1) + l15) * 16;
      float sh[4], sc[4];
      BAR();
      mfma_part<4, 0>(xa, wl, 4 * nw, sigl, lds0, sh, sc, 64 * mw + l15, cg < 5 ? 0.22360679774997896f * 1.4426950408889634f : 1.f, rs, vo,
                      cg < 15 ? OFF_QKV + ((2 * rt + mw) * 240 + 16 * cg + 8 * nw) * 1024 : 0x80000000u, 4 * nw, g);
      mfma_part<3, 10>(xa, wl, lu1 + 3 * nw, sigl, lds0, sh, sc, 0, 1.f, rs, vo,
                       OFF_H + ((2 * rt + mw) * 192 + 12 * cg + 6 * nw) * 1024, 3 * nw, g);
    }
    WAIT_VM(0);
    if (lds_arrive(sync + 2) == 3 && lane == 0) __hip_atomic_store((GAS u64*)(ws + FL_H + b * 128), tag, RLX_AGENT);
#pragma unroll
    for (int k = 0; k < 4; ++k)
#pragma unroll
      for (int j = 0; j < 5; ++j) wpre[k][j] = ld16(rs, lane * 16, wb + k * 5120 + j * 1024);
    const unsigned wl_ = lds0 + slab_of(w);
    lds_wait(sync + 2, 4);
    dma5_sc1(ws + wb + 4 * 5120, lane * 16, wl_);
    dma5_sc1(ws + wb + 5 * 5120, lane * 16, wl_ + 5120);
    const unsigned ab = OFF_H + (mt * 192 + 48 * w) * 1024;
    if (b == 0 && w == 0) {
#pragma unroll 1
      for (int spins = 0; spins < (1 << 20); ++spins) {
        u64 v[4];
#pragma unroll
        for (int i = 0; i < 4; ++i) v[i] = __hip_atomic_load((const GAS u64*)(ws + FL_W + (lane + 64 * i) * 128), RLX_AGENT);
        if (__all(((v[0] ^ tag) | (v[1] ^ tag) | (v[2] ^ tag) | (v[3] ^ tag)) == 0)) break;
        __builtin_amdgcn_s_sleep(1);
      }
      if (lane == 0) __hip_atomic_store((GAS u64*)(ws + OFF_FL), tag, RLX_AGENT);
    }
    poll_flags(ws + FL_H + wg_of(mt >> 1, 4 * w + (lane & 3)) * 128, lane < 4, tag);
    unsigned vi[4];
#pragma unroll
    for (int i = 0; i < 4; ++i) vi[i] = va + ((w + 1 + i) & 3) * 256;
    dma5_sc1(ws + ab + 4 * 4096, lane * 16, wl_ + 10240);
    dma5_sc1(ws + ab + 4 * 4096 + 3072, lane * 16, wl_ + 10240 + 3072);
    bf16x8 af[4][4];
#pragma unroll
    for (int k = 0; k < 4; ++k)
#pragma unroll
      for (int i = 0; i < 4; ++i) af[k][i] = ld16(rs, vi[i], ab + k * 4096);
    SB();
#pragma unroll
    for (int i = 0; i < 4; ++i)
#pragma unroll
      for (int j = 0; j < 5; ++j) acc[i][j] = f32x4{0.f, 0.f, 0.f, 0.f};
#pragma unroll
    for (int k = 0; k < 12; ++k) {
      const int cs = k & 3;
      if (k == 6)
        asm volatile("s_mov_b64 exec, 1\n\tds_add_u32 %0, %1\n\ts_mov_b64 exec, -1" :: "v"(lds0 + SYNC_OFF + 24), "v"(1u) : "memory");
      if (k == W2HOLD) {
        unsigned tv, ts;
        asm volatile(".Lp%=:\n\tds_read_b32 %0, %2\n\ts_waitcnt lgkmcnt(0)\n\tv_readfirstlane_b32 %1, %0\n\t"
                     "s_cmp_ge_u32 %1, 4\n\ts_cbranch_scc1 .Le%=\n\ts_sleep 2\n\ts_branch .Lp%=\n.Le%=:"
                     : "=&v"(tv), "=&s"(ts) : "v"(lds0 + SYNC_OFF + 16) : "memory", "scc");
      }
#pragma unroll
      for (int i = 0; i < 4; ++i) {
#pragma unroll
        for (int j = 0; j < 5; ++j) acc[i][j] = mfma16(wpre[cs][j], af[cs][i], acc[i][j]);
        if (k == 11 && i < 3) {
#pragma unroll
          for (int j = 0; j < 5; ++j)
            *(LAS f32x4*)(smem + REDG_OFF + ((((w + 1 + i) & 3) * 3 + 2 - i) * 5 + j) * 1024 + lane * 16) = acc[i][j];
        }
      }
      SB();
      if (k < 2) {
#pragma unroll
        for (int j = 0; j < 5; ++j) wpre[cs][j] = *(LAS bf16x8*)(size_t)(wl_ + k * 5120 + j * 1024 + lane * 16);
#pragma unroll
        for (int i = 0; i < 4; ++i) af[cs][i] = *(LAS bf16x8*)(size_t)(wl_ + 10240 + k * 4096 + vi[i]);
      } else if (k < 8) {
#pragma unroll
        for (int i = 0; i < 4; ++i) af[cs][i] = ld16(rs, vi[i], ab + (k + 4) * 4096);
#pragma unroll
        for (int j = 0; j < 5; ++j) wpre[cs][j] = ld16(rs, lane * 16, wb + (k + 4) * 5120 + j * 1024);
      }
      if (k == 10) {
        const unsigned orow = orow_f();
#pragma unroll
        for (int j = 0; j < 3; ++j) xv[j] = *(const u32x2*)(x + orow + j * 32);
      }
      SB();
    }
    u32x2 bb[5];
#pragma unroll
    for (int j = 0; j < 5; ++j) bb[j] = *(LAS u32x2*)(smem + B1_OFF + 256 + (16 * j + 4 * g) * 2);
    WAIT_LGKM0();
    lds_arrive(sync + 3);
    lds_wait(sync + 3, 4);
    f32x4 gt[5];
#pragma unroll
    for (int j = 0; j < 5; ++j) {
      gt[j] = acc[3][j];
#pragma unroll
      for (int k = 0; k < 3; ++k) gt[j] += *(LAS f32x4*)(smem + REDG_OFF + ((w * 3 + k) * 5 + j) * 1024 + lane * 16);
    }
#pragma unroll
    for (int j = 0; j < 5; ++j) {
      const float bv[4] = {bflo(bb[j][0]), bfhi(bb[j][0]), bflo(bb[j][1]), bfhi(bb[j][1])};
#pragma unroll
      for (int e = 0; e < 4; ++e) {
        const float t = rbf(rbf(gt[j][e]) + bv[e]);
        gt[j][e] = rbf(t * __builtin_amdgcn_rcpf(1.f + __expf(-t)));
      }
      if (j >= 3) *(LAS f32x4*)(smem + GX_OFF + (w * 2 + j - 3) * 1024 + lane * 16) = gt[j];
    }
    __syncthreads();
    const unsigned orow = orow_f();
#pragma unroll
    for (int j = 0; j < 3; ++j) {
      f32x4 sv = *(LAS f32x4*)(smem + REDS1_OFF + ((w * 4) * 3 + j) * 1024 + lane * 16);
#pragma unroll
      for (int k = 1; k < 4; ++k) sv += *(LAS f32x4*)(smem + REDS1_OFF + ((w * 4 + k) * 3 + j) * 1024 + lane * 16);
      *(u32x2*)(out + orow + j * 32) = fin4(xv[j], sv, gt[j]);
    }
    __builtin_amdgcn_endpgm();
  }
}

extern "C" void kernel_launch(void* const* d_in, const int* in_sizes, int n_in, void* d_out, int out_size, void* d_ws,
                              size_t ws_size, hipStream_t stream) {
  (void)hipFuncSetAttribute((const void*)k_all, hipFuncAttributeMaxDynamicSharedMemorySize, LDS_ALL);
  k_all<<<256, 512, LDS_ALL, stream>>>((const char*)d_in[0], (const char*)d_in[1], (const char*)d_in[4], (const char*)d_in[5],
                                       (const char*)d_in[6], (const char*)d_in[2], (const char*)d_in[7], (char*)d_ws, (char*)d_out);
}
```

```cpp
#include <hip/hip_runtime.h>
#include <stdint.h>

#define LAS __attribute__((address_space(3)))
#define GAS __attribute__((address_space(1)))
#define CAS __attribute__((address_space(4)))
typedef short bf16x8 __attribute__((ext_vector_type(8)));
typedef short s16x4 __attribute__((ext_vector_type(4)));
typedef float f32x4 __attribute__((ext_vector_type(4)));
typedef unsigned u32x4 __attribute__((ext_vector_type(4)));
typedef unsigned u32x2 __attribute__((ext_vector_type(2)));
typedef __bf16 bf16x2_t __attribute__((ext_vector_type(2)));
typedef float f32x2 __attribute__((ext_vector_type(2)));
typedef float f32x16 __attribute__((ext_vector_type(16)));
typedef unsigned long long u64;

constexpr unsigned OFF_FL = 0;
constexpr unsigned FL_W = 1024, FL_Q = FL_W + 32768, FL_H = FL_Q + 32768, FL_O = FL_H + 32768;
constexpr unsigned OFF_RS = 262144;
constexpr unsigned OFF_CS = OFF_RS + 16 * 10 * 2048;
constexpr unsigned OFF_QKV = OFF_CS + 2 * 16 * 10 * 1024;
constexpr unsigned OFF_H = OFF_QKV + 32 * 240 * 1024;
constexpr unsigned OFF_O = OFF_H + 32 * 192 * 1024;
constexpr unsigned OFF_W2F = OFF_O + 32 * 80 * 1024;
constexpr unsigned OFF_WOF = OFF_W2F + 8 * 48 * 5120;
constexpr unsigned WS_USED = OFF_WOF + 8 * 20 * 5120;

__device__ __forceinline__ f32x4 mfma16(bf16x8 a, bf16x8 b, f32x4 c) {
  return __builtin_amdgcn_mfma_f32_16x16x32_bf16(a, b, c, 0, 0, 0);
}
__device__ __forceinline__ unsigned pk2(float a, float b) {
  f32x2 v = {a, b};
  bf16x2_t r = __builtin_convertvector(v, bf16x2_t);
  return __builtin_bit_cast(unsigned, r);
}
__device__ __forceinline__ float rbf(float a) { return __builtin_bit_cast(float, pk2(0.f, a) & 0xffff0000u); }
__device__ __forceinline__ float bflo(unsigned u) { return __builtin_bit_cast(float, u << 16); }
__device__ __forceinline__ float bfhi(unsigned u) { return __builtin_bit_cast(float, u & 0xffff0000u); }
__device__ __forceinline__ u32x2 fin4(u32x2 xv, f32x4 sv, f32x4 gt) {
  return u32x2{pk2(bflo(xv[0]) + rbf(rbf(sv[0]) * gt[0]), bfhi(xv[0]) + rbf(rbf(sv[1]) * gt[1])),
               pk2(bflo(xv[1]) + rbf(rbf(sv[2]) * gt[2]), bfhi(xv[1]) + rbf(rbf(sv[3]) * gt[3]))};
}
__device__ __forceinline__ s16x4 trread(unsigned a) {
  return __builtin_amdgcn_ds_read_tr16_b64_v4i16((LAS s16x4*)(size_t)a);
}
__device__ __forceinline__ bf16x8 cat44(s16x4 a, s16x4 b) {
  return __builtin_shufflevector(a, b, 0, 1, 2, 3, 4, 5, 6, 7);
}
#define WAIT_VM(n) asm volatile("s_waitcnt vmcnt(" #n ")" ::: "memory")
#define WAIT_LGKM0() __builtin_amdgcn_s_waitcnt(0xC07F)
#define BAR() __builtin_amdgcn_s_barrier()
#define SB() __builtin_amdgcn_sched_barrier(0)
#define SGB(m, n) __builtin_amdgcn_sched_group_barrier(m, n, 0)
#define MAX3(a, b, c) __builtin_fmaxf(__builtin_fmaxf(a, b), c)
#define RLX_AGENT __ATOMIC_RELAXED, __HIP_MEMORY_SCOPE_AGENT
constexpr int SC1 = 16;
constexpr int CODE_KB = 38;
template <int N> struct IC { static constexpr int value = N; };

__device__ __forceinline__ void dma1(const char* sbase, unsigned voff, unsigned lds) {
  unsigned keep;
  asm volatile("s_mov_b32 %0, m0\n\ts_mov_b32 m0, %3\n\ts_nop 2\n\tglobal_load_lds_dwordx4 %1, %2\n\ts_mov_b32 m0, %0"
               : "=&s"(keep) : "v"(voff), "s"(sbase), "s"(lds) : "memory");
}
__device__ __forceinline__ void dma_stage(const char* sa, unsigned va0, unsigned va1, unsigned va2, unsigned va3, unsigned la,
                                          const char* sb, unsigned vb0, unsigned vb1, unsigned lb) {
  unsigned keep;
  asm volatile("s_mov_b32 %0, m0\n\ts_mov_b32 m0, %10\n\ts_nop 2\n\tglobal_load_lds_dwordx4 %8, %7\n\t"
               "s_add_u32 m0, m0, 1024\n\ts_nop 0\n\tglobal_load_lds_dwordx4 %9, %7\n\t"
               "s_add_u32 m0, m0, 7040\n\ts_nop 0\n\tglobal_load_lds_dwordx4 %8, %7 offset:128\n\t"
               "s_add_u32 m0, m0, 1024\n\ts_nop 0\n\tglobal_load_lds_dwordx4 %9, %7 offset:128\n\t"
               "s_mov_b32 m0, %6\n\ts_nop 0\n\tglobal_load_lds_dwordx4 %2, %1\n\t"
               "s_add_u32 m0, m0, 1024\n\ts_nop 0\n\tglobal_load_lds_dwordx4 %3, %1\n\t"
               "s_add_u32 m0, m0, 1024\n\ts_nop 0\n\tglobal_load_lds_dwordx4 %4, %1\n\t"
               "s_add_u32 m0, m0, 1024\n\ts_nop 0\n\tglobal_load_lds_dwordx4 %5, %1\n\ts_mov_b32 m0, %0"
               : "=&s"(keep) : "s"(sa), "v"(va0), "v"(va1), "v"(va2), "v"(va3), "s"(la), "s"(sb), "v"(vb0), "v"(vb1), "s"(lb)
               : "memory", "scc");
}
__device__ __forceinline__ void dma_stage_w(const char* sb, unsigned vb0, unsigned vb1, unsigned lb) {
  unsigned keep;
  asm volatile("s_mov_b32 %0, m0\n\ts_mov_b32 m0, %4\n\ts_nop 2\n\tglobal_load_lds_dwordx4 %2, %1\n\t"
               "s_add_u32 m0, m0, 1024\n\ts_nop 0\n\tglobal_load_lds_dwordx4 %3, %1\n\t"
               "s_add_u32 m0, m0, 7040\n\ts_nop 0\n\tglobal_load_lds_dwordx4 %2, %1 offset:128\n\t"
               "s_add_u32 m0, m0, 1024\n\ts_nop 0\n\tglobal_load_lds_dwordx4 %3, %1 offset:128\n\ts_mov_b32 m0, %0"
               : "=&s"(keep) : "s"(sb), "v"(vb0), "v"(vb1), "s"(lb) : "memory", "scc");
}
__device__ __forceinline__ void dma_kv(const char* sb, unsigned vk, unsigned vv, unsigned l) {
  unsigned keep;
  asm volatile("s_mov_b32 %0, m0\n\ts_mov_b32 m0, %4\n\ts_nop 2\n\tglobal_load_lds_dwordx4 %2, %1 sc1\n\t"
               "s_add_u32 m0, m0, 7168\n\ts_nop 0\n\tglobal_load_lds_dwordx4 %2, %1 offset:1024 sc1\n\t"
               "s_add_u32 m0, m0, 7168\n\ts_nop 0\n\tglobal_load_lds_dwordx4 %2, %1 offset:2048 sc1\n\t"
               "s_add_u32 m0, m0, 18432\n\ts_nop 0\n\tglobal_load_lds_dwordx4 %3, %1 sc1\n\t"
               "s_add_u32 m0, m0, 7232\n\ts_nop 0\n\tglobal_load_lds_dwordx4 %3, %1 offset:1024 sc1\n\t"
               "s_add_u32 m0, m0, 7232\n\ts_nop 0\n\tglobal_load_lds_dwordx4 %3, %1 offset:2048 sc1\n\t"
               "s_mov_b32 m0, %0"
               : "=&s"(keep) : "s"(sb), "v"(vk), "v"(vv), "s"(l) : "memory", "scc");
}
__device__ __forceinline__ void dma5_sc1(const char* sb, unsigned v, unsigned l) {
  unsigned keep;
  asm volatile("s_mov_b32 %0, m0\n\ts_mov_b32 m0, %3\n\ts_nop 2\n\tglobal_load_lds_dwordx4 %2, %1 sc1\n\t"
               "global_load_lds_dwordx4 %2, %1 offset:1024 sc1\n\tglobal_load_lds_dwordx4 %2, %1 offset:2048 sc1\n\t"
               "global_load_lds_dwordx4 %2, %1 offset:3072 sc1\n\ts_add_u32 m0, m0, 1024\n\ts_nop 0\n\t"
               "global_load_lds_dwordx4 %4, %1 offset:3072 sc1\n\ts_mov_b32 m0, %0"
               : "=&s"(keep) : "s"(sb), "v"(v), "s"(l), "v"(v + 1024) : "memory", "scc");
}
__device__ __forceinline__ u32x2 aload8(const char* sbase, unsigned voff) {
  u32x2 r;
  asm volatile("s_nop 4\n\tglobal_load_dwordx2 %0, %1, %2 sc1" : "=v"(r) : "v"(voff), "s"(sbase) : "memory");
  return r;
}
__device__ __forceinline__ u32x4 aload16(const char* sbase, unsigned voff) {
  u32x4 r;
  asm volatile("s_nop 4\n\tglobal_load_dwordx4 %0, %1, %2 sc1" : "=v"(r) : "v"(voff), "s"(sbase) : "memory");
  return r;
}
#define PIN(x) asm volatile("" : "+v"(x))
template <int N> __device__ __forceinline__ void wait_vm() { asm volatile("s_waitcnt vmcnt(%0)" :: "i"(N) : "memory"); }
__device__ __forceinline__ void poll_flags(const char* flag, bool need, u64 tag) {
#pragma unroll 1
  for (int spins = 0; spins < (1 << 20); ++spins) {
    const u64 v = need ? __hip_atomic_load((const GAS u64*)flag, RLX_AGENT) : tag;
    if (__all(v == tag)) break;
    __builtin_amdgcn_s_sleep(1);
  }
  __builtin_amdgcn_fence(__ATOMIC_ACQUIRE, "wavefront");
}
__device__ __forceinline__ unsigned lds_arrive(LAS unsigned* c) {
  unsigned r = 0;
  if ((threadIdx.x & 63) == 0) r = __hip_atomic_fetch_add(c, 1u, __ATOMIC_RELAXED, __HIP_MEMORY_SCOPE_WORKGROUP);
  return __builtin_amdgcn_readfirstlane(r);
}
__device__ __forceinline__ void lds_wait(LAS unsigned* c, unsigned n) {
#pragma unroll 1
  for (int spins = 0; spins < (1 << 22); ++spins) {
    if (__builtin_amdgcn_readfirstlane(__hip_atomic_load(c, __ATOMIC_RELAXED, __HIP_MEMORY_SCOPE_WORKGROUP)) >= n) break;
  }
  __builtin_amdgcn_fence(__ATOMIC_ACQUIRE, "wavefront");
}

constexpr int WOPRE = 4;
constexpr int W2HOLD = 10;
constexpr int STG = 32768, RT_OFF = 4 * STG, LDS_ALL = 163840;
constexpr int B1_OFF = 159744;
constexpr int ST_OFF = 160768;
constexpr int CS_OFF = ST_OFF + 1024, SYNC_OFF = ST_OFF + 2048;
constexpr int KP = 8192, VP = 8192 + 64;
constexpr int K_OFF = 0, V_OFF = K_OFF + 4 * KP;
__device__ __forceinline__ constexpr int slab_of(int w) { return 65536 + w * 20480; }
constexpr int REDG_OFF = 0, GX_OFF = 61440, REDS1_OFF = GX_OFF + 8192, REDS2_OFF = REDS1_OFF + 49152;

__device__ __forceinline__ void st16(u32x4 v, __amdgpu_buffer_rsrc_t rs, unsigned voff, unsigned soff) {
  __builtin_amdgcn_raw_buffer_store_b128(v, rs, voff + soff, 0, SC1);
}
__device__ __forceinline__ bf16x8 ld16(__amdgpu_buffer_rsrc_t rs, unsigned voff, unsigned soff) {
  return __builtin_bit_cast(bf16x8, __builtin_amdgcn_raw_buffer_load_b128(rs, voff, soff, SC1));
}
__device__ __forceinline__ int wg_of(int rt, int cg) { return (((cg & 3) * 8 + (rt & 7)) << 3) | ((cg >> 2) << 1) | (rt >> 3); }
template <int N>
__device__ __forceinline__ bool sum_pairs(const u32x4 (&v)[N], unsigned last_on, unsigned tg, float& a, float& b) {
  bool ok = true;
  a = 0.f; b = 0.f;
#pragma unroll
  for (int k = 0; k < N; ++k) {
    ok = ok && v[k][1] == tg && v[k][3] == tg;
    const unsigned m = k == N - 1 ? last_on : 0xffffffffu;
    a += __uint_as_float(v[k][0] & m);
    b += __uint_as_float(v[k][2] & m);
  }
  return __all(ok);
}
template <int N>
__device__ __forceinline__ void regather(__amdgpu_buffer_rsrc_t rs, const unsigned (&vo)[N], unsigned last_on, unsigned tg, float& a, float& b) {
#pragma unroll 1
  for (int spins = 0; spins < (1 << 20); ++spins) {
    u32x4 v[N];
#pragma unroll
    for (int k = 0; k < N; ++k) v[k] = __builtin_amdgcn_raw_buffer_load_b128(rs, vo[k], 0, SC1);
    if (sum_pairs<N>(v, last_on, tg, a, b)) break;
    __builtin_amdgcn_s_sleep(2);
  }
}

#define LDX(set, i, ks) xf[set][i] = *(LAS bf16x8*)(size_t)(xa[ks] + (i) * 2048)
#define LDW(set, j, ks) wf[set][j] = cat44(trread(wa[j] + (ks) * 4096), trread(wa[j] + (ks) * 4096 + 512))
#define HALF(set, ks) do { \
    _Pragma("unroll") for (int j = 0; j < NT; ++j) { \
      if (j == 0) { _Pragma("unroll") for (int i = 0; i < 4; ++i) LDX((set) ^ 1, i, ks); } \
      else if (2 * j - 2 < NT) { LDW((set) ^ 1, 2 * j - 2, ks); if (2 * j - 1 < NT) LDW((set) ^ 1, 2 * j - 1, ks); } \
      _Pragma("unroll") for (int i = 0; i < 4; ++i) acc[i][j] = mfma16(wf[set][j], xf[set][i], acc[i][j]); \
      SGB(0x8, 1); SGB(0x100, 1); SGB(0x8, 1); SGB(0x100, 1); SGB(0x8, 1); SGB(0x100, 1); SGB(0x8, 1); SGB(0x100, 1); \
      asm volatile("" :: "v"(acc[0][j]), "v"(acc[1][j]), "v"(acc[2][j]), "v"(acc[3][j])); \
      SB(); \
    } } while (0)
template <int NT, int S0>
__device__ __forceinline__ void mfma_part(unsigned (&xa)[2], unsigned wl, int tl0, int sigl, unsigned lds0, float (&sh)[4], float (&sc)[4],
                                          int row0, float qs, __amdgpu_buffer_rsrc_t rs, unsigned vo, unsigned so, int cs0, int g) {
  constexpr bool ISH = S0 == 10;
  unsigned wa[NT];
  f32x4 acc[4][NT];
#pragma unroll
  for (int j = 0; j < NT; ++j) {
    const int tl = tl0 + j;
    wa[j] = wl + (S0 & 3) * STG + (tl >> 2) * 8192 + (((tl & 3) ^ sigl) << 5);
#pragma unroll
    for (int i = 0; i < 4; ++i) acc[i][j] = f32x4{0.f, 0.f, 0.f, 0.f};
  }
  bf16x8 xf[2][4], wf[2][NT];
#pragma unroll
  for (int i = 0; i < 4; ++i) LDX(0, i, 0);
#pragma unroll
  for (int j = 0; j < NT; ++j) LDW(0, j, 0);
  WAIT_LGKM0();
  int buf = S0 & 3;
#pragma unroll 1
  for (int t = 0; t < 10; ++t) {
    HALF(0, 1);
    {
      const int d = buf == 3 ? -3 * STG : STG;
      buf = (buf + 1) & 3;
      xa[0] += d; xa[1] += d;
#pragma unroll
      for (int j = 0; j < NT; ++j) wa[j] += d;
    }
    WAIT_LGKM0();
    if (ISH) {
      asm volatile("s_cmp_lg_u32 %0, 1\n\ts_cbranch_scc1 .Lk%=\n\ts_waitcnt vmcnt(0)\n.Lk%=:\n\t"
                   "s_cmp_ge_u32 %0, 8\n\ts_cbranch_scc1 .Lc%=\n\ts_barrier\n.Lc%=:" :: "s"(t) : "memory", "scc");
    } else BAR();
    SB();
    HALF(1, 0);
    WAIT_LGKM0();
  }
  lds_wait((LAS unsigned*)(size_t)(lds0 + SYNC_OFF + (ISH ? 32 : 28)), 4);
  if (!ISH) {
#pragma unroll
    for (int i = 0; i < 4; ++i) {
      const f32x2 st = *(LAS f32x2*)(size_t)(lds0 + ST_OFF + (row0 + 16 * i) * 8);
      sc[i] = st[1]; sh[i] = -st[0] * st[1];
    }
  }
#pragma unroll
  for (int j = 0; j < NT; ++j) {
    const f32x4 c4 = *(LAS f32x4*)(size_t)(lds0 + CS_OFF + (ISH ? 512 : 0) + ((cs0 + j) * 16 + 4 * g) * 4);
    f32x4 bv = {0.f, 0.f, 0.f, 0.f};
    if (ISH) {
      const u32x2 bb = *(LAS u32x2*)(size_t)(lds0 + B1_OFF + ((cs0 + j) * 16 + 4 * g) * 2);
      bv = f32x4{bflo(bb[0]), bfhi(bb[0]), bflo(bb[1]), bfhi(bb[1])};
    }
#pragma unroll
    for (int pp = 0; pp < 2; ++pp) {
      float v[2][4];
#pragma unroll
      for (int i = 0; i < 2; ++i)
#pragma unroll
        for (int r = 0; r < 4; ++r) {
          float t = __builtin_fmaf(sc[2 * pp + i], acc[2 * pp + i][j][r], __builtin_fmaf(sh[2 * pp + i], c4[r], bv[r]));
          if (ISH) { t = t > 0.f ? t : 0.f; t = t * t; } else t *= qs;
          v[i][r] = t;
        }
      const unsigned x0 = pk2(v[0][0], v[0][1]), x1 = pk2(v[0][2], v[0][3]);
      const unsigned y0 = pk2(v[1][0], v[1][1]), y1 = pk2(v[1][2], v[1][3]);
      const auto s0 = __builtin_amdgcn_permlane16_swap(x0, y0, false, false);
      const auto s1 = __builtin_amdgcn_permlane16_swap(x1, y1, false, false);
      const u32x4 o = {s0[0], s1[0], s0[1], s1[1]};
      st16(o, rs, vo + pp * 512, so + 2 * j * 1024);
    }
  }
}

__global__ __launch_bounds__(512, 2) void k_all(const char* __restrict__ x, const char* __restrict__ wqkv, const char* __restrict__ w1,
                                                const char* __restrict__ b1, const char* __restrict__ w2, const char* __restrict__ wo,
                                                const char* __restrict__ b2, char* ws, char* __restrict__ out) {
  extern __shared__ __attribute__((aligned(16))) char smem_[];
  LAS char* smem = (LAS char*)smem_;
  const int tid = threadIdx.x, lane = tid & 63, w = __builtin_amdgcn_readfirstlane(tid >> 6);
  const int b = blockIdx.x, xcd = b & 7, idx = b >> 3;
  const unsigned lds0 = (unsigned)(size_t)smem;
  asm volatile("" :: "s"(x), "s"(wqkv), "s"(w1), "s"(b1), "s"(w2), "s"(wo), "s"(ws));
  u64 pc_;
  asm volatile("s_getpc_b64 %0" : "=s"(pc_));
  const int rt = (xcd & 1) * 8 + (idx & 7), cg = (xcd >> 1) * 4 + (idx >> 3);
  const int rot = (rt + 3 * cg) % 10;
  const int lw = w - 4, lr = lane >> 3, ls = lane & 7;
  const int bk = 16 * lw + lr, sw0 = (ls ^ (bk & 2)) << 4, sw1 = sw0 ^ 64;
  const unsigned vq0 = bk * 3840 + (cg < 15 ? cg : 14) * 256 + sw0, vq1 = (bk + 8) * 3840 + (cg < 15 ? cg : 14) * 256 + sw1;
  const unsigned vA = (rt * 128 + 32 * lw + lr) * 1280 + ((ls ^ (lr >> 1)) << 4);
  const unsigned vA1 = (vA + 10240) ^ 64u, vA2 = vA + 20480, vA3 = (vA + 30720) ^ 64u;
  const int rkb = b / 5, rnb = b - 5 * rkb;
  if (w >= 4) {
    dma_stage(x + rot * 128, vA, vA1, vA2, vA3, lds0 + lw * 4096, wqkv + (size_t)rot * (64 * 3840), vq0, vq1, lds0 + 16384 + lw * 2048);
    if (b < 170) {
      const char* Wr = rkb < 24 ? w2 + (size_t)rkb * 64 * 1280 : wo + (size_t)(rkb - 24) * 64 * 1280;
      const unsigned r0 = bk * 1280 + rnb * 256 + sw0, r1 = (bk + 8) * 1280 + rnb * 256 + sw1;
      dma1(Wr, r0, lds0 + RT_OFF + lw * 2048);
      dma1(Wr, r1, lds0 + RT_OFF + lw * 2048 + 1024);
      dma1(Wr + 128, r0, lds0 + RT_OFF + 8192 + lw * 2048);
      dma1(Wr + 128, r1, lds0 + RT_OFF + 8192 + lw * 2048 + 1024);
    }
    const int kt = rot == 9 ? 0 : rot + 1;
    dma_stage(x + kt * 128, vA, vA1, vA2, vA3, lds0 + STG + lw * 4096, wqkv + (size_t)kt * (64 * 3840), vq0, vq1,
              lds0 + STG + 16384 + lw * 2048);
  }
  SB();
  const int g = lane >> 4, l15 = lane & 15, q4 = l15 >> 2, p4 = lane & 3;
  const char* pc0 = (const char*)(pc_ & ~(u64)63);
  const __amdgpu_buffer_rsrc_t rs = __builtin_amdgcn_make_buffer_rsrc(ws, 0, WS_USED, 0x00020000);
  LAS unsigned* sync = (LAS unsigned*)(smem + SYNC_OFF);
  u64 tag;
  {
    const char* p = ws;
    asm volatile("" : "+s"(p));
    tag = *(const CAS u64*)(p + OFF_FL) + 1;
  }
  const int lu1 = (6 * cg) & 3;
  const int sigl = ((q4 >> 1) & 1) | ((g & 1) << 1), xsw = g ^ (l15 >> 1);
  const int mt = (xcd >> 1) * 8 + (idx & 7), ct = (xcd & 1) * 4 + (idx >> 3);
  const int head = 4 * xcd + (idx & 3), qb = idx >> 2;
  const int ch = (5 * head) >> 1, odd = head & 1;
  auto orow_f = [&]() {
    unsigned ln = lane;
    asm volatile("" : "+v"(ln));
    return (64 * mt + 16 * (w & 3) + (ln & 15)) * 1280 + (80 * ct + 4 * (ln >> 4)) * 2;
  };
  u32x2 xv[3];
  f32x4 acc[4][5];
  const unsigned va = g * 1024 + l15 * 16;
  bf16x8 wpre[5][5];
  const unsigned wb = w < 4 ? OFF_W2F + (ct * 48 + 12 * w) * 5120 : OFF_WOF + (ct * 20 + 5 * (w - 4)) * 5120;

  if (w >= 4) {
    const int lt = tid - 256;
    const unsigned tg = (unsigned)tag;
    const unsigned vh0 = bk * 3072 + ((6 * cg) >> 2) * 128 + sw0, vh1 = (bk + 8) * 3072 + ((6 * cg) >> 2) * 128 + sw1;
    const int rot2 = rot + 6 >= 10 ? rot - 4 : rot + 6;
    int ktn = rot + 2 >= 10 ? rot - 8 : rot + 2;
    auto issue = [&](auto S) {
      constexpr int s = decltype(S)::value;
      const unsigned dst = lds0 + (s & 3) * STG;
      if (s == 10) ktn = rot2;
      const char* sw = s >= 10 ? w1 + (size_t)ktn * (64 * 3072) : wqkv + (size_t)ktn * (64 * 3840);
      if constexpr (s >= 10 && s < 14) dma_stage_w(sw, vh0, vh1, dst + 16384 + lw * 2048);
      else dma_stage(x + ktn * 128, vA, vA1, vA2, vA3, dst + lw * 4096, sw, s >= 10 ? vh0 : vq0, s >= 10 ? vh1 : vq1, dst + 16384 + lw * 2048);
      ktn = ktn == 9 ? 0 : ktn + 1;
    };
    if (lt < 16) sync[lt] = 0u;
    WAIT_VM(8);
    BAR();
    issue(IC<2>{}); issue(IC<3>{});
    const bf16x8 ONE8 = {0x3f80, 0x3f80, 0x3f80, 0x3f80, 0x3f80, 0x3f80, 0x3f80, 0x3f80};
    const f32x4 z4 = {0.f, 0.f, 0.f, 0.f};
    bf16x8 sw_[2][2], sx_[2][2];
    u32x4 gr[5], gc[3];
    u32x2 fl;
    const int fkb = lane < 12 ? 6 * lw + (lane >> 1) : 24 + ((5 * lw) >> 1) + (lane < 18 ? (lane - 12) >> 1 : 0);
    const unsigned fW = FL_W + (fkb * 5 + ((5 * ct + (lane & 1) * 4) >> 3)) * 128;
    const int ty = lane < 24 ? lane >> 3 : 0, rr0 = 2 * qb - 2 + ((lane >> 1) & 3);
    const int rr = rr0 < 0 ? 0 : (ty == 0 && rr0 < 2 * qb ? 2 * qb : rr0);
    const unsigned fQ = FL_Q + wg_of(rr, 5 * ty + ((ch + (lane & 1) * 2) >> 4)) * 128;
    unsigned ro[5], co[3];
#pragma unroll
    for (int k = 0; k < 5; ++k) ro[k] = OFF_RS + (rt * 10 + 2 * k + (lane >> 5)) * 2048 + (32 * lw + (lane & 31)) * 16;
#pragma unroll
    for (int k = 0; k < 3; ++k) co[k] = OFF_CS + (cg * 10 + (k == 2 && g >= 2 ? g : g + 4 * k)) * 1024 + (16 * lw + l15) * 16;
    const unsigned c_on = g < 2 ? 0xffffffffu : 0u;
    auto col_sums = [&](int part) {
#pragma unroll
      for (int k = 0; k < 3; ++k) PIN(gc[k]);
      float c0, c1;
      if (!sum_pairs<3>(gc, c_on, tg, c0, c1)) regather<3>(rs, co, c_on, tg, c0, c1);
      c0 += __shfl_xor(c0, 16); c1 += __shfl_xor(c1, 16);
      c0 += __shfl_xor(c0, 32); c1 += __shfl_xor(c1, 32);
      if (lane < 16) *(LAS f32x2*)(smem + CS_OFF + part * 512 + (16 * lw + lane) * 8) = f32x2{c0, c1};
      WAIT_LGKM0();
      lds_arrive(sync + 7 + part);
    };
    auto stage = [&](auto S) {
      constexpr int s = decltype(S)::value;
      if constexpr (s == 0 || s == 10) {
        const unsigned sb = lds0 + (s & 3) * STG;
#pragma unroll
        for (int i = 0; i < 2; ++i) {
          const int lux = ((s == 0 ? 0 : lu1) + 2 * lw + i) & 7;
          const unsigned wq = sb + 16384 + (lux >> 2) * 8192 + (8 * g + q4) * 128 + (((lux & 3) ^ sigl) << 5) + p4 * 8;
          sw_[i][0] = cat44(trread(wq), trread(wq + 512));
          sw_[i][1] = cat44(trread(wq + 4096), trread(wq + 4608));
          if (s == 0) {
            const unsigned xq = sb + (32 * lw + 16 * i + l15) * 128;
            sx_[i][0] = *(LAS bf16x8*)(size_t)(xq + (xsw << 4));
            sx_[i][1] = *(LAS bf16x8*)(size_t)(xq + ((xsw ^ 4) << 4));
          }
        }
      }
      constexpr int younger[18] = {16, 16, 16, 16, 16, 16, 24, 20, 8, 8, 8 + 5 * WOPRE, 12 + 5 * WOPRE, 16 + 5 * WOPRE, 16, 16, 19, 19, 0};
      wait_vm<younger[s]>();
      WAIT_LGKM0();
      BAR();
      if constexpr (s < 16) issue(IC<s + 4>{});
      if constexpr (s == 2 || s == 3) {
        if (b < 170) {
          const unsigned dstb = rkb < 24 ? OFF_W2F : OFF_WOF;
          const int nks = rkb < 24 ? 48 : 20, kb = rkb < 24 ? rkb : rkb - 24;
          const int nl = 2 * lw + s - 2, nt = 8 * rnb + nl, c5 = nt / 5, jj = nt - 5 * c5;
          const unsigned rp = lds0 + RT_OFF + (nl >> 2) * 8192 + (8 * g + q4) * 128 + (((nl & 3) ^ sigl) << 5) + p4 * 8;
#pragma unroll
          for (int ks = 0; ks < 2; ++ks) {
            const bf16x8 f = cat44(trread(rp + ks * 4096), trread(rp + ks * 4096 + 512));
            st16(__builtin_bit_cast(u32x4, f), rs, lane * 16, dstb + ((c5 * nks + 2 * kb + ks) * 5 + jj) * 1024);
          }
        }
      }
      if constexpr (s == 0 || s == 10) {
        __builtin_amdgcn_s_setprio(3);
        const u32x4 c0 = __builtin_bit_cast(u32x4, mfma16(sw_[0][1], ONE8, mfma16(sw_[0][0], ONE8, z4)));
        const u32x4 c1 = __builtin_bit_cast(u32x4, mfma16(sw_[1][1], ONE8, mfma16(sw_[1][0], ONE8, z4)));
        const u32x4 cu = l15 == 0 ? c0 : c1;
        if (l15 < 2) {
          const unsigned o = OFF_CS + (((s == 10 ? 16 : 0) + cg) * 10 + (s == 10 ? rot2 : rot)) * 1024 + 2 * lw * 128;
          st16(u32x4{cu[0], tg, cu[1], tg}, rs, l15 * 128 + g * 32, o);
          st16(u32x4{cu[2], tg, cu[3], tg}, rs, l15 * 128 + g * 32 + 16, o);
        }
        __builtin_amdgcn_s_setprio(0);
      }
      if constexpr (s == 1) {
        __builtin_amdgcn_s_setprio(3);
#pragma unroll
        for (int i = 0; i < 2; ++i) {
          const f32x4 sx = mfma16(ONE8, sx_[i][1], mfma16(ONE8, sx_[i][0], z4));
          const f32x4 sq = mfma16(sx_[i][1], sx_[i][1], mfma16(sx_[i][0], sx_[i][0], z4));
          const int r = l15 & 3;
          const float sxx = r == 0 ? sq[0] : (r == 1 ? sq[1] : (r == 2 ? sq[2] : sq[3]));
          if (q4 == g)
            st16(u32x4{__float_as_uint(sx[0]), tg, __float_as_uint(sxx), tg}, rs, (32 * lw + 16 * i + l15) * 16,
                 OFF_RS + (rt * 10 + rot) * 2048);
        }
        __builtin_amdgcn_s_setprio(0);
      }
      if constexpr (s == 4) {
        if (lt < 12) dma1(b1 + cg * 192, lane * 16, lds0 + B1_OFF);
        if (lw == 1 && lane < 10) dma1(b2 + ct * 160, lane * 16, lds0 + B1_OFF + 256);
      }
      if constexpr (s == 6 || s == 11) {
        if (lt == 0 && (s == 6 || cg < 15)) __hip_atomic_store((GAS u64*)(ws + (s == 6 ? FL_W : FL_Q) + b * 128), tag, RLX_AGENT);
      }
      if constexpr (s == 5) {
#pragma unroll
        for (int k = 0; k < 5; ++k) gr[k] = aload16(ws, ro[k]);
      }
      if constexpr (s == 5 || s == 14) {
#pragma unroll
        for (int k = 0; k < 3; ++k) { if (s == 14) co[k] += 16 * 10240; gc[k] = aload16(ws, co[k]); }
      }
      if constexpr (s == 8) {
        __builtin_amdgcn_s_setprio(3);
#pragma unroll
        for (int k = 0; k < 5; ++k) PIN(gr[k]);
        float sx, sxx;
        if (!sum_pairs<5>(gr, 0xffffffffu, tg, sx, sxx)) regather<5>(rs, ro, 0xffffffffu, tg, sx, sxx);
        sx += __shfl_xor(sx, 32); sxx += __shfl_xor(sxx, 32);
        const float mean = sx * (1.f / 640.f);
        float var = sxx * (1.f / 640.f) - mean * mean;
        var = var < 0.f ? 0.f : var;
        if (lane < 32) *(LAS f32x2*)(smem + ST_OFF + (32 * lw + lane) * 8) = f32x2{mean, rsqrtf(var + 1e-5f)};
        col_sums(0);
        __builtin_amdgcn_s_setprio(0);
      }
      if constexpr (s == 9) {
        poll_flags(ws + fW, true, tag);
#pragma unroll
        for (int k = 0; k < WOPRE; ++k)
#pragma unroll
          for (int j = 0; j < 5; ++j) wpre[k][j] = ld16(rs, lane * 16, wb + k * 5120 + j * 1024);
      }
      if constexpr (s == 16) fl = aload8(ws, fQ);
    };
    stage(IC<0>{}); stage(IC<1>{}); stage(IC<2>{}); stage(IC<3>{}); stage(IC<4>{}); stage(IC<5>{});
    stage(IC<6>{}); stage(IC<7>{}); stage(IC<8>{}); stage(IC<9>{}); stage(IC<10>{}); stage(IC<11>{});
    stage(IC<12>{}); stage(IC<13>{}); stage(IC<14>{}); stage(IC<15>{}); stage(IC<16>{}); stage(IC<17>{});
    PIN(fl);
    if (!__all(fl[0] == tg && fl[1] == (unsigned)(tag >> 32))) poll_flags(ws + fQ, true, tag);
#pragma unroll
    for (int i = 0; i < 2; ++i) {
      const int seg = 2 * lw + i;
      int tb = 4 * qb - 4 + seg; tb = tb < 0 ? 0 : tb;
      dma_kv(ws + OFF_QKV + (size_t)(tb * 240 + 80 + ch) * 1024, lane * 16, lane * 16 + 81920, lds0 + K_OFF + seg * 1024);
    }
    const int l31 = lane & 31, hh = lane >> 5;
    u32x4 qraw[2][2];
#pragma unroll
    for (int a = 0; a < 2; ++a)
#pragma unroll
      for (int ks = 0; ks < 2; ++ks)
        qraw[a][ks] = __builtin_amdgcn_raw_buffer_load_b128(rs, (ks ? 2 : hh) * 1024 + (32 * a + l31) * 16,
                                                            OFF_QKV + ((4 * qb + lw) * 240 + ch) * 1024, SC1);
    col_sums(1);
    {
      const u32x4 one4 = {0x3f803f80u, 0x3f803f80u, 0x3f803f80u, 0x3f803f80u};
      *(LAS u32x4*)(smem + V_OFF + 3 * VP + lt * 16) = one4;
      if (lt < 40) *(LAS u32x4*)(smem + V_OFF + 3 * VP + 4096 + lt * 16) = one4;
      *(LAS u32x4*)(smem + K_OFF + 3 * KP + lt * 16) = u32x4{qb == 0 ? 0xff80u : 0u, 0u, 0u, 0u};
      *(LAS u32x4*)(smem + K_OFF + 3 * KP + 4096 + lt * 16) = u32x4{0u, 0u, 0u, 0u};
    }
    bf16x8 qf[2][2];
#pragma unroll
    for (int a = 0; a < 2; ++a) {
      u32x4 v0 = qraw[a][0], v1 = qraw[a][1];
      if (odd && hh == 0) { v0[0] = 0u; v0[1] = 0u; }
      if (hh) v1 = u32x4{0x3f80u, 0u, 0u, 0u};
      else if (!odd) { v1[2] = 0u; v1[3] = 0u; }
      qf[a][0] = __builtin_bit_cast(bf16x8, v0);
      qf[a][1] = __builtin_bit_cast(bf16x8, v1);
    }
    f32x16 mF, z16;
#pragma unroll
    for (int e = 0; e < 16; ++e) {
      mF[e] = (e & 3) + 8 * (e >> 2) + 4 * hh > l31 ? 0.f : -INFINITY;
      z16[e] = 0.f;
    }
    asm volatile("" : "+v"(mF), "+v"(qf[0][0]), "+v"(qf[0][1]), "+v"(qf[1][0]), "+v"(qf[1][1]));
    WAIT_VM(0);
    WAIT_LGKM0();
    lds_arrive(sync);
    lds_wait(sync, 4);
    __builtin_amdgcn_s_setprio(2);
    unsigned kp = lds0 + K_OFF + hh * KP + (64 * lw + l31) * 16;
    const bool ones = (g & 1) && (p4 >> 1);
    unsigned vp = lds0 + V_OFF + (2 * (g & 1) + (p4 >> 1)) * VP + ((ones ? 0 : 64 * lw) + 4 * hh + q4) * 16 + (p4 & 1) * 8;
    unsigned ov = l31 * 16 + hh * 8;
    bf16x8 q0 = qf[0][0], q1 = qf[0][1];
#define LDK(T, ks) (*(LAS bf16x8*)(size_t)(kp + (T) * 512 + (ks) * 2 * KP))
#define MF32(a, b, c) __builtin_amdgcn_mfma_f32_32x32x16_bf16(a, b, c, 0, 0, 0)
#define EX2(s_, e) pk2(__builtin_amdgcn_exp2f((s_)[e]), __builtin_amdgcn_exp2f((s_)[(e) + 1]))
    auto tile_max = [&](int T) {
      f32x16 sp = MF32(LDK(T, 1), q1, MF32(LDK(T, 0), q0, z16));
      float m0 = -INFINITY, m1 = -INFINITY;
#pragma unroll
      for (int e = 0; e < 16; ++e) {
        const bool off = T == 0 ? mF[e] != 0.f : (T == 8 ? mF[e] == 0.f : false);
        sp[e] = off ? -INFINITY : sp[e];
      }
#pragma unroll
      for (int e = 0; e < 16; e += 4) { m0 = MAX3(m0, sp[e], sp[e + 1]); m1 = MAX3(m1, sp[e + 2], sp[e + 3]); }
      m0 = fmaxf(m0, m1);
      return fmaxf(m0, __shfl_xor(m0, 32));
    };
    float mx = tile_max(8);
    kp += 512; q0 = qf[1][0]; q1 = qf[1][1];
    const float mx1 = tile_max(8);
    kp -= 512; q0 = qf[0][0]; q1 = qf[0][1];
#pragma unroll 1
    for (int un = 0; un < 2; ++un) {
      f32x16 oa;
#pragma unroll 1
      for (int pass = 0; pass < 2; ++pass) {
      f32x16 ca;
#pragma unroll
      for (int e = 0; e < 16; ++e) ca[e] = -mx;
      oa = z16;
      {
        bf16x8 kc[2][2], vf[2][2];
        f32x16 sc[2];
        u32x4 pa[2] = {{0u, 0u, 0u, 0u}, {0u, 0u, 0u, 0u}};
        kc[0][0] = LDK(0, 0); kc[0][1] = LDK(0, 1); kc[1][0] = LDK(1, 0); kc[1][1] = LDK(1, 1);
        sc[0] = MF32(kc[0][1], q1, MF32(kc[0][0], q0, ca + mF));
        sc[1] = z16;
        asm volatile("" :: "v"(sc[0]));
        SB();
#pragma unroll
        for (int T = 0; T < 10; ++T) {
          const int c = T & 1, n = c ^ 1;
          if (T < 9) {
#pragma unroll
            for (int ks = 0; ks < 2; ++ks) vf[c][ks] = cat44(trread(vp + T * 512 + ks * 256), trread(vp + T * 512 + ks * 256 + 128));
          }
          if (T < 8) sc[n] = MF32(kc[n][1], q1, MF32(kc[n][0], q0, ca));
          if (T < 7) { kc[c][0] = LDK(T + 2, 0); kc[c][1] = LDK(T + 2, 1); }
          if (T > 0) oa = MF32(vf[n][1], __builtin_bit_cast(bf16x8, pa[1]), MF32(vf[n][0], __builtin_bit_cast(bf16x8, pa[0]), oa));
          if (T == 8) {
#pragma unroll
            for (int e = 0; e < 16; ++e) sc[c][e] = mF[e] == 0.f ? -INFINITY : sc[c][e];
          }
          if (T < 9) {
#pragma unroll
            for (int e = 0; e < 4; ++e) { pa[0][e] = EX2(sc[c], 2 * e); pa[1][e] = EX2(sc[c], 8 + 2 * e); }
          }
          SGB(0x100, 6);
          if (T == 8) SGB(0x2, 16);
          SGB(0x8, 1); SGB(0x400, 4); SGB(0x2, 1);
#pragma unroll
          for (int k = 0; k < 3; ++k) { SGB(0x8, 1); SGB(0x400, 2); SGB(0x2, 1); SGB(0x400, 2); SGB(0x2, 1); }
          SGB(0x2, 1);
          asm volatile("" :: "v"(sc[n]), "v"(oa), "v"(pa[0]), "v"(pa[1]));
          SB();
        }
      }
      if (pass || __all(oa[12] < 1.8446744e19f)) break;
#pragma unroll 1
      for (int T = 0; T < 8; ++T) mx = fmaxf(mx, tile_max(T));
      }
      const float il = __builtin_amdgcn_rcpf(oa[12]);
      {
        unsigned pk[3][2];
#pragma unroll
        for (int pl = 0; pl < 3; ++pl) {
          pk[pl][0] = pk2(oa[4 * pl] * il, oa[4 * pl + 1] * il);
          pk[pl][1] = pk2(oa[4 * pl + 2] * il, oa[4 * pl + 3] * il);
        }
        const auto s0 = __builtin_amdgcn_permlane32_swap(odd ? pk[1][0] : pk[0][0], odd ? pk[2][0] : pk[1][0], false, false);
        const auto s1 = __builtin_amdgcn_permlane32_swap(odd ? pk[1][1] : pk[0][1], odd ? pk[2][1] : pk[1][1], false, false);
        const unsigned ob = OFF_O + ((4 * qb + lw) * 80 + ch) * 1024;
        st16(u32x4{s0[0], s1[0], s0[1], s1[1]}, rs, (odd + hh) * 1024 + (ov & ~8u), ob);
        if (hh == odd)
          __builtin_amdgcn_raw_buffer_store_b64(odd ? u32x2{pk[0][0], pk[0][1]} : u32x2{pk[2][0], pk[2][1]}, rs,
                                                (odd ? 0 : 2048) + ov, ob, SC1);
      }
      kp += 512; vp += ones ? 0 : 512; ov += 512;
      q0 = qf[1][0]; q1 = qf[1][1]; mx = mx1;
    }
    __builtin_amdgcn_s_setprio(0);
    lds_arrive(sync + 4);
    WAIT_VM(0);
    if (lane == 0) __hip_atomic_store((GAS u64*)(ws + FL_O + (b * 4 + lw) * 128), tag, RLX_AGENT);
#pragma unroll
    for (int k = WOPRE; k < 5; ++k)
#pragma unroll
      for (int j = 0; j < 5; ++j) wpre[k][j] = ld16(rs, lane * 16, wb + k * 5120 + j * 1024);
    const unsigned orow = orow_f();
#pragma unroll
    for (int j = 0; j < 2; ++j) xv[j] = *(const u32x2*)(x + orow + (3 + j) * 32);
    const unsigned ab = OFF_O + (mt * 80 + 20 * lw) * 1024;
    {
      const int hd = 8 * lw + (lane & 7);
      poll_flags(ws + FL_O + ((((((mt >> 2) << 2) | (hd & 3)) << 3) | (hd >> 2)) * 4 + (mt & 3)) * 128, lane < 8, tag);
    }
    bf16x8 af[4][5];
#pragma unroll
    for (int i = 0; i < 4; ++i)
#pragma unroll
      for (int k = 0; k < 5; ++k) af[i][k] = ld16(rs, va + ((lw + 1 + i) & 3) * 256, ab + k * 4096);
    SB();
    lds_wait(sync + 4, 4);
    lds_wait(sync + 6, 4);
    f32x4 own[2];
#pragma unroll
    for (int i = 0; i < 4; ++i) {
      const int mi = (lw + 1 + i) & 3;
      f32x4 a5[5] = {z4, z4, z4, z4, z4};
#pragma unroll
      for (int k = 0; k < 5; ++k)
#pragma unroll
        for (int j = 0; j < 5; ++j) a5[j] = mfma16(wpre[k][j], af[i][k], a5[j]);
#pragma unroll
      for (int j = 0; j < 3; ++j) *(LAS f32x4*)(smem + REDS1_OFF + ((mi * 4 + lw) * 3 + j) * 1024 + lane * 16) = a5[j];
      if (i < 3) {
#pragma unroll
        for (int j = 0; j < 2; ++j) *(LAS f32x4*)(smem + REDS2_OFF + ((mi * 3 + 2 - i) * 2 + j) * 1024 + lane * 16) = a5[3 + j];
      } else { own[0] = a5[3]; own[1] = a5[4]; }
    }
    __syncthreads();
#pragma unroll
    for (int j = 0; j < 2; ++j) {
      f32x4 sv = own[j];
#pragma unroll
      for (int k = 0; k < 3; ++k) sv += *(LAS f32x4*)(smem + REDS2_OFF + ((lw * 3 + k) * 2 + j) * 1024 + lane * 16);
      const f32x4 gt = *(LAS f32x4*)(smem + GX_OFF + (lw * 2 + j) * 1024 + lane * 16);
      *(u32x2*)(out + orow + (3 + j) * 32) = fin4(xv[j], sv, gt);
    }

  } else {
    if (idx * 4 + w < CODE_KB) {
      const u32x4 t0 = *(const u32x4*)(pc0 + (idx * 4 + w) * 1024 + lane * 16);
      asm volatile("" :: "v"(t0));
    }
    {
      const int mw = w & 1, nw = w >> 1;
      unsigned xa[2];
      xa[0] = lds0 + (64 * mw + l15) * 128 + (xsw << 4);
      xa[1] = lds0 + (64 * mw + l15) * 128 + ((xsw ^ 4) << 4);
      const unsigned wl = lds0 + 16384 + (8 * g + q4) * 128 + p4 * 8;
      const unsigned vo = (g >> 1) * 1024 + (16 * (g & 1) + l15) * 16;
      float sh[4], sc[4];
      BAR();
      mfma_part<4, 0>(xa, wl, 4 * nw, sigl, lds0, sh, sc, 64 * mw + l15, cg < 5 ? 0.22360679774997896f * 1.4426950408889634f : 1.f, rs, vo,
                      cg < 15 ? OFF_QKV + ((2 * rt + mw) * 240 + 16 * cg + 8 * nw) * 1024 : 0x80000000u, 4 * nw, g);
      mfma_part<3, 10>(xa, wl, lu1 + 3 * nw, sigl, lds0, sh, sc, 0, 1.f, rs, vo,
                       OFF_H + ((2 * rt + mw) * 192 + 12 * cg + 6 * nw) * 1024, 3 * nw, g);
    }
    WAIT_VM(0);
    if (lds_arrive(sync + 2) == 3 && lane == 0) __hip_atomic_store((GAS u64*)(ws + FL_H + b * 128), tag, RLX_AGENT);
#pragma unroll
    for (int k = 0; k < 4; ++k)
#pragma unroll
      for (int j = 0; j < 5; ++j) wpre[k][j] = ld16(rs, lane * 16, wb + k * 5120 + j * 1024);
    const unsigned wl_ = lds0 + slab_of(w);
    lds_wait(sync + 2, 4);
    dma5_sc1(ws + wb + 4 * 5120, lane * 16, wl_);
    dma5_sc1(ws + wb + 5 * 5120, lane * 16, wl_ + 5120);
    const unsigned ab = OFF_H + (mt * 192 + 48 * w) * 1024;
    if (b == 0 && w == 0) {
#pragma unroll 1
      for (int spins = 0; spins < (1 << 20); ++spins) {
        u64 v[4];
#pragma unroll
        for (int i = 0; i < 4; ++i) v[i] = __hip_atomic_load((const GAS u64*)(ws + FL_W + (lane + 64 * i) * 128), RLX_AGENT);
        if (__all(((v[0] ^ tag) | (v[1] ^ tag) | (v[2] ^ tag) | (v[3] ^ tag)) == 0)) break;
        __builtin_amdgcn_s_sleep(1);
      }
      if (lane == 0) __hip_atomic_store((GAS u64*)(ws + OFF_FL), tag, RLX_AGENT);
    }
    poll_flags(ws + FL_H + wg_of(mt >> 1, 4 * w + (lane & 3)) * 128, lane < 4, tag);
    unsigned vi[4];
#pragma unroll
    for (int i = 0; i < 4; ++i) vi[i] = va + ((w + 1 + i) & 3) * 256;
    dma5_sc1(ws + ab + 4 * 4096, lane * 16, wl_ + 10240);
    dma5_sc1(ws + ab + 4 * 4096 + 3072, lane * 16, wl_ + 10240 + 3072);
    bf16x8 af[4][4];
#pragma unroll
    for (int k = 0; k < 4; ++k)
#pragma unroll
      for (int i = 0; i < 4; ++i) af[k][i] = ld16(rs, vi[i], ab + k * 4096);
    SB();
#pragma unroll
    for (int i = 0; i < 4; ++i)
#pragma unroll
      for (int j = 0; j < 5; ++j) acc[i][j] = f32x4{0.f, 0.f, 0.f, 0.f};
#pragma unroll
    for (int k = 0; k < 12; ++k) {
      const int cs = k & 3;
      if (k == 6)
        asm volatile("s_mov_b64 exec, 1\n\tds_add_u32 %0, %1\n\ts_mov_b64 exec, -1" :: "v"(lds0 + SYNC_OFF + 24), "v"(1u) : "memory");
      if (k == W2HOLD) {
        unsigned tv, ts;
        asm volatile(".Lp%=:\n\tds_read_b32 %0, %2\n\ts_waitcnt lgkmcnt(0)\n\tv_readfirstlane_b32 %1, %0\n\t"
                     "s_cmp_ge_u32 %1, 4\n\ts_cbranch_scc1 .Le%=\n\ts_sleep 2\n\ts_branch .Lp%=\n.Le%=:"
                     : "=&v"(tv), "=&s"(ts) : "v"(lds0 + SYNC_OFF + 16) : "memory", "scc");
      }
#pragma unroll
      for (int i = 0; i < 4; ++i) {
#pragma unroll
        for (int j = 0; j < 5; ++j) acc[i][j] = mfma16(wpre[cs][j], af[cs][i], acc[i][j]);
        if (k == 11 && i < 3) {
#pragma unroll
          for (int j = 0; j < 5; ++j)
            *(LAS f32x4*)(smem + REDG_OFF + ((((w + 1 + i) & 3) * 3 + 2 - i) * 5 + j) * 1024 + lane * 16) = acc[i][j];
        }
      }
      SB();
      if (k < 2) {
#pragma unroll
        for (int j = 0; j < 5; ++j) wpre[cs][j] = *(LAS bf16x8*)(size_t)(wl_ + k * 5120 + j * 1024 + lane * 16);
#pragma unroll
        for (int i = 0; i < 4; ++i) af[cs][i] = *(LAS bf16x8*)(size_t)(wl_ + 10240 + k * 4096 + vi[i]);
      } else if (k < 8) {
#pragma unroll
        for (int i = 0; i < 4; ++i) af[cs][i] = ld16(rs, vi[i], ab + (k + 4) * 4096);
#pragma unroll
        for (int j = 0; j < 5; ++j) wpre[cs][j] = ld16(rs, lane * 16, wb + (k + 4) * 5120 + j * 1024);
      }
      if (k == 10) {
        const unsigned orow = orow_f();
#pragma unroll
        for (int j = 0; j < 3; ++j) xv[j] = *(const u32x2*)(x + orow + j * 32);
      }
      SB();
    }
    u32x2 bb[5];
#pragma unroll
    for (int j = 0; j < 5; ++j) bb[j] = *(LAS u32x2*)(smem + B1_OFF + 256 + (16 * j + 4 * g) * 2);
    WAIT_LGKM0();
    lds_arrive(sync + 3);
    lds_wait(sync + 3, 4);
    f32x4 gt[5];
#pragma unroll
    for (int j = 0; j < 5; ++j) {
      gt[j] = acc[3][j];
#pragma unroll
      for (int k = 0; k < 3; ++k) gt[j] += *(LAS f32x4*)(smem + REDG_OFF + ((w * 3 + k) * 5 + j) * 1024 + lane * 16);
    }
#pragma unroll
    for (int j = 0; j < 5; ++j) {
      const float bv[4] = {bflo(bb[j][0]), bfhi(bb[j][0]), bflo(bb[j][1]), bfhi(bb[j][1])};
#pragma unroll
      for (int e = 0; e < 4; ++e) {
        const float t = rbf(rbf(gt[j][e]) + bv[e]);
        gt[j][e] = rbf(t * __builtin_amdgcn_rcpf(1.f + __expf(-t)));
      }
      if (j >= 3) *(LAS f32x4*)(smem + GX_OFF + (w * 2 + j - 3) * 1024 + lane * 16) = gt[j];
    }
    __syncthreads();
    const unsigned orow = orow_f();
#pragma unroll
    for (int j = 0; j < 3; ++j) {
      f32x4 sv = *(LAS f32x4*)(smem + REDS1_OFF + ((w * 4) * 3 + j) * 1024 + lane * 16);
#pragma unroll
      for (int k = 1; k < 4; ++k) sv += *(LAS f32x4*)(smem + REDS1_OFF + ((w * 4 + k) * 3 + j) * 1024 + lane * 16);
      *(u32x2*)(out + orow + j * 32) = fin4(xv[j], sv, gt[j]);
    }
    __builtin_amdgcn_endpgm();
  }
}

extern "C" void kernel_launch(void* const* d_in, const int* in_sizes, int n_in, void* d_out, int out_size, void* d_ws,
                              size_t ws_size, hipStream_t stream) {
  (void)hipFuncSetAttribute((const void*)k_all, hipFuncAttributeMaxDynamicSharedMemorySize, LDS_ALL);
  k_all<<<256, 512, LDS_ALL, stream>>>((const char*)d_in[0], (const char*)d_in[1], (const char*)d_in[4], (const char*)d_in[5],
                                       (const char*)d_in[6], (const char*)d_in[2], (const char*)d_in[7], (char*)d_ws, (char*)d_out);
}
```

```cpp
#include <hip/hip_runtime.h>
#include <stdint.h>

#define LAS __attribute__((address_space(3)))
#define GAS __attribute__((address_space(1)))
#define CAS __attribute__((address_space(4)))
typedef short bf16x8 __attribute__((ext_vector_type(8)));
typedef short s16x4 __attribute__((ext_vector_type(4)));
typedef float f32x4 __attribute__((ext_vector_type(4)));
typedef unsigned u32x4 __attribute__((ext_vector_type(4)));
typedef unsigned u32x2 __attribute__((ext_vector_type(2)));
typedef __bf16 bf16x2_t __attribute__((ext_vector_type(2)));
typedef float f32x2 __attribute__((ext_vector_type(2)));
typedef float f32x16 __attribute__((ext_vector_type(16)));
typedef unsigned long long u64;

constexpr unsigned OFF_FL = 0;
constexpr unsigned FL_W = 1024, FL_Q = FL_W + 32768, FL_H = FL_Q + 32768, FL_O = FL_H + 32768;
constexpr unsigned OFF_RS = 262144;
constexpr unsigned OFF_CS = OFF_RS + 16 * 10 * 2048;
constexpr unsigned OFF_QKV = OFF_CS + 2 * 16 * 10 * 1024;
constexpr unsigned OFF_H = OFF_QKV + 32 * 240 * 1024;
constexpr unsigned OFF_O = OFF_H + 32 * 192 * 1024;
constexpr unsigned OFF_W2F = OFF_O + 32 * 80 * 1024;
constexpr unsigned OFF_WOF = OFF_W2F + 8 * 48 * 5120;
constexpr unsigned WS_USED = OFF_WOF + 8 * 20 * 5120;

__device__ __forceinline__ f32x4 mfma16(bf16x8 a, bf16x8 b, f32x4 c) {
  return __builtin_amdgcn_mfma_f32_16x16x32_bf16(a, b, c, 0, 0, 0);
}
__device__ __forceinline__ unsigned pk2(float a, float b) {
  f32x2 v = {a, b};
  bf16x2_t r = __builtin_convertvector(v, bf16x2_t);
  return __builtin_bit_cast(unsigned, r);
}
__device__ __forceinline__ float rbf(float a) { return __builtin_bit_cast(float, pk2(0.f, a) & 0xffff0000u); }
__device__ __forceinline__ float bflo(unsigned u) { return __builtin_bit_cast(float, u << 16); }
__device__ __forceinline__ float bfhi(unsigned u) { return __builtin_bit_cast(float, u & 0xffff0000u); }
__device__ __forceinline__ u32x2 fin4(u32x2 xv, f32x4 sv, f32x4 gt) {
  return u32x2{pk2(bflo(xv[0]) + rbf(rbf(sv[0]) * gt[0]), bfhi(xv[0]) + rbf(rbf(sv[1]) * gt[1])),
               pk2(bflo(xv[1]) + rbf(rbf(sv[2]) * gt[2]), bfhi(xv[1]) + rbf(rbf(sv[3]) * gt[3]))};
}
__device__ __forceinline__ s16x4 trread(unsigned a) {
  return __builtin_amdgcn_ds_read_tr16_b64_v4i16((LAS s16x4*)(size_t)a);
}
__device__ __forceinline__ bf16x8 cat44(s16x4 a, s16x4 b) {
  return __builtin_shufflevector(a, b, 0, 1, 2, 3, 4, 5, 6, 7);
}
#define WAIT_VM(n) asm volatile("s_waitcnt vmcnt(" #n ")" ::: "memory")
#define WAIT_LGKM0() __builtin_amdgcn_s_waitcnt(0xC07F)
#define BAR() __builtin_amdgcn_s_barrier()
#define SB() __builtin_amdgcn_sched_barrier(0)
#define SGB(m, n) __builtin_amdgcn_sched_group_barrier(m, n, 0)
#define MAX3(a, b, c) __builtin_fmaxf(__builtin_fmaxf(a, b), c)
#define RLX_AGENT __ATOMIC_RELAXED, __HIP_MEMORY_SCOPE_AGENT
constexpr int SC1 = 16;
constexpr int CODE_KB = 38;
template <int N> struct IC { static constexpr int value = N; };

__device__ __forceinline__ void dma1(const char* sbase, unsigned voff, unsigned lds) {
  unsigned keep;
  asm volatile("s_mov_b32 %0, m0\n\ts_mov_b32 m0, %3\n\ts_nop 2\n\tglobal_load_lds_dwordx4 %1, %2\n\ts_mov_b32 m0, %0"
               : "=&s"(keep) : "v"(voff), "s"(sbase), "s"(lds) : "memory");
}
__device__ __forceinline__ void dma_stage(const char* sa, unsigned va0, unsigned va1, unsigned va2, unsigned va3, unsigned la,
                                          const char* sb, unsigned vb0, unsigned vb1, unsigned lb) {
  unsigned keep;
  asm volatile("s_mov_b32 %0, m0\n\ts_mov_b32 m0, %10\n\ts_nop 2\n\tglobal_load_lds_dwordx4 %8, %7\n\t"
               "s_add_u32 m0, m0, 1024\n\ts_nop 0\n\tglobal_load_lds_dwordx4 %9, %7\n\t"
               "s_add_u32 m0, m0, 7040\n\ts_nop 0\n\tglobal_load_lds_dwordx4 %8, %7 offset:128\n\t"
               "s_add_u32 m0, m0, 1024\n\ts_nop 0\n\tglobal_load_lds_dwordx4 %9, %7 offset:128\n\t"
               "s_mov_b32 m0, %6\n\ts_nop 0\n\tglobal_load_lds_dwordx4 %2, %1\n\t"
               "s_add_u32 m0, m0, 1024\n\ts_nop 0\n\tglobal_load_lds_dwordx4 %3, %1\n\t"
               "s_add_u32 m0, m0, 1024\n\ts_nop 0\n\tglobal_load_lds_dwordx4 %4, %1\n\t"
               "s_add_u32 m0, m0, 1024\n\ts_nop 0\n\tglobal_load_lds_dwordx4 %5, %1\n\ts_mov_b32 m0, %0"
               : "=&s"(keep) : "s"(sa), "v"(va0), "v"(va1), "v"(va2), "v"(va3), "s"(la), "s"(sb), "v"(vb0), "v"(vb1), "s"(lb)
               : "memory", "scc");
}
__device__ __forceinline__ void dma_stage_w(const char* sb, unsigned vb0, unsigned vb1, unsigned lb) {
  unsigned keep;
  asm volatile("s_mov_b32 %0, m0\n\ts_mov_b32 m0, %4\n\ts_nop 2\n\tglobal_load_lds_dwordx4 %2, %1\n\t"
               "s_add_u32 m0, m0, 1024\n\ts_nop 0\n\tglobal_load_lds_dwordx4 %3, %1\n\t"
               "s_add_u32 m0, m0, 7040\n\ts_nop 0\n\tglobal_load_lds_dwordx4 %2, %1 offset:128\n\t"
               "s_add_u32 m0, m0, 1024\n\ts_nop 0\n\tglobal_load_lds_dwordx4 %3, %1 offset:128\n\ts_mov_b32 m0, %0"
               : "=&s"(keep) : "s"(sb), "v"(vb0), "v"(vb1), "s"(lb) : "memory", "scc");
}
__device__ __forceinline__ void dma_kv(const char* sb, unsigned vk, unsigned vv, unsigned l) {
  unsigned keep;
  asm volatile("s_mov_b32 %0, m0\n\ts_mov_b32 m0, %4\n\ts_nop 2\n\tglobal_load_lds_dwordx4 %2, %1 sc1\n\t"
               "s_add_u32 m0, m0, 7168\n\ts_nop 0\n\tglobal_load_lds_dwordx4 %2, %1 offset:1024 sc1\n\t"
               "s_add_u32 m0, m0, 7168\n\ts_nop 0\n\tglobal_load_lds_dwordx4 %2, %1 offset:2048 sc1\n\t"
               "s_add_u32 m0, m0, 18432\n\ts_nop 0\n\tglobal_load_lds_dwordx4 %3, %1 sc1\n\t"
               "s_add_u32 m0, m0, 7232\n\ts_nop 0\n\tglobal_load_lds_dwordx4 %3, %1 offset:1024 sc1\n\t"
               "s_add_u32 m0, m0, 7232\n\ts_nop 0\n\tglobal_load_lds_dwordx4 %3, %1 offset:2048 sc1\n\t"
               "s_mov_b32 m0, %0"
               : "=&s"(keep) : "s"(sb), "v"(vk), "v"(vv), "s"(l) : "memory", "scc");
}
__device__ __forceinline__ void dma5_sc1(const char* sb, unsigned v, unsigned l) {
  unsigned keep;
  asm volatile("s_mov_b32 %0, m0\n\ts_mov_b32 m0, %3\n\ts_nop 2\n\tglobal_load_lds_dwordx4 %2, %1 sc1\n\t"
               "global_load_lds_dwordx4 %2, %1 offset:1024 sc1\n\tglobal_load_lds_dwordx4 %2, %1 offset:2048 sc1\n\t"
               "global_load_lds_dwordx4 %2, %1 offset:3072 sc1\n\ts_add_u32 m0, m0, 1024\n\ts_nop 0\n\t"
               "global_load_lds_dwordx4 %4, %1 offset:3072 sc1\n\ts_mov_b32 m0, %0"
               : "=&s"(keep) : "s"(sb), "v"(v), "s"(l), "v"(v + 1024) : "memory", "scc");
}
__device__ __forceinline__ u32x2 aload8(const char* sbase, unsigned voff) {
  u32x2 r;
  asm volatile("s_nop 4\n\tglobal_load_dwordx2 %0, %1, %2 sc1" : "=v"(r) : "v"(voff), "s"(sbase) : "memory");
  return r;
}
__device__ __forceinline__ u32x4 aload16(const char* sbase, unsigned voff) {
  u32x4 r;
  asm volatile("s_nop 4\n\tglobal_load_dwordx4 %0, %1, %2 sc1" : "=v"(r) : "v"(voff), "s"(sbase) : "memory");
  return r;
}
#define PIN(x) asm volatile("" : "+v"(x))
template <int N> __device__ __forceinline__ void wait_vm() { asm volatile("s_waitcnt vmcnt(%0)" :: "i"(N) : "memory"); }
__device__ __forceinline__ void poll_flags(const char* flag, bool need, u64 tag) {
#pragma unroll 1
  for (int spins = 0; spins < (1 << 20); ++spins) {
    const u64 v = need ? __hip_atomic_load((const GAS u64*)flag, RLX_AGENT) : tag;
    if (__all(v == tag)) break;
    __builtin_amdgcn_s_sleep(1);
  }
  __builtin_amdgcn_fence(__ATOMIC_ACQUIRE, "wavefront");
}
__device__ __forceinline__ unsigned lds_arrive(LAS unsigned* c) {
  unsigned r = 0;
  if ((threadIdx.x & 63) == 0) r = __hip_atomic_fetch_add(c, 1u, __ATOMIC_RELAXED, __HIP_MEMORY_SCOPE_WORKGROUP);
  return __builtin_amdgcn_readfirstlane(r);
}
__device__ __forceinline__ void lds_wait(LAS unsigned* c, unsigned n) {
#pragma unroll 1
  for (int spins = 0; spins < (1 << 22); ++spins) {
    if (__builtin_amdgcn_readfirstlane(__hip_atomic_load(c, __ATOMIC_RELAXED, __HIP_MEMORY_SCOPE_WORKGROUP)) >= n) break;
  }
  __builtin_amdgcn_fence(__ATOMIC_ACQUIRE, "wavefront");
}

constexpr int WOPRE = 4;
constexpr int W2HOLD = 10;
constexpr int STG = 32768, RT_OFF = 4 * STG, LDS_ALL = 163840;
constexpr int B1_OFF = 159744;
constexpr int ST_OFF = 160768;
constexpr int CS_OFF = ST_OFF + 1024, SYNC_OFF = ST_OFF + 2048;
constexpr int KP = 8192, VP = 8192 + 64;
constexpr int K_OFF = 0, V_OFF = K_OFF + 4 * KP;
__device__ __forceinline__ constexpr int slab_of(int w) { return 65536 + w * 20480; }
constexpr int REDG_OFF = 0, GX_OFF = 61440, REDS1_OFF = GX_OFF + 8192, REDS2_OFF = REDS1_OFF + 49152;

__device__ __forceinline__ void st16(u32x4 v, __amdgpu_buffer_rsrc_t rs, unsigned voff, unsigned soff) {
  __builtin_amdgcn_raw_buffer_store_b128(v, rs, voff + soff, 0, SC1);
}
__device__ __forceinline__ bf16x8 ld16(__amdgpu_buffer_rsrc_t rs, unsigned voff, unsigned soff) {
  return __builtin_bit_cast(bf16x8, __builtin_amdgcn_raw_buffer_load_b128(rs, voff, soff, SC1));
}
__device__ __forceinline__ int wg_of(int rt, int cg) { return (((cg & 3) * 8 + (rt & 7)) << 3) | ((cg >> 2) << 1) | (rt >> 3); }
template <int N>
__device__ __forceinline__ bool sum_pairs(const u32x4 (&v)[N], unsigned last_on, unsigned tg, float& a, float& b) {
  bool ok = true;
  a = 0.f; b = 0.f;
#pragma unroll
  for (int k = 0; k < N; ++k) {
    ok = ok && v[k][1] == tg && v[k][3] == tg;
    const unsigned m = k == N - 1 ? last_on : 0xffffffffu;
    a += __uint_as_float(v[k][0] & m);
    b += __uint_as_float(v[k][2] & m);
  }
  return __all(ok);
}
template <int N>
__device__ __forceinline__ void regather(__amdgpu_buffer_rsrc_t rs, const unsigned (&vo)[N], unsigned last_on, unsigned tg, float& a, float& b) {
#pragma unroll 1
  for (int spins = 0; spins < (1 << 20); ++spins) {
    u32x4 v[N];
#pragma unroll
    for (int k = 0; k < N; ++k) v[k] = __builtin_amdgcn_raw_buffer_load_b128(rs, vo[k], 0, SC1);
    if (sum_pairs<N>(v, last_on, tg, a, b)) break;
    __builtin_amdgcn_s_sleep(2);
  }
}

#define LDX(set, i, ks) xf[set][i] = *(LAS bf16x8*)(size_t)(xa[ks] + (i) * 2048)
#define LDW(set, j, ks) wf[set][j] = cat44(trread(wa[j] + (ks) * 4096), trread(wa[j] + (ks) * 4096 + 512))
#define HALF(set, ks) do { \
    _Pragma("unroll") for (int j = 0; j < NT; ++j) { \
      if (j == 0) { _Pragma("unroll") for (int i = 0; i < 4; ++i) LDX((set) ^ 1, i, ks); } \
      else if (2 * j - 2 < NT) { LDW((set) ^ 1, 2 * j - 2, ks); if (2 * j - 1 < NT) LDW((set) ^ 1, 2 * j - 1, ks); } \
      _Pragma("unroll") for (int i = 0; i < 4; ++i) acc[i][j] = mfma16(wf[set][j], xf[set][i], acc[i][j]); \
      SGB(0x8, 1); SGB(0x100, 1); SGB(0x8, 1); SGB(0x100, 1); SGB(0x8, 1); SGB(0x100, 1); SGB(0x8, 1); SGB(0x100, 1); \
      asm volatile("" :: "v"(acc[0][j]), "v"(acc[1][j]), "v"(acc[2][j]), "v"(acc[3][j])); \
      SB(); \
    } } while (0)
template <int NT, int S0>
__device__ __forceinline__ void mfma_part(unsigned (&xa)[2], unsigned wl, int tl0, int sigl, unsigned lds0, float (&sh)[4], float (&sc)[4],
                                          int row0, float qs, __amdgpu_buffer_rsrc_t rs, unsigned vo, unsigned so, int cs0, int g) {
  constexpr bool ISH = S0 == 10;
  unsigned wa[NT];
  f32x4 acc[4][NT];
#pragma unroll
  for (int j = 0; j < NT; ++j) {
    const int tl = tl0 + j;
    wa[j] = wl + (S0 & 3) * STG + (tl >> 2) * 8192 + (((tl & 3) ^ sigl) << 5);
#pragma unroll
    for (int i = 0; i < 4; ++i) acc[i][j] = f32x4{0.f, 0.f, 0.f, 0.f};
  }
  bf16x8 xf[2][4], wf[2][NT];
#pragma unroll
  for (int i = 0; i < 4; ++i) LDX(0, i, 0);
#pragma unroll
  for (int j = 0; j < NT; ++j) LDW(0, j, 0);
  WAIT_LGKM0();
  int buf = S0 & 3;
#pragma unroll 1
  for (int t = 0; t < 10; ++t) {
    HALF(0, 1);
    {
      const int d = buf == 3 ? -3 * STG : STG;
      buf = (buf + 1) & 3;
      xa[0] += d; xa[1] += d;
#pragma unroll
      for (int j = 0; j < NT; ++j) wa[j] += d;
    }
    WAIT_LGKM0();
    if (ISH) {
      asm volatile("s_cmp_lg_u32 %0, 1\n\ts_cbranch_scc1 .Lk%=\n\ts_waitcnt vmcnt(0)\n.Lk%=:\n\t"
                   "s_cmp_ge_u32 %0, 8\n\ts_cbranch_scc1 .Lc%=\n\ts_barrier\n.Lc%=:" :: "s"(t) : "memory", "scc");
    } else BAR();
    SB();
    HALF(1, 0);
    WAIT_LGKM0();
  }
  lds_wait((LAS unsigned*)(size_t)(lds0 + SYNC_OFF + (ISH ? 32 : 28)), 4);
  if (!ISH) {
#pragma unroll
    for (int i = 0; i < 4; ++i) {
      const f32x2 st = *(LAS f32x2*)(size_t)(lds0 + ST_OFF + (row0 + 16 * i) * 8);
      sc[i] = st[1]; sh[i] = -st[0] * st[1];
    }
  }
#pragma unroll
  for (int j = 0; j < NT; ++j) {
    const f32x4 c4 = *(LAS f32x4*)(size_t)(lds0 + CS_OFF + (ISH ? 512 : 0) + ((cs0 + j) * 16 + 4 * g) * 4);
    f32x4 bv = {0.f, 0.f, 0.f, 0.f};
    if (ISH) {
      const u32x2 bb = *(LAS u32x2*)(size_t)(lds0 + B1_OFF + ((cs0 + j) * 16 + 4 * g) * 2);
      bv = f32x4{bflo(bb[0]), bfhi(bb[0]), bflo(bb[1]), bfhi(bb[1])};
    }
#pragma unroll
    for (int pp = 0; pp < 2; ++pp) {
      float v[2][4];
#pragma unroll
      for (int i = 0; i < 2; ++i)
#pragma unroll
        for (int r = 0; r < 4; ++r) {
          float t = __builtin_fmaf(sc[2 * pp + i], acc[2 * pp + i][j][r], __builtin_fmaf(sh[2 * pp + i], c4[r], bv[r]));
          if (ISH) { t = t > 0.f ? t : 0.f; t = t * t; } else t *= qs;
          v[i][r] = t;
        }
      const unsigned x0 = pk2(v[0][0], v[0][1]), x1 = pk2(v[0][2], v[0][3]);
      const unsigned y0 = pk2(v[1][0], v[1][1]), y1 = pk2(v[1][2], v[1][3]);
      const auto s0 = __builtin_amdgcn_permlane16_swap(x0, y0, false, false);
      const auto s1 = __builtin_amdgcn_permlane16_swap(x1, y1, false, false);
      const u32x4 o = {s0[0], s1[0], s0[1], s1[1]};
      st16(o, rs, vo + pp * 512, so + 2 * j * 1024);
    }
  }
}

__global__ __launch_bounds__(512, 2) void k_all(const char* __restrict__ x, const char* __restrict__ wqkv, const char* __restrict__ w1,
                                                const char* __restrict__ b1, const char* __restrict__ w2, const char* __restrict__ wo,
                                                const char* __restrict__ b2, char* ws, char* __restrict__ out) {
  extern __shared__ __attribute__((aligned(16))) char smem_[];
  LAS char* smem = (LAS char*)smem_;
  const int tid = threadIdx.x, lane = tid & 63, w = __builtin_amdgcn_readfirstlane(tid >> 6);
  const int b = blockIdx.x, xcd = b & 7, idx = b >> 3;
  const unsigned lds0 = (unsigned)(size_t)smem;
  asm volatile("" :: "s"(x), "s"(wqkv), "s"(w1), "s"(b1), "s"(w2), "s"(wo), "s"(ws));
  u64 pc_;
  asm volatile("s_getpc_b64 %0" : "=s"(pc_));
  const int rt = (xcd & 1) * 8 + (idx & 7), cg = (xcd >> 1) * 4 + (idx >> 3);
  const int rot = (rt + 3 * cg) % 10;
  const int lw = w - 4, lr = lane >> 3, ls = lane & 7;
  const int bk = 16 * lw + lr, sw0 = (ls ^ (bk & 2)) << 4, sw1 = sw0 ^ 64;
  const unsigned vq0 = bk * 3840 + (cg < 15 ? cg : 14) * 256 + sw0, vq1 = (bk + 8) * 3840 + (cg < 15 ? cg : 14) * 256 + sw1;
  const unsigned vA = (rt * 128 + 32 * lw + lr) * 1280 + ((ls ^ (lr >> 1)) << 4);
  const unsigned vA1 = (vA + 10240) ^ 64u, vA2 = vA + 20480, vA3 = (vA + 30720) ^ 64u;
  const int rkb = b / 5, rnb = b - 5 * rkb;
  if (w >= 4) {
    dma_stage(x + rot * 128, vA, vA1, vA2, vA3, lds0 + lw * 4096, wqkv + (size_t)rot * (64 * 3840), vq0, vq1, lds0 + 16384 + lw * 2048);
    if (b < 170) {
      const char* Wr = rkb < 24 ? w2 + (size_t)rkb * 64 * 1280 : wo + (size_t)(rkb - 24) * 64 * 1280;
      const unsigned r0 = bk * 1280 + rnb * 256 + sw0, r1 = (bk + 8) * 1280 + rnb * 256 + sw1;
      dma1(Wr, r0, lds0 + RT_OFF + lw * 2048);
      dma1(Wr, r1, lds0 + RT_OFF + lw * 2048 + 1024);
      dma1(Wr + 128, r0, lds0 + RT_OFF + 8192 + lw * 2048);
      dma1(Wr + 128, r1, lds0 + RT_OFF + 8192 + lw * 2048 + 1024);
    }
    const int kt = rot == 9 ? 0 : rot + 1;
    dma_stage(x + kt * 128, vA, vA1, vA2, vA3, lds0 + STG + lw * 4096, wqkv + (size_t)kt * (64 * 3840), vq0, vq1,
              lds0 + STG + 16384 + lw * 2048);
  }
  SB();
  const int g = lane >> 4, l15 = lane & 15, q4 = l15 >> 2, p4 = lane & 3;
  const char* pc0 = (const char*)(pc_ & ~(u64)63);
  const __amdgpu_buffer_rsrc_t rs = __builtin_amdgcn_make_buffer_rsrc(ws, 0, WS_USED, 0x00020000);
  LAS unsigned* sync = (LAS unsigned*)(smem + SYNC_OFF);
  u64 tag;
  {
    const char* p = ws;
    asm volatile("" : "+s"(p));
    tag = *(const CAS u64*)(p + OFF_FL) + 1;
  }
  const int lu1 = (6 * cg) & 3;
  const int sigl = ((q4 >> 1) & 1) | ((g & 1) << 1), xsw = g ^ (l15 >> 1);
  const int mt = (xcd >> 1) * 8 + (idx & 7), ct = (xcd & 1) * 4 + (idx >> 3);
  const int head = 4 * xcd + (idx & 3), qb = idx >> 2;
  const int ch = (5 * head) >> 1, odd = head & 1;
  auto orow_f = [&]() {
    unsigned ln = lane;
    asm volatile("" : "+v"(ln));
    return (64 * mt + 16 * (w & 3) + (ln & 15)) * 1280 + (80 * ct + 4 * (ln >> 4)) * 2;
  };
  u32x2 xv[3];
  f32x4 acc[4][5];
  const unsigned va = g * 1024 + l15 * 16;
  bf16x8 wpre[5][5];
  const unsigned wb = w < 4 ? OFF_W2F + (ct * 48 + 12 * w) * 5120 : OFF_WOF + (ct * 20 + 5 * (w - 4)) * 5120;

  if (w >= 4) {
    const int lt = tid - 256;
    const unsigned tg = (unsigned)tag;
    const unsigned vh0 = bk * 3072 + ((6 * cg) >> 2) * 128 + sw0, vh1 = (bk + 8) * 3072 + ((6 * cg) >> 2) * 128 + sw1;
    const int rot2 = rot + 6 >= 10 ? rot - 4 : rot + 6;
    int ktn = rot + 2 >= 10 ? rot - 8 : rot + 2;
    auto issue = [&](auto S) {
      constexpr int s = decltype(S)::value;
      const unsigned dst = lds0 + (s & 3) * STG;
      if (s == 10) ktn = rot2;
      const char* sw = s >= 10 ? w1 + (size_t)ktn * (64 * 3072) : wqkv + (size_t)ktn * (64 * 3840);
      if constexpr (s >= 10 && s < 14) dma_stage_w(sw, vh0, vh1, dst + 16384 + lw * 2048);
      else dma_stage(x + ktn * 128, vA, vA1, vA2, vA3, dst + lw * 4096, sw, s >= 10 ? vh0 : vq0, s >= 10 ? vh1 : vq1, dst + 16384 + lw * 2048);
      ktn = ktn == 9 ? 0 : ktn + 1;
    };
    if (lt < 16) sync[lt] = 0u;
    WAIT_VM(8);
    BAR();
    issue(IC<2>{}); issue(IC<3>{});
    const bf16x8 ONE8 = {0x3f80, 0x3f80, 0x3f80, 0x3f80, 0x3f80, 0x3f80, 0x3f80, 0x3f80};
    const f32x4 z4 = {0.f, 0.f, 0.f, 0.f};
    bf16x8 sw_[2][2], sx_[2][2];
    u32x4 gr[5], gc[3];
    u32x2 fl;
    const int fkb = lane < 12 ? 6 * lw + (lane >> 1) : 24 + ((5 * lw) >> 1) + (lane < 18 ? (lane - 12) >> 1 : 0);
    const unsigned fW = FL_W + (fkb * 5 + ((5 * ct + (lane & 1) * 4) >> 3)) * 128;
    const int ty = lane < 24 ? lane >> 3 : 0, rr0 = 2 * qb - 2 + ((lane >> 1) & 3);
    const int rr = rr0 < 0 ? 0 : (ty == 0 && rr0 < 2 * qb ? 2 * qb : rr0);
    const unsigned fQ = FL_Q + wg_of(rr, 5 * ty + ((ch + (lane & 1) * 2) >> 4)) * 128;
    unsigned ro[5], co[3];
#pragma unroll
    for (int k = 0; k < 5; ++k) ro[k] = OFF_RS + (rt * 10 + 2 * k + (lane >> 5)) * 2048 + (32 * lw + (lane & 31)) * 16;
#pragma unroll
    for (int k = 0; k < 3; ++k) co[k] = OFF_CS + (cg * 10 + (k == 2 && g >= 2 ? g : g + 4 * k)) * 1024 + (16 * lw + l15) * 16;
    const unsigned c_on = g < 2 ? 0xffffffffu : 0u;
    auto col_sums = [&](int part) {
#pragma unroll
      for (int k = 0; k < 3; ++k) PIN(gc[k]);
      float c0, c1;
      if (!sum_pairs<3>(gc, c_on, tg, c0, c1)) regather<3>(rs, co, c_on, tg, c0, c1);
      c0 += __shfl_xor(c0, 16); c1 += __shfl_xor(c1, 16);
      c0 += __shfl_xor(c0, 32); c1 += __shfl_xor(c1, 32);
      if (lane < 16) *(LAS f32x2*)(smem + CS_OFF + part * 512 + (16 * lw + lane) * 8) = f32x2{c0, c1};
      WAIT_LGKM0();
      lds_arrive(sync + 7 + part);
    };
    auto stage = [&](auto S) {
      constexpr int s = decltype(S)::value;
      if constexpr (s == 0 || s == 10) {
        const unsigned sb = lds0 + (s & 3) * STG;
#pragma unroll
        for (int i = 0; i < 2; ++i) {
          const int lux = ((s == 0 ? 0 : lu1) + 2 * lw + i) & 7;
          const unsigned wq = sb + 16384 + (lux >> 2) * 8192 + (8 * g + q4) * 128 + (((lux & 3) ^ sigl) << 5) + p4 * 8;
          sw_[i][0] = cat44(trread(wq), trread(wq + 512));
          sw_[i][1] = cat44(trread(wq + 4096), trread(wq + 4608));
          if (s == 0) {
            const unsigned xq = sb + (32 * lw + 16 * i + l15) * 128;
            sx_[i][0] = *(LAS bf16x8*)(size_t)(xq + (xsw << 4));
            sx_[i][1] = *(LAS bf16x8*)(size_t)(xq + ((xsw ^ 4) << 4));
          }
        }
      }
      constexpr int younger[18] = {16, 16, 16, 16, 16, 16, 24, 20, 8, 8, 8 + 5 * WOPRE, 12 + 5 * WOPRE, 16 + 5 * WOPRE, 16, 16, 19, 19, 0};
      wait_vm<younger[s]>();
      WAIT_LGKM0();
      BAR();
      if constexpr (s < 16) issue(IC<s + 4>{});
      if constexpr (s == 2 || s == 3) {
        if (b < 170) {
          const unsigned dstb = rkb < 24 ? OFF_W2F : OFF_WOF;
          const int nks = rkb < 24 ? 48 : 20, kb = rkb < 24 ? rkb : rkb - 24;
          const int nl = 2 * lw + s - 2, nt = 8 * rnb + nl, c5 = nt / 5, jj = nt - 5 * c5;
          const unsigned rp = lds0 + RT_OFF + (nl >> 2) * 8192 + (8 * g + q4) * 128 + (((nl & 3) ^ sigl) << 5) + p4 * 8;
#pragma unroll
          for (int ks = 0; ks < 2; ++ks) {
            const bf16x8 f = cat44(trread(rp + ks * 4096), trread(rp + ks * 4096 + 512));
            st16(__builtin_bit_cast(u32x4, f), rs, lane * 16, dstb + ((c5 * nks + 2 * kb + ks) * 5 + jj) * 1024);
          }
        }
      }
      if constexpr (s == 0 || s == 10) {
        __builtin_amdgcn_s_setprio(3);
        const u32x4 c0 = __builtin_bit_cast(u32x4, mfma16(sw_[0][1], ONE8, mfma16(sw_[0][0], ONE8, z4)));
        const u32x4 c1 = __builtin_bit_cast(u32x4, mfma16(sw_[1][1], ONE8, mfma16(sw_[1][0], ONE8, z4)));
        const u32x4 cu = l15 == 0 ? c0 : c1;
        if (l15 < 2) {
          const unsigned o = OFF_CS + (((s == 10 ? 16 : 0) + cg) * 10 + (s == 10 ? rot2 : rot)) * 1024 + 2 * lw * 128;
          st16(u32x4{cu[0], tg, cu[1], tg}, rs, l15 * 128 + g * 32, o);
          st16(u32x4{cu[2], tg, cu[3], tg}, rs, l15 * 128 + g * 32 + 16, o);
        }
        __builtin_amdgcn_s_setprio(0);
      }
      if constexpr (s == 1) {
        __builtin_amdgcn_s_setprio(3);
#pragma unroll
        for (int i = 0; i < 2; ++i) {
          const f32x4 sx = mfma16(ONE8, sx_[i][1], mfma16(ONE8, sx_[i][0], z4));
          const f32x4 sq = mfma16(sx_[i][1], sx_[i][1], mfma16(sx_[i][0], sx_[i][0], z4));
          const int r = l15 & 3;
          const float sxx = r == 0 ? sq[0] : (r == 1 ? sq[1] : (r == 2 ? sq[2] : sq[3]));
          if (q4 == g)
            st16(u32x4{__float_as_uint(sx[0]), tg, __float_as_uint(sxx), tg}, rs, (32 * lw + 16 * i + l15) * 16,
                 OFF_RS + (rt * 10 + rot) * 2048);
        }
        __builtin_amdgcn_s_setprio(0);
      }
      if constexpr (s == 4) {
        if (lt < 12) dma1(b1 + cg * 192, lane * 16, lds0 + B1_OFF);
        if (lw == 1 && lane < 10) dma1(b2 + ct * 160, lane * 16, lds0 + B1_OFF + 256);
      }
      if constexpr (s == 6 || s == 11) {
        if (lt == 0 && (s == 6 || cg < 15)) __hip_atomic_store((GAS u64*)(ws + (s == 6 ? FL_W : FL_Q) + b * 128), tag, RLX_AGENT);
      }
      if constexpr (s == 5) {
#pragma unroll
        for (int k = 0; k < 5; ++k) gr[k] = aload16(ws, ro[k]);
      }
      if constexpr (s == 5 || s == 14) {
#pragma unroll
        for (int k = 0; k < 3; ++k) { if (s == 14) co[k] += 16 * 10240; gc[k] = aload16(ws, co[k]); }
      }
      if constexpr (s == 8) {
        __builtin_amdgcn_s_setprio(3);
#pragma unroll
        for (int k = 0; k < 5; ++k) PIN(gr[k]);
        float sx, sxx;
        if (!sum_pairs<5>(gr, 0xffffffffu, tg, sx, sxx)) regather<5>(rs, ro, 0xffffffffu, tg, sx, sxx);
        sx += __shfl_xor(sx, 32); sxx += __shfl_xor(sxx, 32);
        const float mean = sx * (1.f / 640.f);
        float var = sxx * (1.f / 640.f) - mean * mean;
        var = var < 0.f ? 0.f : var;
        if (lane < 32) *(LAS f32x2*)(smem + ST_OFF + (32 * lw + lane) * 8) = f32x2{mean, rsqrtf(var + 1e-5f)};
        col_sums(0);
        __builtin_amdgcn_s_setprio(0);
      }
      if constexpr (s == 9) {
        poll_flags(ws + fW, true, tag);
#pragma unroll
        for (int k = 0; k < WOPRE; ++k)
#pragma unroll
          for (int j = 0; j < 5; ++j) wpre[k][j] = ld16(rs, lane * 16, wb + k * 5120 + j * 1024);
      }
      if constexpr (s == 16) fl = aload8(ws, fQ);
    };
    stage(IC<0>{}); stage(IC<1>{}); stage(IC<2>{}); stage(IC<3>{}); stage(IC<4>{}); stage(IC<5>{});
    stage(IC<6>{}); stage(IC<7>{}); stage(IC<8>{}); stage(IC<9>{}); stage(IC<10>{}); stage(IC<11>{});
    stage(IC<12>{}); stage(IC<13>{}); stage(IC<14>{}); stage(IC<15>{}); stage(IC<16>{}); stage(IC<17>{});
    PIN(fl);
    if (!__all(fl[0] == tg && fl[1] == (unsigned)(tag >> 32))) poll_flags(ws + fQ, true, tag);
#pragma unroll
    for (int i = 0; i < 2; ++i) {
      const int seg = 2 * lw + i;
      int tb = 4 * qb - 4 + seg; tb = tb < 0 ? 0 : tb;
      dma_kv(ws + OFF_QKV + (size_t)(tb * 240 + 80 + ch) * 1024, lane * 16, lane * 16 + 81920, lds0 + K_OFF + seg * 1024);
    }
    const int l31 = lane & 31, hh = lane >> 5;
    u32x4 qraw[2][2];
#pragma unroll
    for (int a = 0; a < 2; ++a)
#pragma unroll
      for (int ks = 0; ks < 2; ++ks)
        qraw[a][ks] = __builtin_amdgcn_raw_buffer_load_b128(rs, (ks ? 2 : hh) * 1024 + (32 * a + l31) * 16,
                                                            OFF_QKV + ((4 * qb + lw) * 240 + ch) * 1024, SC1);
    col_sums(1);
    {
      const u32x4 one4 = {0x3f803f80u, 0x3f803f80u, 0x3f803f80u, 0x3f803f80u};
      *(LAS u32x4*)(smem + V_OFF + 3 * VP + lt * 16) = one4;
      if (lt < 40) *(LAS u32x4*)(smem + V_OFF + 3 * VP + 4096 + lt * 16) = one4;
      *(LAS u32x4*)(smem + K_OFF + 3 * KP + lt * 16) = u32x4{qb == 0 ? 0xff80u : 0u, 0u, 0u, 0u};
      *(LAS u32x4*)(smem + K_OFF + 3 * KP + 4096 + lt * 16) = u32x4{0u, 0u, 0u, 0u};
    }
    bf16x8 qf[2][2];
#pragma unroll
    for (int a = 0; a < 2; ++a) {
      u32x4 v0 = qraw[a][0], v1 = qraw[a][1];
      if (odd && hh == 0) { v0[0] = 0u; v0[1] = 0u; }
      if (hh) v1 = u32x4{0x3f80u, 0u, 0u, 0u};
      else if (!odd) { v1[2] = 0u; v1[3] = 0u; }
      qf[a][0] = __builtin_bit_cast(bf16x8, v0);
      qf[a][1] = __builtin_bit_cast(bf16x8, v1);
    }
    f32x16 mF, z16;
#pragma unroll
    for (int e = 0; e < 16; ++e) {
      mF[e] = (e & 3) + 8 * (e >> 2) + 4 * hh > l31 ? 0.f : -INFINITY;
      z16[e] = 0.f;
    }
    asm volatile("" : "+v"(mF), "+v"(qf[0][0]), "+v"(qf[0][1]), "+v"(qf[1][0]), "+v"(qf[1][1]));
    WAIT_VM(0);
    WAIT_LGKM0();
    lds_arrive(sync);
    lds_wait(sync, 4);
    __builtin_amdgcn_s_setprio(2);
    unsigned kp = lds0 + K_OFF + hh * KP + (64 * lw + l31) * 16;
    const bool ones = (g & 1) && (p4 >> 1);
    unsigned vp = lds0 + V_OFF + (2 * (g & 1) + (p4 >> 1)) * VP + ((ones ? 0 : 64 * lw) + 4 * hh + q4) * 16 + (p4 & 1) * 8;
    unsigned ov = l31 * 16 + hh * 8;
    bf16x8 q0 = qf[0][0], q1 = qf[0][1];
#define LDK(T, ks) (*(LAS bf16x8*)(size_t)(kp + (T) * 512 + (ks) * 2 * KP))
#define MF32(a, b, c) __builtin_amdgcn_mfma_f32_32x32x16_bf16(a, b, c, 0, 0, 0)
#define EX2(s_, e) pk2(__builtin_amdgcn_exp2f((s_)[e]), __builtin_amdgcn_exp2f((s_)[(e) + 1]))
    auto tile_max = [&](int T) {
      f32x16 sp = MF32(LDK(T, 1), q1, MF32(LDK(T, 0), q0, z16));
      float m0 = -INFINITY, m1 = -INFINITY;
#pragma unroll
      for (int e = 0; e < 16; ++e) {
        const bool off = T == 0 ? mF[e] != 0.f : (T == 8 ? mF[e] == 0.f : false);
        sp[e] = off ? -INFINITY : sp[e];
      }
#pragma unroll
      for (int e = 0; e < 16; e += 4) { m0 = MAX3(m0, sp[e], sp[e + 1]); m1 = MAX3(m1, sp[e + 2], sp[e + 3]); }
      m0 = fmaxf(m0, m1);
      return fmaxf(m0, __shfl_xor(m0, 32));
    };
#pragma unroll 1
    for (int un = 0; un < 2; ++un) {
      float mx = tile_max(8);
      f32x16 oa;
#pragma unroll 1
      for (int pass = 0; pass < 2; ++pass) {
      f32x16 ca;
#pragma unroll
      for (int e = 0; e < 16; ++e) ca[e] = -mx;
      oa = z16;
      {
        bf16x8 kc[2][2], vf[2][2];
        f32x16 sc[2];
        u32x4 pa[2] = {{0u, 0u, 0u, 0u}, {0u, 0u, 0u, 0u}};
        kc[0][0] = LDK(0, 0); kc[0][1] = LDK(0, 1); kc[1][0] = LDK(1, 0); kc[1][1] = LDK(1, 1);
        sc[0] = MF32(kc[0][1], q1, MF32(kc[0][0], q0, ca + mF));
        sc[1] = z16;
        asm volatile("" :: "v"(sc[0]));
        SB();
#pragma unroll
        for (int T = 0; T < 10; ++T) {
          const int c = T & 1, n = c ^ 1;
          if (T < 9) {
#pragma unroll
            for (int ks = 0; ks < 2; ++ks) vf[c][ks] = cat44(trread(vp + T * 512 + ks * 256), trread(vp + T * 512 + ks * 256 + 128));
          }
          if (T < 8) sc[n] = MF32(kc[n][1], q1, MF32(kc[n][0], q0, ca));
          if (T < 7) { kc[c][0] = LDK(T + 2, 0); kc[c][1] = LDK(T + 2, 1); }
          if (T > 0) oa = MF32(vf[n][1], __builtin_bit_cast(bf16x8, pa[1]), MF32(vf[n][0], __builtin_bit_cast(bf16x8, pa[0]), oa));
          if (T == 8) {
#pragma unroll
            for (int e = 0; e < 16; ++e) sc[c][e] = mF[e] == 0.f ? -INFINITY : sc[c][e];
          }
          if (T < 9) {
#pragma unroll
            for (int e = 0; e < 4; ++e) { pa[0][e] = EX2(sc[c], 2 * e); pa[1][e] = EX2(sc[c], 8 + 2 * e); }
          }
          SGB(0x100, 6);
          if (T == 8) SGB(0x2, 16);
          SGB(0x8, 1); SGB(0x400, 4); SGB(0x2, 1);
#pragma unroll
          for (int k = 0; k < 3; ++k) { SGB(0x8, 1); SGB(0x400, 2); SGB(0x2, 1); SGB(0x400, 2); SGB(0x2, 1); }
          SGB(0x2, 1);
          asm volatile("" :: "v"(sc[n]), "v"(oa), "v"(pa[0]), "v"(pa[1]));
          SB();
        }
      }
      if (pass || __all(oa[12] < 4294967296.f)) break;
#pragma unroll 1
      for (int T = 0; T < 8; ++T) mx = fmaxf(mx, tile_max(T));
      }
      const float il = __builtin_amdgcn_rcpf(oa[12]);
#pragma unroll
      for (int pl = 0; pl < 3; ++pl) {
        const bool ok = pl == 1 || (pl == 0 ? !(odd && hh == 0) : (odd || hh == 0));
        if (ok) {
          const u32x2 pkd = {pk2(oa[4 * pl] * il, oa[4 * pl + 1] * il), pk2(oa[4 * pl + 2] * il, oa[4 * pl + 3] * il)};
          __builtin_amdgcn_raw_buffer_store_b64(pkd, rs, pl * 1024 + ov, OFF_O + ((4 * qb + lw) * 80 + ch) * 1024, SC1);
        }
      }
      kp += 512; vp += ones ? 0 : 512; ov += 512;
      q0 = qf[1][0]; q1 = qf[1][1];
    }
    __builtin_amdgcn_s_setprio(0);
    lds_arrive(sync + 4);
    WAIT_VM(0);
    if (lane == 0) __hip_atomic_store((GAS u64*)(ws + FL_O + (b * 4 + lw) * 128), tag, RLX_AGENT);
#pragma unroll
    for (int k = WOPRE; k < 5; ++k)
#pragma unroll
      for (int j = 0; j < 5; ++j) wpre[k][j] = ld16(rs, lane * 16, wb + k * 5120 + j * 1024);
    const unsigned orow = orow_f();
#pragma unroll
    for (int j = 0; j < 2; ++j) xv[j] = *(const u32x2*)(x + orow + (3 + j) * 32);
    const unsigned ab = OFF_O + (mt * 80 + 20 * lw) * 1024;
    {
      const int hd = 8 * lw + (lane & 7);
      poll_flags(ws + FL_O + ((((((mt >> 2) << 2) | (hd & 3)) << 3) | (hd >> 2)) * 4 + (mt & 3)) * 128, lane < 8, tag);
    }
    bf16x8 af[4][5];
#pragma unroll
    for (int i = 0; i < 4; ++i)
#pragma unroll
      for (int k = 0; k < 5; ++k) af[i][k] = ld16(rs, va + ((lw + 1 + i) & 3) * 256, ab + k * 4096);
    SB();
    lds_wait(sync + 4, 4);
    lds_wait(sync + 6, 4);
    f32x4 own[2];
#pragma unroll
    for (int i = 0; i < 4; ++i) {
      const int mi = (lw + 1 + i) & 3;
      f32x4 a5[5] = {z4, z4, z4, z4, z4};
#pragma unroll
      for (int k = 0; k < 5; ++k)
#pragma unroll
        for (int j = 0; j < 5; ++j) a5[j] = mfma16(wpre[k][j], af[i][k], a5[j]);
#pragma unroll
      for (int j = 0; j < 3; ++j) *(LAS f32x4*)(smem + REDS1_OFF + ((mi * 4 + lw) * 3 + j) * 1024 + lane * 16) = a5[j];
      if (i < 3) {
#pragma unroll
        for (int j = 0; j < 2; ++j) *(LAS f32x4*)(smem + REDS2_OFF + ((mi * 3 + 2 - i) * 2 + j) * 1024 + lane * 16) = a5[3 + j];
      } else { own[0] = a5[3]; own[1] = a5[4]; }
    }
    __syncthreads();
#pragma unroll
    for (int j = 0; j < 2; ++j) {
      f32x4 sv = own[j];
#pragma unroll
      for (int k = 0; k < 3; ++k) sv += *(LAS f32x4*)(smem + REDS2_OFF + ((lw * 3 + k) * 2 + j) * 1024 + lane * 16);
      const f32x4 gt = *(LAS f32x4*)(smem + GX_OFF + (lw * 2 + j) * 1024 + lane * 16);
      *(u32x2*)(out + orow + (3 + j) * 32) = fin4(xv[j], sv, gt);
    }
  } else {
    if (idx * 4 + w < CODE_KB) {
      const u32x4 t0 = *(const u32x4*)(pc0 + (idx * 4 + w) * 1024 + lane * 16);
      asm volatile("" :: "v"(t0));
    }
    {
      const int mw = w & 1, nw = w >> 1;
      unsigned xa[2];
      xa[0] = lds0 + (64 * mw + l15) * 128 + (xsw << 4);
      xa[1] = lds0 + (64 * mw + l15) * 128 + ((xsw ^ 4) << 4);
      const unsigned wl = lds0 + 16384 + (8 * g + q4) * 128 + p4 * 8;
      const unsigned vo = (g >> 1) * 1024 + (16 * (g & 1) + l15) * 16;
      float sh[4], sc[4];
      BAR();
      mfma_part<4, 0>(xa, wl, 4 * nw, sigl, lds0, sh, sc, 64 * mw + l15, cg < 5 ? 0.22360679774997896f * 1.4426950408889634f : 1.f, rs, vo,
                      cg < 15 ? OFF_QKV + ((2 * rt + mw) * 240 + 16 * cg + 8 * nw) * 1024 : 0x80000000u, 4 * nw, g);
      mfma_part<3, 10>(xa, wl, lu1 + 3 * nw, sigl, lds0, sh, sc, 0, 1.f, rs, vo,
                       OFF_H + ((2 * rt + mw) * 192 + 12 * cg + 6 * nw) * 1024, 3 * nw, g);
    }
    WAIT_VM(0);
    if (lds_arrive(sync + 2) == 3 && lane == 0) __hip_atomic_store((GAS u64*)(ws + FL_H + b * 128), tag, RLX_AGENT);
#pragma unroll
    for (int k = 0; k < 4; ++k)
#pragma unroll
      for (int j = 0; j < 5; ++j) wpre[k][j] = ld16(rs, lane * 16, wb + k * 5120 + j * 1024);
    const unsigned wl_ = lds0 + slab_of(w);
    lds_wait(sync + 2, 4);
    dma5_sc1(ws + wb + 4 * 5120, lane * 16, wl_);
    dma5_sc1(ws + wb + 5 * 5120, lane * 16, wl_ + 5120);
    const unsigned ab = OFF_H + (mt * 192 + 48 * w) * 1024;
    if (b == 0 && w == 0) {
#pragma unroll 1
      for (int spins = 0; spins < (1 << 20); ++spins) {
        u64 v[4];
#pragma unroll
        for (int i = 0; i < 4; ++i) v[i] = __hip_atomic_load((const GAS u64*)(ws + FL_W + (lane + 64 * i) * 128), RLX_AGENT);
        if (__all(((v[0] ^ tag) | (v[1] ^ tag) | (v[2] ^ tag) | (v[3] ^ tag)) == 0)) break;
        __builtin_amdgcn_s_sleep(1);
      }
      if (lane == 0) __hip_atomic_store((GAS u64*)(ws + OFF_FL), tag, RLX_AGENT);
    }
    poll_flags(ws + FL_H + wg_of(mt >> 1, 4 * w + (lane & 3)) * 128, lane < 4, tag);
    unsigned vi[4];
#pragma unroll
    for (int i = 0; i < 4; ++i) vi[i] = va + ((w + 1 + i) & 3) * 256;
    dma5_sc1(ws + ab + 4 * 4096, lane * 16, wl_ + 10240);
    dma5_sc1(ws + ab + 4 * 4096 + 3072, lane * 16, wl_ + 10240 + 3072);
    bf16x8 af[4][4];
#pragma unroll
    for (int k = 0; k < 4; ++k)
#pragma unroll
      for (int i = 0; i < 4; ++i) af[k][i] = ld16(rs, vi[i], ab + k * 4096);
    SB();
#pragma unroll
    for (int i = 0; i < 4; ++i)
#pragma unroll
      for (int j = 0; j < 5; ++j) acc[i][j] = f32x4{0.f, 0.f, 0.f, 0.f};
#pragma unroll
    for (int k = 0; k < 12; ++k) {
      const int cs = k & 3;
      if (k == 6)
        asm volatile("s_mov_b64 exec, 1\n\tds_add_u32 %0, %1\n\ts_mov_b64 exec, -1" :: "v"(lds0 + SYNC_OFF + 24), "v"(1u) : "memory");
      if (k == W2HOLD) {
        unsigned tv, ts;
        asm volatile(".Lp%=:\n\tds_read_b32 %0, %2\n\ts_waitcnt lgkmcnt(0)\n\tv_readfirstlane_b32 %1, %0\n\t"
                     "s_cmp_ge_u32 %1, 4\n\ts_cbranch_scc1 .Le%=\n\ts_sleep 2\n\ts_branch .Lp%=\n.Le%=:"
                     : "=&v"(tv), "=&s"(ts) : "v"(lds0 + SYNC_OFF + 16) : "memory", "scc");
      }
#pragma unroll
      for (int i = 0; i < 4; ++i) {
#pragma unroll
        for (int j = 0; j < 5; ++j) acc[i][j] = mfma16(wpre[cs][j], af[cs][i], acc[i][j]);
        if (k == 11 && i < 3) {
#pragma unroll
          for (int j = 0; j < 5; ++j)
            *(LAS f32x4*)(smem + REDG_OFF + ((((w + 1 + i) & 3) * 3 + 2 - i) * 5 + j) * 1024 + lane * 16) = acc[i][j];
        }
      }
      SB();
      if (k < 2) {
#pragma unroll
        for (int j = 0; j < 5; ++j) wpre[cs][j] = *(LAS bf16x8*)(size_t)(wl_ + k * 5120 + j * 1024 + lane * 16);
#pragma unroll
        for (int i = 0; i < 4; ++i) af[cs][i] = *(LAS bf16x8*)(size_t)(wl_ + 10240 + k * 4096 + vi[i]);
      } else if (k < 8) {
#pragma unroll
        for (int i = 0; i < 4; ++i) af[cs][i] = ld16(rs, vi[i], ab + (k + 4) * 4096);
#pragma unroll
        for (int j = 0; j < 5; ++j) wpre[cs][j] = ld16(rs, lane * 16, wb + (k + 4) * 5120 + j * 1024);
      }
      if (k == 10) {
        const unsigned orow = orow_f();
#pragma unroll
        for (int j = 0; j < 3; ++j) xv[j] = *(const u32x2*)(x + orow + j * 32);
      }
      SB();
    }
    u32x2 bb[5];
#pragma unroll
    for (int j = 0; j < 5; ++j) bb[j] = *(LAS u32x2*)(smem + B1_OFF + 256 + (16 * j + 4 * g) * 2);
    WAIT_LGKM0();
    lds_arrive(sync + 3);
    lds_wait(sync + 3, 4);
    f32x4 gt[5];
#pragma unroll
    for (int j = 0; j < 5; ++j) {
      gt[j] = acc[3][j];
#pragma unroll
      for (int k = 0; k < 3; ++k) gt[j] += *(LAS f32x4*)(smem + REDG_OFF + ((w * 3 + k) * 5 + j) * 1024 + lane * 16);
    }
#pragma unroll
    for (int j = 0; j < 5; ++j) {
      const float bv[4] = {bflo(bb[j][0]), bfhi(bb[j][0]), bflo(bb[j][1]), bfhi(bb[j][1])};
#pragma unroll
      for (int e = 0; e < 4; ++e) {
        const float t = rbf(rbf(gt[j][e]) + bv[e]);
        gt[j][e] = rbf(t * __builtin_amdgcn_rcpf(1.f + __expf(-t)));
      }
      if (j >= 3) *(LAS f32x4*)(smem + GX_OFF + (w * 2 + j - 3) * 1024 + lane * 16) = gt[j];
    }
    __syncthreads();
    const unsigned orow = orow_f();
#pragma unroll
    for (int j = 0; j < 3; ++j) {
      f32x4 sv = *(LAS f32x4*)(smem + REDS1_OFF + ((w * 4) * 3 + j) * 1024 + lane * 16);
#pragma unroll
      for (int k = 1; k < 4; ++k) sv += *(LAS f32x4*)(smem + REDS1_OFF + ((w * 4 + k) * 3 + j) * 1024 + lane * 16);
      *(u32x2*)(out + orow + j * 32) = fin4(xv[j], sv, gt[j]);
    }
    __builtin_amdgcn_endpgm();
  }
}

extern "C" void kernel_launch(void* const* d_in, const int* in_sizes, int n_in, void* d_out, int out_size, void* d_ws,
                              size_t ws_size, hipStream_t stream) {
  (void)hipFuncSetAttribute((const void*)k_all, hipFuncAttributeMaxDynamicSharedMemorySize, LDS_ALL);
  k_all<<<256, 512, LDS_ALL, stream>>>((const char*)d_in[0], (const char*)d_in[1], (const char*)d_in[4], (const char*)d_in[5],
                                       (const char*)d_in[6], (const char*)d_in[2], (const char*)d_in[7], (char*)d_ws, (char*)d_out);
}
```
